```python
import torch

def _softmax(x, axis=-1):
    return torch.softmax(x, dim=axis)
def _gelu(x):
    return torch.nn.functional.gelu(x, approximate="tanh")
def _silu(x):
    return torch.nn.functional.silu(x)
def _sigmoid(x):
    return torch.sigmoid(x)
def _layernorm(x, axis=-1, eps=1e-5):
    m = x.mean(dim=axis, keepdim=True)
    v = x.var(dim=axis, keepdim=True, unbiased=False)
    return (x - m) / torch.sqrt(v + eps)
def _scatter_add(tgt, idx, src, axis=0):
    return tgt.clone().index_add_(axis, idx, src)
def _segment_sum(src, seg, n):
    out = torch.zeros((n,) + src.shape[1:], dtype=src.dtype)
    return out.index_add_(0, seg, src)
def _pad(x, pw):
    flat = tuple(v for pair in reversed(pw) for v in pair)
    return torch.nn.functional.pad(x, flat)

kernel_name = 'parallel_sum__spectral_gfnet__dense_activation_gelu__prompt'

def setup_inputs():

    _w = (lambda a: (lambda t: t.to(torch.bfloat16) if t.is_floating_point() else t)(torch.as_tensor(a)))
    def _A_setup():
        B, S, D = 1, 2048, 576
        Df = D // 2 + 1
        return {
            "x": _w((lambda *s: torch.randn(*s, dtype=torch.bfloat16) * 0.05)(B, S, D)),
            "w_r": _w((lambda *s: torch.randn(*s, dtype=torch.bfloat16) * 0.8)(S, Df) * 0.02),
            "w_i": _w((lambda *s: torch.randn(*s, dtype=torch.bfloat16) * 0.8)(S, Df) * 0.02),
        }
    _A_in = _A_setup()

    def _B_setup():
        B, S, D, F = 1, 2048, 576, 4608
        return {
            "x": _w((lambda *s: torch.randn(*s, dtype=torch.bfloat16) * 0.05)(B, S, D)),
            "w1": _w((lambda *s: torch.randn(*s, dtype=torch.bfloat16) * 0.4)(D, F) * (D ** -0.5)),
            "b1": _w((lambda *s: torch.randn(*s, dtype=torch.bfloat16) * 12.8)(F) * 0.01),
            "w2": _w((lambda *s: torch.randn(*s, dtype=torch.bfloat16) * 0.2)(F, D) * (F ** -0.5)),
            "b2": _w((lambda *s: torch.randn(*s, dtype=torch.bfloat16) * 1.6)(D) * 0.01),
        }
    _B_in = _B_setup()

    return {
        "A_x": _A_in["x"],
        "A_w_r": _A_in["w_r"],
        "A_w_i": _A_in["w_i"],
        "B_x": _B_in["x"],
        "B_w1": _B_in["w1"],
        "B_b1": _B_in["b1"],
        "B_w2": _B_in["w2"],
        "B_b2": _B_in["b2"],
    }

def reference(A_x, A_w_r, A_w_i, B_x, B_w1, B_b1, B_w2, B_b2):
    def _A_ref(x, w_r, w_i):
        B, S, D = x.shape
        X = (lambda x, axes=(-2,-1): torch.fft.rfft2(x.float(), dim=axes))(x, axes=(-2, -1))
        W = w_r + 1j * w_i
        X = X * W
        y = (lambda x, s, axes=(-2,-1): torch.fft.irfft2(x, s=s, dim=axes).to(torch.bfloat16))(X, (S, D), axes=(-2, -1))
        return y

    def _B_ref(x, w1, b1, w2, b2):
        h = torch.einsum("bsd,df->bsf", x, w1) + b1
        h = _gelu(h)
        return torch.einsum("bsf,fd->bsd", h, w2) + b2

    x = A_x
    xn = _layernorm(x, axis=-1)
    return x + _A_ref(xn, A_w_r, A_w_i) + _B_ref(xn, B_w1, B_b1, B_w2, B_b2)
```

```cpp
#include <hip/hip_runtime.h>
#include <hip/hip_bf16.h>
#include <math.h>
typedef __hip_bfloat16 bf16;
constexpr int S=2048, D=576, F=4608, DF=289;
__device__ inline float bfr(float x){ return __bfloat162float(__float2bfloat16(x)); }
__global__ void k_ln(const bf16* x, float* xn){
  int r=blockIdx.x; __shared__ float red[256];
  float s=0; for(int i=threadIdx.x;i<D;i+=256) s+=__bfloat162float(x[r*D+i]);
  red[threadIdx.x]=s; __syncthreads();
  for(int o=128;o>0;o>>=1){ if(threadIdx.x<o) red[threadIdx.x]+=red[threadIdx.x+o]; __syncthreads(); }
  float mean=red[0]/D; __syncthreads();
  float v=0; for(int i=threadIdx.x;i<D;i+=256){ float d=__bfloat162float(x[r*D+i])-mean; v+=d*d; }
  red[threadIdx.x]=v; __syncthreads();
  for(int o=128;o>0;o>>=1){ if(threadIdx.x<o) red[threadIdx.x]+=red[threadIdx.x+o]; __syncthreads(); }
  float var=bfr(red[0]/D); float m=bfr(mean);
  float sd=bfr(sqrtf(bfr(var+1e-5f)));
  for(int i=threadIdx.x;i<D;i+=256){ float d=bfr(__bfloat162float(x[r*D+i])-m); xn[r*D+i]=bfr(d/sd); }
}
__global__ void k_rowdft(const float* xn, float2* U){
  int r=blockIdx.x; __shared__ float row[D]; __shared__ float2 tw[D];
  for(int i=threadIdx.x;i<D;i+=blockDim.x){ row[i]=xn[r*D+i]; float s,c; sincospif(2.0f*i/D,&s,&c); tw[i]=make_float2(c,-s);} __syncthreads();
  for(int k=threadIdx.x;k<DF;k+=blockDim.x){ float a=0,b=0; int idx=0; for(int d=0;d<D;d++){ a+=row[d]*tw[idx].x; b+=row[d]*tw[idx].y; idx+=k; if(idx>=D) idx-=D; } U[r*DF+k]=make_float2(a,b);}
}
__global__ void k_coldft(const float2* in, float2* out, float sgn, const bf16* wr, const bf16* wi, int usew){
  int k=blockIdx.x; __shared__ float2 col[S]; __shared__ float2 tw[S];
  for(int i=threadIdx.x;i<S;i+=blockDim.x){ col[i]=in[i*DF+k]; float s,c; sincospif(2.0f*i/S,&s,&c); tw[i]=make_float2(c,sgn*s);} __syncthreads();
  for(int t=threadIdx.x;t<S;t+=blockDim.x){ float a=0,b=0; for(int s=0;s<S;s++){ float2 w=tw[(s*t)&(S-1)]; float2 v=col[s]; a+=v.x*w.x-v.y*w.y; b+=v.x*w.y+v.y*w.x; }
    if(usew){ float p=__bfloat162float(wr[t*DF+k]), q=__bfloat162float(wi[t*DF+k]); float a2=a*p-b*q, b2=a*q+b*p; a=a2; b=b2; }
    out[t*DF+k]=make_float2(a,b);}
}
__global__ void k_gemm1(const float* xn, const bf16* w1, const bf16* b1, float* h){
  int r0=blockIdx.y*8; int f=blockIdx.x*256+threadIdx.x; __shared__ float xs[8][D];
  for(int i=threadIdx.x;i<8*D;i+=256) xs[i/D][i%D]=xn[r0*D+i]; __syncthreads();
  float acc[8]={0}; for(int d=0;d<D;d++){ float w=__bfloat162float(w1[d*F+f]); for(int j=0;j<8;j++) acc[j]+=xs[j][d]*w; }
  for(int j=0;j<8;j++){ float v=bfr(bfr(acc[j])+__bfloat162float(b1[f])); float g=0.5f*v*(1.f+tanhf(0.7978845608f*(v+0.044715f*v*v*v))); h[(size_t)(r0+j)*F+f]=bfr(g);}
}
__global__ void k_final(const bf16* x, const float2* Z, const float* h, const bf16* w2, const bf16* b2, bf16* out){
  int r=blockIdx.x; __shared__ float hs[F]; __shared__ float2 z[DF]; __shared__ float2 tw[D];
  for(int i=threadIdx.x;i<F;i+=blockDim.x) hs[i]=h[(size_t)r*F+i];
  for(int i=threadIdx.x;i<DF;i+=blockDim.x) z[i]=Z[r*DF+i];
  for(int i=threadIdx.x;i<D;i+=blockDim.x){ float s,c; sincospif(2.0f*i/D,&s,&c); tw[i]=make_float2(c,s);} __syncthreads();
  int d=threadIdx.x;
  float acc=0; for(int f=0;f<F;f++) acc+=hs[f]*__bfloat162float(w2[f*D+d]);
  float B=bfr(bfr(acc)+__bfloat162float(b2[d]));
  float y=z[0].x + ((d&1)?-z[288].x:z[288].x); int idx=d;
  for(int k=1;k<288;k++){ y+=2.f*(z[k].x*tw[idx].x - z[k].y*tw[idx].y); idx+=d; if(idx>=D) idx-=D; }
  y=bfr(y/(float)(S*D));
  out[r*D+d]=__float2bfloat16(bfr(__bfloat162float(x[r*D+d])+y)+B);
}
extern "C" void kernel_launch(void* const* d_in, const int* in_sizes, int n_in, void* d_out, int out_size, void* d_ws, size_t ws_size, hipStream_t stream){
  const bf16* x=(const bf16*)d_in[0]; const bf16* wr=(const bf16*)d_in[1]; const bf16* wi=(const bf16*)d_in[2];
  const bf16* w1=(const bf16*)d_in[4]; const bf16* b1=(const bf16*)d_in[5]; const bf16* w2=(const bf16*)d_in[6]; const bf16* b2=(const bf16*)d_in[7];
  char* ws=(char*)d_ws; float* xn=(float*)ws; float2* U=(float2*)(ws+(8<<20)); float2* V=(float2*)(ws+(16<<20)); float2* Z=(float2*)(ws+(24<<20)); float* h=(float*)(ws+(32<<20));
  k_ln<<<S,256,0,stream>>>(x,xn);
  k_rowdft<<<S,320,0,stream>>>(xn,U);
  k_coldft<<<DF,1024,0,stream>>>(U,V,-1.f,wr,wi,1);
  k_coldft<<<DF,1024,0,stream>>>(V,Z,1.f,wr,wi,0);
  k_gemm1<<<dim3(F/256,S/8),256,0,stream>>>(xn,w1,b1,h);
  k_final<<<S,D,0,stream>>>(x,Z,h,w2,b2,(bf16*)d_out);
}
```

```cpp
#include <hip/hip_runtime.h>
#include <stdint.h>
#include <type_traits>

typedef unsigned short u16;
typedef short s16x4 __attribute__((ext_vector_type(4)));
typedef short s16x8 __attribute__((ext_vector_type(8)));
typedef __bf16 bf16x8 __attribute__((ext_vector_type(8)));
typedef __bf16 bf16x2 __attribute__((ext_vector_type(2)));
typedef _Float16 f16x8 __attribute__((ext_vector_type(8)));
typedef _Float16 f16x2 __attribute__((ext_vector_type(2)));
typedef float f32x16 __attribute__((ext_vector_type(16)));
typedef float f32x4 __attribute__((ext_vector_type(4)));
typedef float f32x2 __attribute__((ext_vector_type(2)));
typedef unsigned u32x4 __attribute__((ext_vector_type(4)));
typedef unsigned u32x2 __attribute__((ext_vector_type(2)));
typedef unsigned long long u64;
#define LAS __attribute__((address_space(3)))
#define GAS __attribute__((address_space(1)))
#define DI __device__ __forceinline__

namespace tab {
constexpr double PI = 3.14159265358979323846;
constexpr double ccos(int a, int n) {
  a %= n; if (a < 0) a += n;
  if (2 * a > n) a = n - a;
  bool neg = false;
  if (4 * a > n) { a = n / 2 - a; neg = true; }
  if (4 * a == n) return 0.0;
  double x = 2 * PI * a / n, t = 1, s = 1;
  for (int i = 1; i <= 13; i++) { t *= -x * x / ((2 * i - 1) * (2 * i)); s += t; }
  return neg ? -s : s;
}
constexpr u16 to_bf(double v) { float f = (float)v; uint32_t u = __builtin_bit_cast(uint32_t, f); u += 0x7fffu + ((u >> 16) & 1u); return (u16)(u >> 16); }
constexpr u16 to_h(double v) { _Float16 h = (_Float16)v; return __builtin_bit_cast(u16, h); }
struct C576 { u16 h[576]; u16 b1[576]; u16 b2[576]; };
constexpr C576 gen576() { C576 r{}; for (int i = 0; i < 576; i++) { double c = ccos(i, 576) / 24.0; r.h[i] = to_h(c); r.b1[i] = to_bf(c); r.b2[i] = to_bf(2 * c); } return r; }
constexpr C576 c576 = gen576();
constexpr int colk(int c) { return c == 0 ? 0 : c == 1 ? 288 : c < 288 ? (c & ~1) : ((c - 288) & ~1) + 1; }
constexpr int colp(int c) { return c < 2 ? 0 : (c & 1); }
struct Chunk { u16 v[9216]; };
constexpr Chunk genTF(int u) {
  Chunk r{};
  for (int i = 0; i < 1152; i++) { int s = i >> 6, l = i & 63, c = 32 * u + (l & 31), k = colk(c), o = 144 * colp(c), d = 16 * s + 8 * (l >> 5);
    for (int j = 0; j < 8; j++) r.v[i * 8 + j] = c576.h[((d + j) * k + o) % 576]; }
  return r;
}
constexpr Chunk genTI(int q) {
  Chunk r{};
  for (int i = 0; i < 16; i++) { int c = 16 * q + i, k = colk(c), o = 144 * colp(c);
    if (c < 2) { for (int d = 0; d < 576; d++) r.v[i * 576 + d] = c576.b1[(d * k + o) % 576]; }
    else { for (int d = 0; d < 576; d++) r.v[i * 576 + d] = c576.b2[(d * k + o) % 576]; } }
  return r;
}
template <int I> constexpr Chunk cTF = genTF(I);
template <int I> constexpr Chunk cTI = genTI(I);
struct Small { u16 f64[4][2][2][512]; u16 f32[2][2][512]; u16 g32[2][2][512]; u16 g64[4][2][2][512]; };
constexpr Small genSmall() {
  Small r{};
  double c64[64] = {}; for (int i = 0; i < 64; i++) c64[i] = ccos(i, 64);
  const double r32 = 0.17677669529663688110;
  for (int l = 0; l < 64; l++) for (int j = 0; j < 8; j++) {
    int rr = l & 15, gq = l >> 4;
    int pl = 16 * (gq >> 1) + 8 * (j >> 2) + 4 * (gq & 1) + (j & 3);
    int p2 = 16 * (j >> 2) + 4 * gq + (j & 3);
    for (int w = 0; w < 4; w++) for (int a = 0; a < 2; a++) {
      int e = (32 * a + pl) * (16 * w + rr);
      r.f64[w][0][a][l * 8 + j] = to_h(c64[e & 63] / 8); r.f64[w][1][a][l * 8 + j] = to_h(c64[(e + 16) & 63] / 8);
      int e2 = (16 * w + rr) * (32 * a + 8 * gq + j);
      r.g64[w][0][a][l * 8 + j] = to_bf(c64[e2 & 63] / 8); r.g64[w][1][a][l * 8 + j] = to_bf(c64[(e2 + 48) & 63] / 8);
    }
    for (int t = 0; t < 2; t++) {
      int e = 2 * ((16 * t + rr) * p2);
      r.f32[0][t][l * 8 + j] = to_h(c64[e & 63] * r32); r.f32[1][t][l * 8 + j] = to_h(c64[(e + 16) & 63] * r32);
      r.g32[0][t][l * 8 + j] = to_bf(c64[e & 63] * r32); r.g32[1][t][l * 8 + j] = to_bf(c64[(e + 48) & 63] * r32);
    }
  }
  return r;
}
struct alignas(256) All { Chunk tf[18]; Chunk ti[36]; Small sm; };
template <int... I, int... J> constexpr All mk(std::integer_sequence<int, I...>, std::integer_sequence<int, J...>) { return All{{cTF<I>...}, {cTI<J>...}, genSmall()}; }
}
static __device__ const tab::All g_tab = tab::mk(std::make_integer_sequence<int, 18>{}, std::make_integer_sequence<int, 36>{});

constexpr uint32_t BUF = 36864, L_XS = 73728, XS_STRIDE = 656, L_B1 = 157696, L_Z = 9216, L_CX = BUF, LDS_BYTES = 163840;
constexpr uint32_t WS_EPOCH = 0, WS_FU = 4096, WS_FZ = 8192, WS_FH = 16384, WS_FP = 49152;
constexpr uint32_t WS_H = 1u << 20, WS_UT = 20u << 20, WS_WT = 23u << 20, WS_ZT = 26u << 20, WS_PART = 29u << 20, WS_END = 48u << 20;
constexpr uint32_t TAB_TF = 0, TAB_TI = 18 * 18432, TAB_F64 = 54 * 18432, TAB_F32 = TAB_F64 + 16384, TAB_G32 = TAB_F32 + 4096, TAB_G64 = TAB_G32 + 4096;

template <int I, int N, class Fn> DI void sfor(Fn&& f) { if constexpr (I < N) { f(std::integral_constant<int, I>{}); sfor<I + 1, N>(f); } }

template <int IMM> DI void gld16(u32x4& d, uint32_t voff, const char* sb) { asm volatile("global_load_dwordx4 %0, %1, %2 offset:%3" : "=v"(d) : "v"(voff), "s"(sb), "i"(IMM) : "memory"); }
template <int IMM> DI void gld16c(u32x4& d, uint32_t voff, const char* sb) { asm volatile("global_load_dwordx4 %0, %1, %2 offset:%3 sc1" : "=v"(d) : "v"(voff), "s"(sb), "i"(IMM) : "memory"); }
DI void gldu16(unsigned& d, uint32_t voff, const char* sb) { asm volatile("global_load_ushort %0, %1, %2" : "=v"(d) : "v"(voff), "s"(sb) : "memory"); }
DI void dma16(uint32_t voff, const char* sb, uint32_t lds) { asm volatile("s_mov_b32 m0, %2\n\ts_nop 0\n\tglobal_load_lds_dwordx4 %0, %1" :: "v"(voff), "s"(sb), "s"(lds) : "memory"); }
DI void dma16h(uint32_t voff, const char* sb, uint32_t lds) {
  asm volatile("s_mov_b32 m0, %2\n\ts_mov_b32 exec_hi, 0\n\tglobal_load_lds_dwordx4 %0, %1\n\ts_mov_b32 exec_hi, -1" :: "v"(voff), "s"(sb), "s"(lds) : "memory"); }
template <int IMM> DI void st16c(u32x4 d, u32x4 srd, uint32_t voff, uint32_t soff) {
  asm volatile("buffer_store_dwordx4 %0, %1, %2, %3 offen offset:%4 sc1\n\ts_nop 1" :: "v"(d), "v"(voff), "s"(srd), "s"(soff), "i"(IMM) : "memory"); }
template <int N> DI void waitv() { asm volatile("s_waitcnt vmcnt(%0)" :: "i"(N) : "memory"); }
template <int N> DI void waitv_bar() { asm volatile("s_waitcnt vmcnt(%0) lgkmcnt(0)\n\ts_barrier" :: "i"(N) : "memory"); }
DI void bar() { asm volatile("s_waitcnt lgkmcnt(0)\n\ts_barrier" ::: "memory"); }
DI void pin(u32x4& v) { asm volatile("" : "+v"(v)); }
DI void pin(unsigned& v) { asm volatile("" : "+v"(v)); }

DI float asf(unsigned u) { return __builtin_bit_cast(float, u); }
DI unsigned asu(float f) { return __builtin_bit_cast(unsigned, f); }
DI unsigned pk_bf(float a, float b) { f32x2 p = {a, b}; bf16x2 r = __builtin_convertvector(p, bf16x2); return __builtin_bit_cast(unsigned, r); }
DI unsigned pk_h(float a, float b) { f32x2 p = {a, b}; f16x2 r = __builtin_convertvector(p, f16x2); return __builtin_bit_cast(unsigned, r); }
DI float bfr(float a) { return asf(pk_bf(a, a) << 16); }
DI float lo_f(unsigned u) { return asf(u << 16); }
DI float hi_f(unsigned u) { return asf(u & 0xffff0000u); }
DI float dot2(unsigned a, unsigned b, float c) { return __builtin_amdgcn_fdot2_f32_bf16(__builtin_bit_cast(bf16x2, a), __builtin_bit_cast(bf16x2, b), c, false); }
template <int CTRL> DI float dpp_add(float v) { return v + asf(__builtin_amdgcn_update_dpp(0, asu(v), CTRL, 0xf, 0xf, true)); }
DI f32x16 mm32b(u32x4 a, u32x4 b, f32x16 c) { return __builtin_amdgcn_mfma_f32_32x32x16_bf16(__builtin_bit_cast(bf16x8, a), __builtin_bit_cast(bf16x8, b), c, 0, 0, 0); }
DI f32x16 mm32h(u32x4 a, u32x4 b, f32x16 c) { return __builtin_amdgcn_mfma_f32_32x32x16_f16(__builtin_bit_cast(f16x8, a), __builtin_bit_cast(f16x8, b), c, 0, 0, 0); }
DI f32x4 mm16b(u32x4 a, u32x4 b, f32x4 c) { return __builtin_amdgcn_mfma_f32_16x16x32_bf16(__builtin_bit_cast(bf16x8, a), __builtin_bit_cast(bf16x8, b), c, 0, 0, 0); }
DI f32x4 mm16h(u32x4 a, u32x4 b, f32x4 c) { return __builtin_amdgcn_mfma_f32_16x16x32_f16(__builtin_bit_cast(f16x8, a), __builtin_bit_cast(f16x8, b), c, 0, 0, 0); }
DI u32x4 tr2(LAS char* p, int second) {
  s16x4 a = __builtin_amdgcn_ds_read_tr16_b64_v4i16((LAS s16x4*)p);
  s16x4 b = __builtin_amdgcn_ds_read_tr16_b64_v4i16((LAS s16x4*)(p + second));
  s16x8 c = __builtin_shufflevector(a, b, 0, 1, 2, 3, 4, 5, 6, 7);
  return __builtin_bit_cast(u32x4, c);
}
DI float gelu(float v) {
  float p = __builtin_fmaf(v * v, -0.10294324f, -2.3022082f);
  float e = __builtin_amdgcn_exp2f(v * p);
  return v * __builtin_amdgcn_rcpf(1.0f + e);
}
#define RLX_AGENT __ATOMIC_RELAXED, __HIP_MEMORY_SCOPE_AGENT
template <int N> DI void poll(const char* base, uint32_t off, uint32_t stride, u64 tag, bool active) {
  for (unsigned spins = 0; spins < (1u << 17); ++spins) {
    bool ok = true;
#pragma unroll
    for (int k = 0; k < N; ++k) { u64 v = __hip_atomic_load((GAS u64*)(base + off + k * stride), RLX_AGENT); ok &= (v == tag) | !active; }
    if (__all(ok)) break;
    __builtin_amdgcn_s_sleep(1);
  }
  __builtin_amdgcn_fence(__ATOMIC_ACQUIRE, "wavefront");
}
DI void setflag(char* ws, uint32_t off, u64 tag) { __hip_atomic_store((GAS u64*)(ws + off), tag, RLX_AGENT); }

__global__ __launch_bounds__(512, 2) void fused_kernel(const char* __restrict__ x, const char* __restrict__ wr, const char* __restrict__ wi,
    const char* __restrict__ w1, const char* __restrict__ b1, const char* __restrict__ w2, const char* __restrict__ b2, char* out, char* ws) {
  extern __shared__ __attribute__((aligned(16))) char smem_[];
  LAS char* smem = (LAS char*)smem_;
  const int tid = threadIdx.x, l = tid & 63;
  const int w = __builtin_amdgcn_readfirstlane(tid >> 6), g = w & 3, hf = w >> 2;
  const int b = blockIdx.x, xcd = b & 7, jj = b >> 3;
  const int ks = xcd >> 1, mt = (xcd & 1) * 8 + (jj >> 2), nt = jj & 3, fj = ks * 4 + nt;
  const char* tb = (const char*)&g_tab;
  const u32x4 rs = {(unsigned)(size_t)ws, (unsigned)((size_t)ws >> 32) & 0xffffu, WS_END, 0x00020000u};
  const int i16 = l & 15, q4 = i16 >> 2, p4 = i16 & 3, hh = l >> 5, gq = l >> 4;

  u32x4 xr[2][9];
  { const uint32_t vo = (l >> 3) * 36864 + (l & 7) * 16;
    sfor<0, 2>([&](auto RB) { const char* sb = x + (size_t)((32 * (g & 1) + 16 * hf + 8 * RB) * 32 + 2 * mt + (g >> 1)) * 1152;
      sfor<0, 9>([&](auto C) { gld16<C * 128>(xr[RB][C], vo, sb); }); }); }
  const int odd = fj & 1;
  const int lone = odd ? 0 : 8;
  const int uA = fj < 8 ? fj : fj + 1;
  const bool hasB = (fj & 7) == 0;
  const int uB = fj < 8 ? 8 : 17;
  { const uint32_t vo = (l >> 2) * 9216 + (l & 3) * 16;
    const char* sb = w1 + (size_t)(288 * hf) * 9216 + (288 * fj + 32 * lone) * 2;
    sfor<0, 4>([&](auto I) { dma16(vo, sb + (size_t)(64 * g + 16 * I) * 9216, hf * BUF + (64 * g + 16 * I) * 64); });
    dma16h(vo, sb + (size_t)(256 + 8 * g) * 9216, hf * BUF + (256 + 8 * g) * 64);
    const uint32_t v2 = l * 16;
    sfor<0, 3>([&](auto I) { int pc = g + 4 * I; pc = pc > 8 ? 8 : pc; dma16(v2, tb + TAB_TF + uA * 18432 + (9 * hf + pc) * 1024, hf * BUF + 18432 + pc * 1024); });
    if (hasB) sfor<0, 3>([&](auto I) { int pc = g + 4 * I; pc = pc > 8 ? 8 : pc; dma16(v2, tb + TAB_TF + uB * 18432 + (9 * hf + pc) * 1024, hf * BUF + 27648 + pc * 1024); });
  }
  unsigned wv[16], b1v; u32x2 ep;
  { const int kc = tid < 288 ? tid : 288; const uint32_t vo = kc * 2;
    const int k1 = b & 63, gw = b >> 6;
    sfor<0, 8>([&](auto J) { const size_t ro = (size_t)(k1 + 64 * (16 * (J >> 2) + 4 * gw + (J & 3))) * 578;
      gldu16(wv[J], vo, wr + ro); gldu16(wv[8 + J], vo, wi + ro); });
    const uint32_t vb = (tid < 287 ? tid : 287) * 2;
    gldu16(b1v, vb, b1 + 576 * fj);
    const uint32_t z = 0;
    asm volatile("global_load_dwordx2 %0, %1, %2 sc1" : "=v"(ep) : "v"(z), "s"(ws + WS_EPOCH) : "memory");
  }
  sfor<0, 2>([&](auto RB) {
    waitv<(RB == 0 ? 35 : 26)>();
    sfor<0, 9>([&](auto C) { pin(xr[RB][C]); });
    float s0 = 0, s1 = 0, q0 = 0, q1 = 0;
    sfor<0, 9>([&](auto C) { u32x4 v = xr[RB][C];
      s0 = dot2(v.x, 0x3f803f80u, s0); q0 = dot2(v.x, v.x, q0); s1 = dot2(v.y, 0x3f803f80u, s1); q1 = dot2(v.y, v.y, q1);
      s0 = dot2(v.z, 0x3f803f80u, s0); q0 = dot2(v.z, v.z, q0); s1 = dot2(v.w, 0x3f803f80u, s1); q1 = dot2(v.w, v.w, q1); });
    float s = s0 + s1, q = q0 + q1;
    s = dpp_add<0xB1>(s); q = dpp_add<0xB1>(q); s = dpp_add<0x4E>(s); q = dpp_add<0x4E>(q); s = dpp_add<0x141>(s); q = dpp_add<0x141>(q);
    const float mean = s * (1.0f / 576.0f);
    const float var = fmaxf(q * (1.0f / 576.0f) - mean * mean, 0.0f);
    const float sd = bfr(__builtin_sqrtf(bfr(bfr(var) + 1e-5f)));
    const float r = 1.0f / sd, mb = -bfr(mean) * r;
    sfor<0, 9>([&](auto C) { u32x4 v = xr[RB][C], o;
      o.x = pk_bf(__builtin_fmaf(lo_f(v.x), r, mb), __builtin_fmaf(hi_f(v.x), r, mb)); o.y = pk_bf(__builtin_fmaf(lo_f(v.y), r, mb), __builtin_fmaf(hi_f(v.y), r, mb));
      o.z = pk_bf(__builtin_fmaf(lo_f(v.z), r, mb), __builtin_fmaf(hi_f(v.z), r, mb)); o.w = pk_bf(__builtin_fmaf(lo_f(v.w), r, mb), __builtin_fmaf(hi_f(v.w), r, mb));
      xr[RB][C] = o;
      if constexpr (C < 5) *(LAS u32x4*)(smem + L_XS + (32 * g + 16 * hf + 8 * RB + (l >> 3)) * XS_STRIDE + C * 128 + (l & 7) * 16) = o; });
  });
  u32x4 xn[36];
  { LAS char* rd = smem + L_XS + (32 * g + (l & 31)) * XS_STRIDE + hh * 16;
    bar();
    sfor<0, 20>([&](auto S) { xn[S] = *(LAS u32x4*)(rd + S * 32); });
    bar();
    sfor<0, 2>([&](auto RB) { sfor<5, 9>([&](auto C) { *(LAS u32x4*)(smem + L_XS + (32 * g + 16 * hf + 8 * RB + (l >> 3)) * XS_STRIDE + (C - 5) * 128 + (l & 7) * 16) = xr[RB][C]; }); });
    bar();
    sfor<20, 36>([&](auto S) { xn[S] = *(LAS u32x4*)(rd + (S - 20) * 32); });
  }
  waitv<0>();
  sfor<0, 16>([&](auto J) { pin(wv[J]); }); pin(b1v); asm volatile("" : "+v"(ep));
  u64 tag;
  { u64 e = ((u64)(unsigned)__builtin_amdgcn_readfirstlane(ep.y) << 32) | (unsigned)__builtin_amdgcn_readfirstlane(ep.x);
    e += 0x9E3779B97F4A7C15ull; e = (e ^ (e >> 30)) * 0xBF58476D1CE4E5B9ull; e = (e ^ (e >> 27)) * 0x94D049BB133111EBull; tag = e ^ (e >> 31); }
  if (tid < 288) *(LAS float*)(smem + L_B1 + tid * 4) = lo_f(b1v);
  if (tid < 289) {
    const int T = tid == 288 ? 288 : (tid & 1) ? 144 + (tid >> 1) : (tid >> 1);
    const uint32_t o = WS_WT + T * 8192 + (b & 63) * 128 + (b >> 6) * 32;
    u32x4 a = {wv[0] | (wv[1] << 16), wv[2] | (wv[3] << 16), wv[4] | (wv[5] << 16), wv[6] | (wv[7] << 16)};
    u32x4 c = {wv[8] | (wv[9] << 16), wv[10] | (wv[11] << 16), wv[12] | (wv[13] << 16), wv[14] | (wv[15] << 16)};
    st16c<0>(a, rs, o, 0); st16c<16>(c, rs, o, 0);
  }
  bar();

  const uint32_t vo1 = (l >> 3) * 9216 + (((l & 7) ^ (((l >> 4) & 1) << 2)) * 16);
  const int rot = mt & 3;
  auto pair_unit = [&](int r) { return odd + 2 * ((r - 1 + rot) & 3); };
  auto issue_w1 = [&](int st) {
    const char* sb = w1 + (size_t)(288 * (st & 1)) * 9216 + (288 * fj + 32 * pair_unit(st >> 1)) * 2;
    const uint32_t lb = (st & 3) * BUF;
    sfor<0, 4>([&](auto I) { dma16(vo1, sb + (size_t)(32 * w + 8 * I) * 9216, lb + (32 * w + 8 * I) * 128); });
    dma16h(vo1, sb + (size_t)(256 + 4 * w) * 9216, lb + (256 + 4 * w) * 128);
  };
  uint32_t vo2a, vo2b;
  { int c = w * 64 + l; vo2a = (c / 18) * 1152 + (c % 18) * 16; c = 512 + (w & 1) * 32 + (l & 31); vo2b = (c / 18) * 1152 + (c % 18) * 16; }
  auto fblk = [&](int i) { int v = 9 * nt + i; return 36 * ks + (v >= 36 ? v - 36 : v); };
  const int c0 = ks < 2 ? 160 * ks : 320 + 128 * (ks - 2);
  auto issue_w2 = [&](int j) {
    const uint32_t lb = ((j + 2) & 3) * BUF;
    auto base = [&](int i) { return j < 9 ? w2 + (size_t)fblk(4 * j + i) * 36864 + 288 * nt : tb + TAB_TI + (size_t)(c0 + 32 * i) * 1152 + 288 * nt; };
    sfor<0, 4>([&](auto I) { dma16(vo2a, base(I), lb + I * 9216 + w * 1024); });
    dma16h(vo2b, base(w >> 1), lb + (w >> 1) * 9216 + 8192 + (w & 1) * 512);
  };
  issue_w1(2); issue_w1(3);

  const uint32_t hvo = ((l >> 4) & 1) * 1024 + hh * 256 + i16 * 16;
  f32x16 acc[2];
  auto bias_init = [&](int unit) { f32x16 a; LAS char* p = smem + L_B1 + (32 * unit + 4 * hh) * 4;
    sfor<0, 4>([&](auto A) { f32x4 v = *(LAS f32x4*)(p + A * 32); a[4 * A] = v.x; a[4 * A + 1] = v.y; a[4 * A + 2] = v.z; a[4 * A + 3] = v.w; }); return a; };
  auto gelu_store = [&](const f32x16& a, auto P, int unit, u32x4 r) {
    u32x4 o; o.x = pk_bf(gelu(a[8 * P]), gelu(a[8 * P + 1])); o.y = pk_bf(gelu(a[8 * P + 2]), gelu(a[8 * P + 3]));
    o.z = pk_bf(gelu(a[8 * P + 4]), gelu(a[8 * P + 5])); o.w = pk_bf(gelu(a[8 * P + 6]), gelu(a[8 * P + 7]));
    st16c<P * 512>(o, r, hvo, WS_H + ((mt * 144 + 9 * fj + unit) * 4 + g) * 2048); };
  { f32x16 ua = {}, ub = {};
    if (hf == 0) acc[0] = bias_init(lone);
    const float sg = fj < 8 ? 1.0f : -1.0f;
    sfor<0, 2>([&](auto ST) {
      if constexpr (ST == 1) { bar(); issue_w1(4); }
      if (hf == 0) {
        LAS char* p = smem + ST * BUF + (8 * hh + q4) * 64 + (gq & 1) * 32 + p4 * 8;
        sfor<0, 18>([&](auto S) { acc[0] = mm32b(tr2(p + S * 1024, 256), xn[18 * ST + S], acc[0]); });
      } else {
        LAS char* p = smem + ST * BUF + 18432 + l * 16;
        sfor<0, 9>([&](auto S) { const u32x4 a = xn[9 * ST + S], c = xn[18 + 9 * ST + S]; u32x4 f;
          f.x = pk_h(__builtin_fmaf(lo_f(c.x), sg, lo_f(a.x)), __builtin_fmaf(hi_f(c.x), sg, hi_f(a.x))); f.y = pk_h(__builtin_fmaf(lo_f(c.y), sg, lo_f(a.y)), __builtin_fmaf(hi_f(c.y), sg, hi_f(a.y)));
          f.z = pk_h(__builtin_fmaf(lo_f(c.z), sg, lo_f(a.z)), __builtin_fmaf(hi_f(c.z), sg, hi_f(a.z))); f.w = pk_h(__builtin_fmaf(lo_f(c.w), sg, lo_f(a.w)), __builtin_fmaf(hi_f(c.w), sg, hi_f(a.w)));
          ua = mm32h(f, *(LAS u32x4*)(p + S * 1024), ua);
          if (hasB) ub = mm32h(f, *(LAS u32x4*)(p + 9216 + S * 1024), ub); });
      }
    });
    if (hf == 1) {
      const uint32_t vo = (l & 31) * 4096 + hh * 16, so = WS_UT + (2 * mt + (g >> 1)) * 128 + (g & 1) * 64;
      sfor<0, 2>([&](auto P) { u32x4 o = {pk_h(ua[8 * P], ua[8 * P + 1]), pk_h(ua[8 * P + 2], ua[8 * P + 3]), pk_h(ua[8 * P + 4], ua[8 * P + 5]), pk_h(ua[8 * P + 6], ua[8 * P + 7])};
        st16c<P * 32>(o, rs, vo, so + uA * 131072); });
      if (hasB) sfor<0, 2>([&](auto P) { u32x4 o = {pk_h(ub[8 * P], ub[8 * P + 1]), pk_h(ub[8 * P + 2], ub[8 * P + 3]), pk_h(ub[8 * P + 4], ub[8 * P + 5]), pk_h(ub[8 * P + 6], ub[8 * P + 7])};
        st16c<P * 32>(o, rs, vo, so + uB * 131072); });
      acc[0] = ua;
    }
  }
  const u32x4 rs0 = {rs.x, rs.y, hf == 0 ? WS_END : 0u, 0x00020000u};
  const uint32_t a0 = (8 * hh + q4) * 128 + ((hf ^ (q4 >> 1)) * 64) + (gq & 1) * 32 + p4 * 8;
  sfor<2, 10>([&](auto ST) {
    constexpr int R = ST >> 1;
    if constexpr (ST == 9) { if (w == 0) poll<4>(ws, WS_FU + l * 8, 512, tag, true); }
    if constexpr (ST == 3) { waitv_bar<0>(); if (tid == 0) setflag(ws, WS_FU + b * 8, tag); }
    else waitv_bar<(ST == 2 ? 10 : ST == 4 ? 6 : 12)>();
    if constexpr (ST < 7) issue_w1(ST + 3); else issue_w2(ST - 7);
    if constexpr ((ST & 1) == 0) acc[R & 1] = bias_init(pair_unit(R) + hf);
    LAS char* p = smem + (ST & 3) * BUF + a0;
    sfor<0, 18>([&](auto S) { acc[R & 1] = mm32b(tr2(p + S * 2048, 512), xn[18 * (ST & 1) + S], acc[R & 1]); });
    gelu_store(acc[(R - 1) & 1], std::integral_constant<int, (ST & 1)>{}, R == 1 ? lone : pair_unit(R - 1) + hf, R == 1 ? rs0 : rs);
  });

  const int T = hf == 0 ? b : 256 + b;
  const bool fft_on = T <= 288;
  const bool special = T == 0 || T == 288;
  const int cre = T == 288 ? 1 : 2 * T;
  u32x4 tF64[2][2], tF32[2][2], tG32[2][2], tG64[2][2], uu[2][2][2], wt[2];
  if (fft_on) {
    const uint32_t vu = i16 * 128 + gq * 16; const char* su = ws + WS_UT + (size_t)cre * 4096;
    sfor<0, 2>([&](auto P) { sfor<0, 2>([&](auto TT) { sfor<0, 2>([&](auto A) { gld16c<TT * 2048 + A * 64>(uu[P][TT][A], vu, su + P * 4096); }); }); });
    const uint32_t v2 = l * 16;
    sfor<0, 2>([&](auto C) { sfor<0, 2>([&](auto A) { gld16<C * 2048 + A * 1024>(tF64[C][A], v2, tb + TAB_F64 + g * 4096); }); });
    sfor<0, 2>([&](auto C) { sfor<0, 2>([&](auto A) { gld16<C * 2048 + A * 1024>(tF32[C][A], v2, tb + TAB_F32); }); });
    const uint32_t vw = i16 * 128 + gq * 32;
    sfor<0, 2>([&](auto P) { gld16c<P * 16>(wt[P], vw, ws + WS_WT + T * 8192 + g * 2048); });
    sfor<0, 2>([&](auto C) { sfor<0, 2>([&](auto A) { gld16<C * 2048 + A * 1024>(tG32[C][A], v2, tb + TAB_G32); }); });
    sfor<0, 2>([&](auto C) { sfor<0, 2>([&](auto A) { gld16<C * 2048 + A * 1024>(tG64[C][A], v2, tb + TAB_G64 + g * 4096); }); });
  }
  gelu_store(acc[0], std::integral_constant<int, 0>{}, pair_unit(4) + hf, rs);
  gelu_store(acc[0], std::integral_constant<int, 1>{}, pair_unit(4) + hf, rs);
  bar();
  LAS char* cx = smem + L_CX + hf * 9216;
  if (fft_on) {
    waitv<2>();
    sfor<0, 2>([&](auto C) { sfor<0, 2>([&](auto A) { pin(tF64[C][A]); pin(tF32[C][A]); pin(tG32[C][A]); pin(tG64[C][A]); pin(uu[0][C][A]); pin(uu[1][C][A]); }); pin(wt[C]); });
    if (special) sfor<0, 2>([&](auto TT) { sfor<0, 2>([&](auto A) { uu[1][TT][A] = u32x4{0, 0, 0, 0}; }); });
    const f32x4 z4 = {0, 0, 0, 0};
    float br[8], bi[8];
    sfor<0, 2>([&](auto TT) {
      f32x4 P = mm16h(uu[0][TT][0], tF64[0][0], z4); P = mm16h(uu[0][TT][1], tF64[0][1], P);
      f32x4 Q = mm16h(uu[1][TT][0], tF64[1][0], z4); Q = mm16h(uu[1][TT][1], tF64[1][1], Q);
      f32x4 I = mm16h(uu[0][TT][0], tF64[1][0], z4); I = mm16h(uu[0][TT][1], tF64[1][1], I);
      I = mm16h(uu[1][TT][0], tF64[0][0], I); I = mm16h(uu[1][TT][1], tF64[0][1], I);
      sfor<0, 4>([&](auto J) { const float an = (float)((16 * TT + 4 * gq + J) * (16 * g + i16)) * (1.0f / 2048.0f);
        const float c = __builtin_amdgcn_cosf(an), s = __builtin_amdgcn_sinf(an), re = P[(int)J] - Q[(int)J], im = I[(int)J];
        br[4 * TT + J] = re * c + im * s; bi[4 * TT + J] = im * c - re * s; });
    });
    const u32x4 Br = {pk_h(br[0], br[1]), pk_h(br[2], br[3]), pk_h(br[4], br[5]), pk_h(br[6], br[7])};
    const u32x4 Bi = {pk_h(bi[0], bi[1]), pk_h(bi[2], bi[3]), pk_h(bi[4], bi[5]), pk_h(bi[6], bi[7])};
    float yr[8], yi[8];
    sfor<0, 2>([&](auto TT) {
      f32x4 P = mm16h(tF32[0][TT], Br, z4), Q = mm16h(tF32[1][TT], Bi, z4);
      f32x4 I = mm16h(tF32[1][TT], Br, z4); I = mm16h(tF32[0][TT], Bi, I);
      sfor<0, 4>([&](auto J) { const unsigned a = wt[0][2 * TT + (J >> 1)], c = wt[1][2 * TT + (J >> 1)];
        const float p = (J & 1) ? hi_f(a) : lo_f(a), q = (J & 1) ? hi_f(c) : lo_f(c), re = P[(int)J] - Q[(int)J], im = I[(int)J];
        yr[4 * TT + J] = re * p - im * q; yi[4 * TT + J] = re * q + im * p; });
    });
    const u32x4 Yr = {pk_bf(yr[0], yr[1]), pk_bf(yr[2], yr[3]), pk_bf(yr[4], yr[5]), pk_bf(yr[6], yr[7])};
    const u32x4 Yi = {pk_bf(yi[0], yi[1]), pk_bf(yi[2], yi[3]), pk_bf(yi[4], yi[5]), pk_bf(yi[6], yi[7])};
    sfor<0, 2>([&](auto TT) {
      f32x4 P = mm16b(Yr, tG32[0][TT], z4), Q = mm16b(Yi, tG32[1][TT], z4);
      f32x4 I = mm16b(Yr, tG32[1][TT], z4); I = mm16b(Yi, tG32[0][TT], I);
      float cr[4], ci[4];
      sfor<0, 4>([&](auto J) { const float an = (float)((16 * TT + i16) * (16 * g + 4 * gq + J)) * (1.0f / 2048.0f);
        const float c = __builtin_amdgcn_cosf(an), s = __builtin_amdgcn_sinf(an), re = P[(int)J] - Q[(int)J], im = I[(int)J];
        cr[J] = re * c - im * s; ci[J] = im * c + re * s; });
      LAS char* p = cx + (16 * TT + i16) * 144 + (16 * g + 4 * gq) * 2;
      *(LAS u32x2*)p = u32x2{pk_bf(cr[0], cr[1]), pk_bf(cr[2], cr[3])};
      *(LAS u32x2*)(p + 4608) = u32x2{pk_bf(ci[0], ci[1]), pk_bf(ci[2], ci[3])};
    });
  }
  waitv_bar<0>();
  if (tid == 0) setflag(ws, WS_FH + b * 128, tag);
  u32x4 hf_[4][4];
  const uint32_t v16 = l * 16;
  auto issue_h = [&](auto J) { sfor<0, 2>([&](auto KK) { const char* sb = ws + WS_H + (size_t)((mt * 144 + fblk(4 * J + 2 * hf + KK)) * 4 + g) * 2048;
      gld16c<0>(hf_[J & 3][2 * KK], v16, sb); gld16c<1024>(hf_[J & 3][2 * KK + 1], v16, sb); }); };
  issue_h(std::integral_constant<int, 0>{}); issue_h(std::integral_constant<int, 1>{});
  if (fft_on) {
    const f32x4 z4 = {0, 0, 0, 0};
    sfor<0, 2>([&](auto TT) {
      LAS char* p = cx + (16 * TT + i16) * 144 + gq * 16;
      const u32x4 r0 = *(LAS u32x4*)p, r1 = *(LAS u32x4*)(p + 64), i0 = *(LAS u32x4*)(p + 4608), i1 = *(LAS u32x4*)(p + 4608 + 64);
      f32x4 P = mm16b(tG64[0][0], r0, z4); P = mm16b(tG64[0][1], r1, P);
      f32x4 Q = mm16b(tG64[1][0], i0, z4); Q = mm16b(tG64[1][1], i1, Q);
      f32x4 I = mm16b(tG64[1][0], r0, z4); I = mm16b(tG64[1][1], r1, I); I = mm16b(tG64[0][0], i0, I); I = mm16b(tG64[0][1], i1, I);
      unsigned ra = pk_bf(P.x - Q.x, P.y - Q.y), rb = pk_bf(P.z - Q.z, P.w - Q.w), ia = pk_bf(I.x, I.y), ib = pk_bf(I.z, I.w);
      auto sa = __builtin_amdgcn_permlane16_swap(ra, ia, false, false); auto sb = __builtin_amdgcn_permlane16_swap(rb, ib, false, false);
      const unsigned sa0 = sa[0], sa1 = sa[1], sb0 = sb[0], sb1 = sb[1];
      const u32x4 o = {sa0, sb0, sa1, sb1};
      const uint32_t vo = (gq & 1) * 4096 + (16 * TT + i16) * 128 + (gq >> 1) * 16;
      if (!(special && (gq & 1))) st16c<0>(o, rs, vo, WS_ZT + cre * 4096 + g * 32);
    });
  }

  f32x4 oc[2][9];
  sfor<0, 2>([&](auto TT) { sfor<0, 9>([&](auto DT) { oc[TT][DT] = f32x4{0, 0, 0, 0}; }); });
  const uint32_t a2 = (16 * (gq >> 1) + 4 * (gq & 1) + q4) * 288 + p4 * 8;
  u32x4 zr[5];
  sfor<0, 9>([&](auto J) {
    if constexpr (J == 0) { if (w == 0) poll<1>(ws, WS_FH + ((b & ~24) + (l & 3) * 8) * 128, 0, tag, true); waitv_bar<4>(); }
    else if constexpr (J == 1) { waitv_bar<0>(); if (tid == 0) setflag(ws, WS_FZ + b * 8, tag); }
    else if constexpr (J == 7) { if (w == 0) poll<4>(ws, WS_FZ + l * 8, 512, tag, true); waitv_bar<14>(); }
    else if constexpr (J == 8) waitv_bar<0>();
    else waitv_bar<(J == 2 ? 9 : 18)>();
    sfor<0, 4>([&](auto I) { pin(hf_[J & 3][I]); });
    u32x4 cur[4]; sfor<0, 4>([&](auto I) { cur[I] = hf_[J & 3][I]; });
    if constexpr (J + 3 <= 9) issue_w2(J + 3);
    if constexpr (J == 0) issue_h(std::integral_constant<int, 2>{});
    if constexpr (J + 3 <= 8) issue_h(std::integral_constant<int, J + 3>{});
    if constexpr (J == 7) {
      if (ks < 2) { dma16(vo2a, tb + TAB_TI + (size_t)(c0 + 128) * 1152 + 288 * nt, w * 1024); if (w < 2) dma16h(vo2b, tb + TAB_TI + (size_t)(c0 + 128) * 1152 + 288 * nt, 8192 + (w & 1) * 512); }
      const uint32_t vz = gq * 4096 + i16 * 16;
      sfor<0, 5>([&](auto I) { gld16c<0>(zr[I], vz, ws + WS_ZT + (size_t)(c0 + (I == 4 && ks >= 2 ? 0 : 32 * I) + 4 * w) * 4096 + mt * 256); });
    }
    if constexpr (J == 8) { sfor<0, 5>([&](auto I) { pin(zr[I]); *(LAS u32x4*)(smem + L_Z + (32 * I + 4 * w + gq) * 288 + i16 * 16) = zr[I]; }); }
    LAS char* p = smem + ((J + 2) & 3) * BUF + hf * 18432 + a2;
    sfor<0, 2>([&](auto KK) { sfor<0, 9>([&](auto DT) { const u32x4 a = tr2(p + KK * 9216 + DT * 32, 2304);
      oc[0][DT] = mm16b(a, cur[2 * KK], oc[0][DT]); oc[1][DT] = mm16b(a, cur[2 * KK + 1], oc[1][DT]); }); });
  });
  bar();
  { auto zstep = [&](LAS char* p, LAS char* pz) { const u32x4 z0 = tr2(pz, 2304), z1 = tr2(pz + 32, 2304);
      sfor<0, 9>([&](auto DT) { const u32x4 a = tr2(p + DT * 32, 2304); oc[0][DT] = mm16b(a, z0, oc[0][DT]); oc[1][DT] = mm16b(a, z1, oc[1][DT]); }); };
    sfor<0, 2>([&](auto KK) { zstep(smem + 3 * BUF + hf * 18432 + KK * 9216 + a2, smem + L_Z + hf * 18432 + KK * 9216 + a2 + g * 64); });
    if (ks < 2 && hf == 0) zstep(smem + a2, smem + L_Z + 36864 + a2 + g * 64);
  }
  bar();
  f32x4 res[9];
  { LAS char* pw = smem + w * 9216 + l * 16; LAS char* pr = smem + (w ^ 4) * 9216 + l * 16;
    sfor<0, 9>([&](auto DT) { *(LAS f32x4*)(pw + DT * 1024) = hf ? oc[0][DT] : oc[1][DT]; });
    bar();
    sfor<0, 9>([&](auto DT) { res[DT] = (hf ? oc[1][DT] : oc[0][DT]) + *(LAS f32x4*)(pr + DT * 1024); });
  }
  if (g != ks) {
    const uint32_t so = WS_PART + ((((mt * 4 + nt) * 4 + g) * 4 + ks) * 2 + hf) * 9216;
    sfor<0, 9>([&](auto DT) { st16c<(DT & 3) * 1024>(__builtin_bit_cast(u32x4, res[DT]), rs, v16, so + (DT >> 2) * 4096); });
  }
  waitv_bar<0>();
  if (tid == 0) setflag(ws, WS_FP + b * 128, tag);
  if (g == ks) {
    const int srow = 32 * (32 * (g & 1) + 16 * hf + i16) + 2 * mt + (g >> 1);
    const uint32_t xo = srow * 1152 + (144 * nt + 4 * gq) * 2;
    u32x2 xv[9], bv[9];
    sfor<0, 9>([&](auto DT) { xv[DT] = *(const u32x2*)(x + xo + DT * 32); bv[DT] = *(const u32x2*)(b2 + (144 * nt + 4 * gq) * 2 + DT * 32); });
    poll<1>(ws, WS_FP + ((b & ~6) + (l & 3) * 2) * 128, 0, tag, true);
    u32x4 pp[3][9];
    sfor<1, 4>([&](auto K) { const char* sb = ws + WS_PART + ((((mt * 4 + nt) * 4 + g) * 4 + ((ks + K) & 3)) * 2 + hf) * 9216;
      sfor<0, 9>([&](auto DT) { gld16c<(DT & 3) * 1024>(pp[K - 1][DT], v16, sb + (DT >> 2) * 4096); }); });
    waitv<0>();
    sfor<0, 3>([&](auto K) { sfor<0, 9>([&](auto DT) { pin(pp[K][DT]); res[DT] += __builtin_bit_cast(f32x4, pp[K][DT]); }); });
    sfor<0, 9>([&](auto DT) { const f32x4 r = res[DT];
      u32x2 o = {pk_bf(r.x + lo_f(xv[DT].x) + lo_f(bv[DT].x), r.y + hi_f(xv[DT].x) + hi_f(bv[DT].x)), pk_bf(r.z + lo_f(xv[DT].y) + lo_f(bv[DT].y), r.w + hi_f(xv[DT].y) + hi_f(bv[DT].y))};
      *(u32x2*)(out + xo + DT * 32) = o; });
  }
  if (b == 0 && tid == 0) setflag(ws, WS_EPOCH, tag);
}

extern "C" void kernel_launch(void* const* d_in, const int* in_sizes, int n_in, void* d_out, int out_size, void* d_ws, size_t ws_size, hipStream_t stream) {
  (void)hipFuncSetAttribute((const void*)fused_kernel, hipFuncAttributeMaxDynamicSharedMemorySize, LDS_BYTES);
  fused_kernel<<<256, 512, LDS_BYTES, stream>>>((const char*)d_in[0], (const char*)d_in[1], (const char*)d_in[2], (const char*)d_in[4], (const char*)d_in[5],
                                                (const char*)d_in[6], (const char*)d_in[7], (char*)d_out, (char*)d_ws);
}
```

```cpp
#include <hip/hip_runtime.h>
#include <stdint.h>
#include <type_traits>

typedef unsigned short u16;
typedef short s16x4 __attribute__((ext_vector_type(4)));
typedef short s16x8 __attribute__((ext_vector_type(8)));
typedef __bf16 bf16x8 __attribute__((ext_vector_type(8)));
typedef __bf16 bf16x2 __attribute__((ext_vector_type(2)));
typedef _Float16 f16x8 __attribute__((ext_vector_type(8)));
typedef _Float16 f16x2 __attribute__((ext_vector_type(2)));
typedef float f32x4 __attribute__((ext_vector_type(4)));
typedef float f32x2 __attribute__((ext_vector_type(2)));
typedef unsigned u32x4 __attribute__((ext_vector_type(4)));
typedef unsigned u32x2 __attribute__((ext_vector_type(2)));
typedef unsigned long long u64;
#define LAS __attribute__((address_space(3)))
#define GAS __attribute__((address_space(1)))
#define DI __device__ __forceinline__

namespace tab {
constexpr double PI = 3.14159265358979323846;
constexpr double ccos(int a, int n) {
  a %= n; if (a < 0) a += n;
  if (2 * a > n) a = n - a;
  bool neg = false;
  if (4 * a > n) { a = n / 2 - a; neg = true; }
  if (4 * a == n) return 0.0;
  double x = 2 * PI * a / n, t = 1, s = 1;
  for (int i = 1; i <= 13; i++) { t *= -x * x / ((2 * i - 1) * (2 * i)); s += t; }
  return neg ? -s : s;
}
constexpr u16 to_bf(double v) { float f = (float)v; uint32_t u = __builtin_bit_cast(uint32_t, f); u += 0x7fffu + ((u >> 16) & 1u); return (u16)(u >> 16); }
constexpr u16 to_h(double v) { _Float16 h = (_Float16)v; return __builtin_bit_cast(u16, h); }
struct C576 { u16 b1[576]; u16 b2[576]; };
constexpr C576 gen576() { C576 r{}; for (int i = 0; i < 576; i++) { double c = ccos(i, 576) / 24.0; r.b1[i] = to_bf(c); r.b2[i] = to_bf(2 * c); } return r; }
constexpr C576 c576 = gen576();
constexpr int colk(int c) { return c == 0 ? 0 : c == 1 ? 288 : c < 288 ? (c & ~1) : ((c - 288) & ~1) + 1; }
constexpr int colp(int c) { return c < 2 ? 0 : (c & 1); }
struct Chunk { u16 v[9216]; };
constexpr Chunk genTF(int u) {
  Chunk r{};
  for (int i = 0; i < 1152; i++) { int t = i / 576, s = (i % 576) >> 6, l = i & 63, c = 32 * u + 16 * t + (l & 15), k = colk(c), o = 144 * colp(c), d = 32 * s + 8 * (l >> 4);
    for (int j = 0; j < 8; j++) r.v[i * 8 + j] = c576.b1[((d + j) * k + o) % 576]; }
  return r;
}
constexpr Chunk genTI(int q) {
  Chunk r{};
  for (int i = 0; i < 64; i++) { int c = 32 * (q >> 1) + (i & 31), k = colk(c), o = 144 * colp(c), d0 = 144 * (2 * (q & 1) + (i >> 5));
    if (c < 2) { for (int d = 0; d < 144; d++) r.v[i * 144 + d] = c576.b1[((d0 + d) * k + o) % 576]; }
    else { for (int d = 0; d < 144; d++) r.v[i * 144 + d] = c576.b2[((d0 + d) * k + o) % 576]; } }
  return r;
}
template <int I> constexpr Chunk cTF = genTF(I);
template <int I> constexpr Chunk cTI = genTI(I);
struct Small { u16 f64[4][2][2][512]; u16 f32[2][2][512]; u16 g32[2][2][512]; u16 g64[4][2][2][512]; };
constexpr Small genSmall() {
  Small r{};
  double c64[64] = {}; for (int i = 0; i < 64; i++) c64[i] = ccos(i, 64);
  const double r32 = 0.17677669529663688110;
  for (int l = 0; l < 64; l++) for (int j = 0; j < 8; j++) {
    int rr = l & 15, gq = l >> 4;
    int p2 = 16 * (j >> 2) + 4 * gq + (j & 3);
    for (int w = 0; w < 4; w++) for (int a = 0; a < 2; a++) {
      int e = (16 * w + rr) * (32 * a + 8 * gq + j), e1 = (16 * w + rr) * (32 * a + p2);
      r.f64[w][0][a][l * 8 + j] = to_h(c64[e1 & 63] / 8); r.f64[w][1][a][l * 8 + j] = to_h(c64[(e1 + 16) & 63] / 8);
      r.g64[w][0][a][l * 8 + j] = to_bf(c64[e & 63] / 8); r.g64[w][1][a][l * 8 + j] = to_bf(c64[(e + 48) & 63] / 8);
    }
    for (int t = 0; t < 2; t++) {
      int e = 2 * ((16 * t + rr) * p2);
      r.f32[0][t][l * 8 + j] = to_h(c64[e & 63] * r32); r.f32[1][t][l * 8 + j] = to_h(c64[(e + 16) & 63] * r32);
      r.g32[0][t][l * 8 + j] = to_bf(c64[e & 63] * r32); r.g32[1][t][l * 8 + j] = to_bf(c64[(e + 48) & 63] * r32);
    }
  }
  return r;
}
struct alignas(256) All { Chunk tf[18]; Chunk ti[36]; Small sm; };
template <int... I, int... J> constexpr All mk(std::integer_sequence<int, I...>, std::integer_sequence<int, J...>) { return All{{cTF<I>...}, {cTI<J>...}, genSmall()}; }
}
static __device__ const tab::All g_tab = tab::mk(std::make_integer_sequence<int, 18>{}, std::make_integer_sequence<int, 36>{});

constexpr uint32_t BUF = 36864, L_SP = 147456, L_B1 = L_SP, L_JUNK = L_SP + 2048, L_W = L_SP + 4096, LDS_BYTES = 163840;
constexpr uint32_t WS_EPOCH = 0, WS_FU = 4096, WS_FZ = 8192, WS_FH = 16384, WS_FP = 49152;
constexpr uint32_t WS_H = 1u << 20, WS_UT = 20u << 20, WS_WT = 23u << 20, WS_ZT = 26u << 20, WS_PART = 29u << 20, WS_END = 48u << 20;
constexpr uint32_t TAB_TF = 0, TAB_TI = 18 * 18432, TAB_F64 = 54 * 18432, TAB_F32 = TAB_F64 + 16384, TAB_G32 = TAB_F32 + 4096, TAB_G64 = TAB_G32 + 4096, TAB_END = TAB_G64 + 16384;

template <int I, int N, class Fn> DI void sfor(Fn&& f) { if constexpr (I < N) { f(std::integral_constant<int, I>{}); sfor<I + 1, N>(f); } }
template <int V> using IC = std::integral_constant<int, V>;

DI u32x4 mksrd(const void* p, uint32_t n) { return u32x4{(unsigned)(size_t)p, (unsigned)((size_t)p >> 32) & 0xffffu, n, 0x00020000u}; }
template <int IMM> DI void ld16(u32x4& d, u32x4 srd, uint32_t voff, uint32_t soff) { asm volatile("buffer_load_dwordx4 %0, %1, %2, %3 offen offset:%4" : "=v"(d) : "v"(voff), "s"(srd), "s"(soff), "i"(IMM) : "memory"); }
template <int IMM> DI void ld16c(u32x4& d, u32x4 srd, uint32_t voff, uint32_t soff) { asm volatile("buffer_load_dwordx4 %0, %1, %2, %3 offen offset:%4 sc1" : "=v"(d) : "v"(voff), "s"(srd), "s"(soff), "i"(IMM) : "memory"); }
template <int IMM> DI void ld8(u32x2& d, u32x4 srd, uint32_t voff, uint32_t soff) { asm volatile("buffer_load_dwordx2 %0, %1, %2, %3 offen offset:%4" : "=v"(d) : "v"(voff), "s"(srd), "s"(soff), "i"(IMM) : "memory"); }
DI void ld8c(u32x2& d, u32x4 srd, uint32_t voff, uint32_t soff) { asm volatile("buffer_load_dwordx2 %0, %1, %2, %3 offen sc1" : "=v"(d) : "v"(voff), "s"(srd), "s"(soff) : "memory"); }
DI void ldu16(unsigned& d, u32x4 srd, uint32_t voff, uint32_t soff) { asm volatile("buffer_load_ushort %0, %1, %2, %3 offen" : "=v"(d) : "v"(voff), "s"(srd), "s"(soff) : "memory"); }
DI void dma(u32x4 srd, uint32_t voff, uint32_t soff, uint32_t lds) { asm volatile("s_mov_b32 m0, %3\n\ts_nop 0\n\tbuffer_load_dwordx4 %0, %1, %2 offen lds" :: "v"(voff), "s"(srd), "s"(soff), "s"(lds) : "memory"); }
DI void dmah(u32x4 srd, uint32_t voff, uint32_t soff, uint32_t lds, u64 mask) {
  asm volatile("s_mov_b32 m0, %3\n\ts_mov_b64 exec, %4\n\tbuffer_load_dwordx4 %0, %1, %2 offen lds\n\ts_mov_b64 exec, -1" :: "v"(voff), "s"(srd), "s"(soff), "s"(lds), "s"(mask) : "memory"); }
DI void dma4m(u32x4 srd, uint32_t voff, uint32_t soff, uint32_t lds) {
  asm volatile("s_mov_b32 m0, %3\n\ts_mov_b32 exec_hi, 0xff\n\tbuffer_load_dword %0, %1, %2 offen lds\n\ts_mov_b32 exec_hi, -1" :: "v"(voff), "s"(srd), "s"(soff), "s"(lds) : "memory"); }
DI void touch(u32x4 srd, uint32_t voff, uint32_t soff) {
  asm volatile("s_mov_b32 m0, %3\n\ts_nop 0\n\tbuffer_load_dword %0, %1, %2 offen lds" :: "v"(voff), "s"(srd), "s"(soff), "s"(L_JUNK) : "memory"); }
template <int IMM> DI void st16c(u32x4 d, u32x4 srd, uint32_t voff, uint32_t soff) {
  asm volatile("buffer_store_dwordx4 %0, %1, %2, %3 offen offset:%4 sc1\n\ts_nop 1" :: "v"(d), "v"(voff), "s"(srd), "s"(soff), "i"(IMM) : "memory"); }
DI void st8c(u32x2 d, u32x4 srd, uint32_t voff, uint32_t soff) {
  asm volatile("buffer_store_dwordx2 %0, %1, %2, %3 offen sc1\n\ts_nop 1" :: "v"(d), "v"(voff), "s"(srd), "s"(soff) : "memory"); }
template <int N> DI void waitv() { asm volatile("s_waitcnt vmcnt(%0)" :: "i"(N) : "memory"); }
template <int N> DI void waitv_b() { asm volatile("s_waitcnt vmcnt(%0)\n\ts_barrier" :: "i"(N) : "memory"); }
template <int N> DI void waitv_lb() { asm volatile("s_waitcnt vmcnt(%0) lgkmcnt(0)\n\ts_barrier" :: "i"(N) : "memory"); }
DI void bar0() { asm volatile("s_barrier" ::: "memory"); }
DI void barl() { asm volatile("s_waitcnt lgkmcnt(0)\n\ts_barrier" ::: "memory"); }
DI void pin(u32x4& v) { asm volatile("" : "+v"(v)); }
DI void pin(u32x2& v) { asm volatile("" : "+v"(v)); }
DI void pin(unsigned& v) { asm volatile("" : "+v"(v)); }
DI uint32_t oq_s(uint32_t v) { asm volatile("" : "+s"(v)); return v; }
DI uint32_t oq_v(uint32_t v) { asm volatile("" : "+v"(v)); return v; }
#define SB() __builtin_amdgcn_sched_barrier(0)
template <int C> DI void tif() { asm volatile("; @IF %0" :: "i"(C)); }
template <int C> DI void tend() { asm volatile("; @END %0" :: "i"(C)); }

DI float asf(unsigned u) { return __builtin_bit_cast(float, u); }
DI unsigned asu(float f) { return __builtin_bit_cast(unsigned, f); }
DI unsigned pk_bf(float a, float b) { f32x2 p = {a, b}; bf16x2 r = __builtin_convertvector(p, bf16x2); return __builtin_bit_cast(unsigned, r); }
DI unsigned pk_h(float a, float b) { f32x2 p = {a, b}; f16x2 r = __builtin_convertvector(p, f16x2); return __builtin_bit_cast(unsigned, r); }
DI float bfr(float a) { return asf(pk_bf(a, a) << 16); }
DI float lo_f(unsigned u) { return asf(u << 16); }
DI float hi_f(unsigned u) { return asf(u & 0xffff0000u); }
DI float dot2(unsigned a, unsigned b, float c) { return __builtin_amdgcn_fdot2_f32_bf16(__builtin_bit_cast(bf16x2, a), __builtin_bit_cast(bf16x2, b), c, false); }
template <int CTRL> DI float dpp_add(float v) { return v + asf(__builtin_amdgcn_update_dpp(0, asu(v), CTRL, 0xf, 0xf, true)); }
DI float sum8(float v) { return dpp_add<0x141>(dpp_add<0x4E>(dpp_add<0xB1>(v))); }
DI f32x4 mmb(u32x4 a, u32x4 b, f32x4 c) { return __builtin_amdgcn_mfma_f32_16x16x32_bf16(__builtin_bit_cast(bf16x8, a), __builtin_bit_cast(bf16x8, b), c, 0, 0, 0); }
DI f32x4 mmh(u32x4 a, u32x4 b, f32x4 c) { return __builtin_amdgcn_mfma_f32_16x16x32_f16(__builtin_bit_cast(f16x8, a), __builtin_bit_cast(f16x8, b), c, 0, 0, 0); }
DI u32x4 tr2(LAS char* p, int second) {
  s16x4 a = __builtin_amdgcn_ds_read_tr16_b64_v4i16((LAS s16x4*)p);
  s16x4 b = __builtin_amdgcn_ds_read_tr16_b64_v4i16((LAS s16x4*)(p + second));
  s16x8 c = __builtin_shufflevector(a, b, 0, 1, 2, 3, 4, 5, 6, 7);
  return __builtin_bit_cast(u32x4, c);
}
DI unsigned norm1(unsigned u, float r, float mb) { return pk_bf(__builtin_fmaf(lo_f(u), r, mb), __builtin_fmaf(hi_f(u), r, mb)); }
struct Gelu { float a0, a1;
  template <int Q> DI void step(float v0, float v1, unsigned& o) {
    if constexpr (Q == 0) { a0 = __builtin_fmaf(v0 * v0, -0.10294324f, -2.3022082f); a1 = __builtin_fmaf(v1 * v1, -0.10294324f, -2.3022082f); }
    if constexpr (Q == 1) { a0 = __builtin_amdgcn_exp2f(v0 * a0); a1 = __builtin_amdgcn_exp2f(v1 * a1); }
    if constexpr (Q == 2) { a0 = __builtin_amdgcn_rcpf(1.0f + a0); a1 = __builtin_amdgcn_rcpf(1.0f + a1); }
    if constexpr (Q == 3) { o = pk_bf(v0 * a0, __builtin_fmaf(v1, a1, 0.0f)); }
  }
  template <int K> DI void run(const f32x4 (&p)[2], u32x4& o) {
    constexpr int P = K >> 2; const float v0 = p[P >> 1][2 * (P & 1)], v1 = p[P >> 1][2 * (P & 1) + 1];
    unsigned t = 0; step<K & 3>(v0, v1, t); if constexpr ((K & 3) == 3) o[P] = t; }
};
#define ST_(i) do {} while (0)
#define RLX_AGENT __ATOMIC_RELAXED, __HIP_MEMORY_SCOPE_AGENT
DI bool lvl_ok(unsigned lo, unsigned hi, u64 tag, unsigned lvl) { return (hi == (unsigned)(tag >> 32)) & (lo - (unsigned)tag - lvl <= 7u - lvl); }
template <int N> DI void spin(const char* base, uint32_t off, u64 tag, unsigned lvl) {
#pragma nounroll
  for (unsigned spins = 0; spins < (1u << 17); ++spins) {
    bool ok = true;
#pragma unroll
    for (int k = 0; k < N; ++k) { u64 v = __hip_atomic_load((GAS u64*)(base + off + (k & 1) * 8 + (k >> 1) * 1024), RLX_AGENT); ok &= lvl_ok((unsigned)v, (unsigned)(v >> 32), tag, lvl); }
    if (__all(ok)) break;
    __builtin_amdgcn_s_sleep(1);
  }
}
DI void setflag(char* ws, uint32_t off, u64 v) { __hip_atomic_store((GAS u64*)(ws + off), v, RLX_AGENT); }

__global__ __launch_bounds__(512, 2) void fused_kernel(const char* __restrict__ x, const char* __restrict__ wr, const char* __restrict__ wi,
    const char* __restrict__ w1, const char* __restrict__ b1, const char* __restrict__ w2, const char* __restrict__ b2, char* out, char* ws) {
  extern __shared__ __attribute__((aligned(16))) char smem_[];
  LAS char* smem = (LAS char*)smem_;
  const int tid = threadIdx.x, l = tid & 63;
  const int w = __builtin_amdgcn_readfirstlane(tid >> 6), g = w & 3, hf = w >> 2;
  const int b = blockIdx.x, xcd = b & 7, jj = b >> 3;
  const int ks = xcd >> 1, mt = (xcd & 1) * 8 + (jj >> 2), nt = jj & 3, fj = ks * 4 + nt;
  const u32x4 rs = mksrd(ws, WS_END), rx = mksrd(x, 2048 * 1152), r1 = mksrd(w1, 576 * 9216), r2 = mksrd(w2, 4608 * 1152), rt = mksrd(&g_tab, TAB_END);
  const int i16 = l & 15, q4 = i16 >> 2, p4 = i16 & 3, hh = l >> 5, gq = l >> 4;
  const u64 hmask = (w & 1) ? 0xffffffff00000000ull : 0xffffffffull;
  const uint32_t v16 = l * 16;
  ST_(0);
  u32x4 xr[2][9], x0[18], x1[18];
  const uint32_t vx = (l >> 3) * 36864 + (l & 7) * 16;
  const uint32_t sx = (uint32_t)((32 * (g & 1) + 8 * hf) * 32 + 2 * mt + (g >> 1)) * 1152;
  sfor<0, 9>([&](auto C) { ld16<C * 128>(xr[0][C], rx, vx, sx); });
  bar0();
  sfor<0, 9>([&](auto C) { ld16<C * 128>(xr[1][C], rx, vx, sx + 589824); });
  const int odd = fj & 1, lone = odd ? 0 : 8;
  const int tileA = 18 * odd + 2 * (fj >> 1), tileC = 18 * odd + 16 + (nt >> 1);
  unsigned b1v; u32x2 ep;
  auto early = [&](auto Q) {
    if constexpr (Q < 5) { const uint32_t vo = (8 * ((l >> 4) & 1) + 4 * hh + ((l >> 2) & 3)) * 9216 + (((l & 3) ^ (((l >> 4) & 1) << 1)) * 16), so = (288 * fj + 32 * lone) * 2;
      if constexpr (Q < 4) dma(r1, vo, so + (64 * w + 16 * Q) * 9216, (4 * w + Q) * 1024);
      else dmah(r1, vo, so + (512 + 16 * (w >> 1)) * 9216, (32 + (w >> 1)) * 1024, hmask); }
    else if constexpr (Q < 7) dma(rt, v16, TAB_TF + tileA * 9216 + (w + 8 * (Q - 5)) * 1024, BUF + (w + 8 * (Q - 5)) * 1024);
    else if constexpr (Q == 7) {
      if (w < 2) dma(rt, v16, TAB_TF + tileA * 9216 + (w + 16) * 1024, BUF + (w + 16) * 1024);
      if (ks == 2) { dma(rt, v16, TAB_TF + tileC * 9216 + w * 1024, BUF + 18432 + w * 1024); if (w == 7) dma(rt, v16, TAB_TF + tileC * 9216 + 8192, BUF + 18432 + 8192); }
      ld8c(ep, rs, 0, WS_EPOCH); }
    else if constexpr (Q == 8) ldu16(b1v, mksrd(b1, 9216), tid * 2, 576 * fj);
    else if constexpr (Q == 9) { const int i = jj * 324 + (tid < 324 ? tid : 323); touch(r1, (i / 18) * 9216 + (i % 18) * 128, ks * 2304); }
    else {
      constexpr int J = (Q - 10) & 3, P = (Q - 10) >> 2;
      dma4m(mksrd(P ? wi : wr, 2048 * 578), l * 4, ((8 * (b & 7) + w + 64 * (4 * ((b >> 3) & 7) + J)) * 578 + 144 * (b >> 6)) & ~3u, L_W + ((Q - 10) * 8 + w) * 160); }
  };
  const uint32_t vo1 = (8 * hh + ((l >> 3) & 3)) * 9216 + (((l & 7) ^ (((l >> 4) & 3) << 1)) * 16);
  uint32_t so1 = 0, sw = 0;
  auto dma1 = [&](auto ST, auto I) {
    if constexpr (I == 0) { sw = oq_s(w); so1 = (288 * (ST & 1)) * 9216 + (288 * fj + 32 * (odd + 2 * ((ST >> 1) - 1))) * 2; }
    constexpr uint32_t lb = (ST & 3) * BUF;
    if constexpr (I < 4) dma(r1, vo1, so1 + (32 * sw + 16 * (I >> 1) + 4 * (I & 1)) * 9216, lb + (4 * sw + I) * 1024);
    else dmah(r1, vo1, so1 + (256 + 16 * (sw >> 2) + 4 * ((sw >> 1) & 1)) * 9216, lb + (32 + (sw >> 1)) * 1024, hmask);
  };
  const uint32_t axw = 2 * BUF + g * 18432 + ((l >> 2) & 1) * 1024 + (l & 3) * 256 + (((8 * hf + (l >> 3)) ^ (l & 7)) * 16);
  const uint32_t axr0 = 2 * BUF + g * 18432 + gq * 256 + ((i16 ^ gq) * 16), axr1 = axr0 ^ 64;
  u64 tag = 0;
  sfor<0, 2>([&](auto RB) {
    float s0 = 0, s1 = 0, q0 = 0, q1 = 0;
    sfor<0, 9>([&](auto C) {
      waitv<(RB == 0 ? 17 : 26 - C)>(); pin(xr[RB][C]);
      const u32x4 v = xr[RB][C];
      s0 = dot2(v.x, 0x3f803f80u, s0); q0 = dot2(v.x, v.x, q0); s1 = dot2(v.y, 0x3f803f80u, s1); q1 = dot2(v.y, v.y, q1);
      s0 = dot2(v.z, 0x3f803f80u, s0); q0 = dot2(v.z, v.z, q0); s1 = dot2(v.w, 0x3f803f80u, s1); q1 = dot2(v.w, v.w, q1);
      if constexpr (RB == 0) early(C);
      SB(); });
    ST_(1 + 2 * RB);
    const float mean = sum8(s0 + s1) * (1.0f / 576.0f);
    const float var = fmaxf(sum8(q0 + q1) * (1.0f / 576.0f) - mean * mean, 0.0f);
    const float sd = bfr(__builtin_sqrtf(bfr(bfr(var) + 1e-5f)));
    const float rn = 1.0f / sd, mb = -bfr(mean) * rn;
    if constexpr (RB == 1) barl();
    sfor<0, 9>([&](auto C) { const u32x4 v = xr[RB][C];
      *(LAS u32x4*)(smem + axw + C * 2048) = u32x4{norm1(v.x, rn, mb), norm1(v.y, rn, mb), norm1(v.z, rn, mb), norm1(v.w, rn, mb)};
      if constexpr (RB == 0) early(IC<9 + C>{});
      SB(); });
    if constexpr (RB == 0) { barl(); ST_(2); sfor<0, 18>([&](auto S) { x0[S] = *(LAS u32x4*)(smem + ((S & 1) ? axr1 : axr0) + S * 1024); }); SB(); }
    else {
      waitv<9>(); pin(b1v); pin(ep);
      u64 e = ((u64)(unsigned)__builtin_amdgcn_readfirstlane(ep.y) << 32) | (unsigned)__builtin_amdgcn_readfirstlane(ep.x);
      e += 0x9E3779B97F4A7C15ull; e = (e ^ (e >> 30)) * 0xBF58476D1CE4E5B9ull; e = (e ^ (e >> 27)) * 0x94D049BB133111EBull; tag = (e ^ (e >> 31)) & ~7ull;
      if (tid < 288) *(LAS float*)(smem + L_B1 + tid * 4) = lo_f(b1v);
      barl(); ST_(4); }
  });
  f32x4 c0[2], c1[2], p0[2], p1[2];
  auto bias = [&](int unit, auto FT) { return *(LAS f32x4*)(smem + L_B1 + (32 * unit + 16 * FT + 4 * gq) * 4); };
  const int rho = 16 * (gq >> 1) + 4 * (gq & 1) + q4;
  uint32_t vo2a, vo2b;
  { auto f = [&](int c, int stride) { const int r = c / 18, k = c % 18; return (uint32_t)(r * stride + (k < 16 ? ((k + 4 * ks) & 15) : k) * 16); };
    vo2a = f(w * 64 + l, 1152); vo2b = f(512 + l, 1152); }
  auto fblk = [&](auto J, int i) {
    int pn, u;
    if constexpr (J < 2) { pn = nt; u = odd + 2 * i + J; }
    else if constexpr (J == 9) { pn = (nt + i) & 3; u = (pn & 1) ? 0 : 8; }
    else { const int q = 4 * (J - 2) + i; const int r = q / 6, m = q - 6 * r; pn = (nt + 1 + (m >> 1)) & 3; u = (pn & 1) + 2 * r + (m & 1); }
    return __builtin_amdgcn_readfirstlane(36 * ks + 9 * pn + u); };
  auto dma2 = [&](auto J, auto I) {
    if constexpr (I == 0) sw = oq_s(w);
    constexpr uint32_t lb = ((J + 2) & 3) * BUF;
    if constexpr (I < 4) dma(r2, vo2a, fblk(J, I) * 36864 + 288 * nt, lb + I * 9216 + sw * 1024);
    else dmah(r2, vo2b, fblk(J, sw >> 1) * 36864 + 288 * nt, lb + (sw >> 1) * 9216 + 8192, hmask);
  };
  const uint32_t sh = WS_H + (mt * 144 * 4 + g) * 2048;
  const uint32_t vho = v16 + hf * 1024, vhs = v16 + (hf ^ 1) * 1024;
  const uint32_t vfh = WS_FH + ((b & ~24) + gq * 8) * 128;
  auto check1 = [&](u32x2& p, unsigned lvl) { pin(p); if (!__all(lvl_ok(p.x, p.y, tag, lvl))) spin<1>(ws, vfh, tag, lvl); };
  auto check4 = [&](u32x4 (&p)[2], uint32_t base) { pin(p[0]); pin(p[1]);
    if (!__all(lvl_ok(p[0].x, p[0].y, tag, 0) & lvl_ok(p[0].z, p[0].w, tag, 0) & lvl_ok(p[1].x, p[1].y, tag, 0) & lvl_ok(p[1].z, p[1].w, tag, 0))) spin<4>(ws, base + v16, tag, 0); };
  const int T = hf ? b : 256 + (xcd & 1) * 32 + jj;
  const bool fft_on = hf || (ks == 3 && T <= 288);
  const bool special = T == 0 || T == 288;
  u32x4 tF64[2][2], tF32[2][2], tG32[2][2], tG64[2][2], uu[2][2][2], ur[2], wt[2], pu[2], hc[4][2], hown[4][2];
  u32x2 pl;

  const uint32_t a1 = rho * 128 + (((2 * hf) ^ ((rho >> 1) & 3)) * 32) + p4 * 8;
  uint32_t pa0 = 0, pa1 = 0;
  u32x4 fr[4];
  auto rd1 = [&](auto ST, auto S) {
    if constexpr (S == 0) { pa0 = oq_v(a1 + (ST & 3) * BUF); pa1 = oq_v(pa0 ^ 32); }
    return tr2(smem + ((S & 1) ? pa1 : pa0) + (S >> 1) * 4096, 1024); };
  LAS char* cx = smem + L_SP + hf * 8192;
  auto wtask = [&](int cl) {
    LAS char* p = smem + L_W + (l & 7) * 160 + (l & 1) * 2 + cl * 2; unsigned v[8];
    sfor<0, 8>([&](auto J) { v[J] = *(LAS u16*)(p + J * 1280); });
    const int kc = 72 * (b >> 6) + cl;
    st16c<0>(u32x4{v[0] | (v[1] << 16), v[2] | (v[3] << 16), v[4] | (v[5] << 16), v[6] | (v[7] << 16)}, rs,
             (uint32_t)(kc == 288 ? 288 : (kc & 1) ? 144 + (kc >> 1) : (kc >> 1)) * 8192 + (l & 7) * 16,
             WS_WT + ((b >> 1) & 3) * 2048 + ((b >> 5) & 1) * 1024 + ((b >> 3) & 3) * 256 + (b & 1) * 128); };
  auto stage = [&](auto ST, auto HF) {
    constexpr int R = ST >> 1, KB = 9 * (ST & 1);
    if constexpr (ST == 3) bar0(); else waitv_b<(ST == 2 ? 2 * HF : ST == 4 ? 8 : 6)>();
    ST_(3 + ST);
    if constexpr (ST == 5) { if (tid == 0) setflag(ws, WS_FU + b * 8, tag); }
    if constexpr (ST == 7 || ST == 9) { if (tid == 0) setflag(ws, WS_FH + b * 128, tag + (ST - 5) / 2); }
    if constexpr (ST == 2 || ST == 4) sfor<0, 3>([&](auto S) { fr[(18 * ST + S) & 3] = rd1(ST, S); });
    f32x4 bi[2];
    if constexpr ((ST & 1) == 0) { const int u = odd + 2 * (R - 1) + hf; bi[0] = bias(u, IC<0>{}); bi[1] = bias(u, IC<1>{}); }
    if constexpr (ST == 8) { ld8c(pl, rs, vfh, 0); ld16c<0>(pu[0], rs, v16, WS_FU); ld16c<1024>(pu[1], rs, v16, WS_FU); }
    if constexpr (ST == 9) {
      check1(pl, 1);
      if (fft_on) { tif<2>(); check4(pu, WS_FU);
        sfor<0, 2>([&](auto Q) { ld16c<Q * 1024>(ur[Q], rs, v16, WS_UT + (T == 288 ? 0 : T * 8192) + g * 2048); });
        sfor<0, 2>([&](auto C) { sfor<0, 2>([&](auto A) { ld16<C * 2048 + A * 1024>(tF64[C][A], rt, v16, TAB_F64 + g * 4096); }); }); }
      tend<2>();
      if (hf == 0) { tif<0>(); sfor<0, 4>([&](auto I) { const uint32_t s = sh + fblk(IC<2>{}, I) * 8192; ld16c<0>(hc[I][0], rs, v16, s); ld16c<1024>(hc[I][1], rs, v16, s); }); }
      tend<0>();
    }
    Gelu ge; u32x4 o;
    SB();
    sfor<0, 18>([&](auto S) {
      constexpr int G = 18 * ST + S, FT = S & 1;
      const u32x4 a = fr[G & 3];
      if constexpr ((ST & 1) == 0 && S < 2) c0[FT] = mmb(a, x0[KB + (S >> 1)], bi[FT]); else c0[FT] = mmb(a, x0[KB + (S >> 1)], c0[FT]);
      SB();
      if constexpr (S + 3 < 18) fr[(G + 3) & 3] = rd1(ST, IC<S + 3>{});
      else if constexpr (ST != 3 && ST != 9) fr[(G + 3) & 3] = rd1(IC<ST + 1>{}, IC<S + 3 - 18>{});
      SB();
      if constexpr ((ST & 1) == 0 && S < 2) c1[FT] = mmb(a, x1[KB + (S >> 1)], bi[FT]); else c1[FT] = mmb(a, x1[KB + (S >> 1)], c1[FT]);
      SB();
      if constexpr (HF != 1 && S >= 1 && S < 17) { if constexpr (ST & 1) ge.run<S - 1>(p1, o); else ge.run<S - 1>(p0, o); }
      if constexpr (ST == 2) {
        if constexpr (S < 5) dma1(IC<4>{}, S); else if constexpr (S < 10) dma1(IC<5>{}, IC<S - 5>{});
        if constexpr (S == 11) touch(r2, (jj * 324 + (tid < 324 ? tid : 323)) * 128, ks * 1152 * 1152);
        if constexpr (HF == 1 && (S == 12 || S == 15)) wtask(8 * g + (S == 12 ? 0 : 32) + (l >> 3));
      } else if constexpr (S % 3 == 1 && S / 3 < 5) { if constexpr (ST < 7) dma1(IC<ST + 3>{}, IC<S / 3>{}); else dma2(IC<ST - 7>{}, IC<S / 3>{}); }
      if constexpr (HF == 1 && ST == 3 && S == 15) { if (g == 0 || (g == 1 && b >= 192)) wtask(g == 0 ? 64 + (l >> 3) : 72); }
      if constexpr (ST == 9 && S == 15) {
        if (fft_on) { tif<2>(); waitv<9>();
          sfor<0, 2>([&](auto Q) { pin(ur[Q]); *(LAS u32x4*)(cx + (g >> 1) * 4096 + ((l >> 2) & 1) * 2048 + ((l >> 3) & 1) * 1024 + (l & 3) * 256 + (((8 * (g & 1) + 4 * Q + gq) ^ (l & 7)) * 16)) = ur[Q]; }); }
        tend<2>(); }
      if constexpr (HF != 1 && S == 17) { st16c<(ST & 1) * 1024>(o, rs, v16, sh + (9 * fj + (R == 1 ? lone : odd + 2 * (R - 2) + hf)) * 8192); if constexpr (R > 1) hown[R - 2][ST & 1] = o; }
      SB();
    });
    if constexpr (ST & 1) { p0[0] = c0[0]; p0[1] = c0[1]; p1[0] = c1[0]; p1[1] = c1[1]; }
  };
  auto rdx1 = [&](auto S) { x1[S] = *(LAS u32x4*)(smem + ((S & 1) ? axr1 : axr0) + S * 1024); };
  auto fill0 = [&](auto S) {
    if constexpr (S == 6) barl();
    if constexpr (S >= 7 && S < 12) dma1(IC<2>{}, IC<S - 7>{});
    if constexpr (S >= 12 && S < 22 && !(S & 1)) dma1(IC<3>{}, IC<(S - 12) / 2>{}); };
  if (hf == 0) { tif<0>();
    const uint32_t al0 = oq_v(rho * 64 + (gq & 1) * 32 + p4 * 8), al1 = oq_v(al0 ^ 32);
    auto rd = [&](auto S) { return tr2(smem + ((S & 1) ? al1 : al0) + (S >> 1) * 2048, 512); };
    f32x4 bi[2] = {bias(lone, IC<0>{}), bias(lone, IC<1>{})};
    sfor<0, 3>([&](auto S) { fr[S] = rd(S); });
    sfor<0, 18>(rdx1); SB();
    sfor<0, 36>([&](auto S) { const u32x4 a = fr[S & 3];
      p0[S & 1] = mmb(a, x0[S >> 1], S < 2 ? bi[S & 1] : p0[S & 1]); SB();
      if constexpr (S + 3 < 36) fr[(S + 3) & 3] = rd(IC<S + 3>{});
      SB();
      p1[S & 1] = mmb(a, x1[S >> 1], S < 2 ? bi[S & 1] : p1[S & 1]); SB();
      fill0(S); SB(); });
    stage(IC<2>{}, IC<0>{}); stage(IC<3>{}, IC<0>{});
  } else { tif<1>();
    const uint32_t at = oq_v(BUF + v16);
    const float sg = odd ? -1.0f : 1.0f;
    const f32x4 z4 = {0, 0, 0, 0};
    const uint32_t vu = (i16 >> 1) * 8192 + (i16 & 1) * 64 + gq * 16, su = WS_UT + (2 * mt + (g >> 1)) * 256 + (g & 1) * 128;
    f32x4 ua[2][4];
    auto ust = [&](const f32x4 (&u)[4], int tile) { f32x4 a, c;
      sfor<0, 4>([&](auto J) { a[(int)J] = __builtin_fmaf(u[2][(int)J], sg, u[0][(int)J]); c[(int)J] = __builtin_fmaf(u[3][(int)J], sg, u[1][(int)J]); });
      st16c<0>(u32x4{pk_h(a.x, a.y), pk_h(a.z, a.w), pk_h(c.x, c.y), pk_h(c.z, c.w)}, rs, vu, su + tile * 65536); };
    auto rd = [&](auto S) { return *(LAS u32x4*)(smem + at + (S & 1) * 9216 + (S >> 1) * 1024); };
    sfor<0, 2>([&](auto S) { fr[S] = rd(S); });
    sfor<0, 9>([&](auto S) { rdx1(S); rdx1(IC<S + 9>{}); }); SB();
    sfor<0, 18>([&](auto S) { constexpr int I = S >> 1, TT = S & 1; const u32x4 t = fr[S & 3];
      ua[TT][0] = mmb(x0[I], t, I ? ua[TT][0] : z4); SB();
      if constexpr (S + 2 < 18) fr[(S + 2) & 3] = rd(IC<S + 2>{});
      SB();
      ua[TT][2] = mmb(x0[I + 9], t, I ? ua[TT][2] : z4); SB(); fill0(IC<2 * S>{}); SB();
      ua[TT][1] = mmb(x1[I], t, I ? ua[TT][1] : z4); ua[TT][3] = mmb(x1[I + 9], t, I ? ua[TT][3] : z4); SB(); fill0(IC<2 * S + 1>{}); SB(); });
    if (ks == 2) { f32x4 uc[4];
      sfor<0, 9>([&](auto I) { const u32x4 t = *(LAS u32x4*)(smem + at + 18432 + I * 1024);
        uc[0] = mmb(x0[I], t, I ? uc[0] : z4); uc[2] = mmb(x0[I + 9], t, I ? uc[2] : z4); uc[1] = mmb(x1[I], t, I ? uc[1] : z4); uc[3] = mmb(x1[I + 9], t, I ? uc[3] : z4); });
      ust(uc, tileC); }
    ust(ua[0], tileA); ust(ua[1], tileA + 1);
    stage(IC<2>{}, IC<1>{}); stage(IC<3>{}, IC<1>{});
  }
  tend<0>(); tend<1>();
  sfor<4, 10>([&](auto ST) { stage(ST, IC<2>{}); });

  f32x4 oo[9], os[9];
  const uint32_t a2 = (4 * gq + q4) * 288 + p4 * 8;
  uint32_t pb = 0;
  auto blocks = [&](auto NB, auto G0, auto PRE, auto NEXT, auto ZERO, auto BOTH, uint32_t cur, uint32_t nxt, auto&& hfrag, auto&& fill) {
    auto rd = [&](auto S) { return tr2(smem + pb + (S / 9) * 9216 + (S % 9) * 32, 4608); };
    if constexpr (!PRE) { pb = oq_v(a2 + cur); sfor<0, 3>([&](auto S) { fr[(G0 + S) & 3] = rd(S); }); SB(); }
    sfor<0, 9 * NB>([&](auto S) {
      constexpr int G = G0 + S, B = S / 9, DT = S % 9;
      const u32x4 a = fr[G & 3];
      const f32x4 z4 = {0, 0, 0, 0};
      oo[DT] = mmb(a, hfrag(IC<B>{}, IC<0>{}), (ZERO && B == 0) ? z4 : oo[DT]);
      SB();
      if constexpr (S + 3 < 9 * NB) fr[(G + 3) & 3] = rd(IC<S + 3>{});
      else if constexpr (NEXT) { if constexpr (S + 3 == 9 * NB) pb = oq_v(a2 + nxt); fr[(G + 3) & 3] = rd(IC<S + 3 - 9 * NB>{}); }
      SB();
      if constexpr (BOTH) os[DT] = mmb(a, hfrag(IC<B>{}, IC<1>{}), (ZERO && B == 0) ? z4 : os[DT]);
      SB();
      fill(S);
      SB();
    }); };
  using T1 = IC<1>; using F0 = IC<0>;
  const uint32_t sown = sh + (9 * fj + odd + 6 + hf) * 8192;
  barl();
  ST_(13);
  ld8c(pl, rs, vfh, 0);
  if (fft_on) { tif<2>();
    { LAS char* pi = cx + 2048 + gq * 256 + ((i16 ^ (4 + gq)) * 16); LAS char* pr = T == 288 ? pi : cx + gq * 256 + ((i16 ^ gq) * 16);
      sfor<0, 2>([&](auto TT) { sfor<0, 2>([&](auto A) { uu[0][TT][A] = *(LAS u32x4*)(pr + TT * 4096 + A * 1024); uu[1][TT][A] = *(LAS u32x4*)(pi + TT * 4096 + A * 1024); }); }); }
    sfor<0, 2>([&](auto C) { sfor<0, 2>([&](auto A) { ld16<C * 2048 + A * 1024>(tF32[C][A], rt, v16, TAB_F32); }); });
    sfor<0, 2>([&](auto P) { ld16c<P * 1024>(wt[P], rs, v16, WS_WT + T * 8192 + g * 2048); });
    sfor<0, 2>([&](auto C) { sfor<0, 2>([&](auto A) { ld16<C * 2048 + A * 1024>(tG32[C][A], rt, v16, TAB_G32); }); });
    sfor<0, 2>([&](auto C) { sfor<0, 2>([&](auto A) { ld16<C * 2048 + A * 1024>(tG64[C][A], rt, v16, TAB_G64 + g * 4096); }); });
  }
  tend<2>();
  if (hf) { tif<1>();
    Gelu ge, g2;
    sfor<0, 16>([&](auto S) { ge.run<S>(p0, hown[3][0]); g2.run<S>(p1, hown[3][1]); });
    st16c<0>(hown[3][0], rs, v16, sown); st16c<1024>(hown[3][1], rs, v16, sown);
    sfor<0, 5>([&](auto I) { dma2(IC<3>{}, I); });
  }
  tend<1>();
  ST_(14);
  if (fft_on) { tif<2>();
    waitv_lb<28>();
    sfor<0, 2>([&](auto C) { sfor<0, 2>([&](auto A) { pin(tF64[C][A]); }); });
    if (special) sfor<0, 2>([&](auto TT) { sfor<0, 2>([&](auto A) { uu[1][TT][A] = u32x4{0, 0, 0, 0}; }); });
    const f32x4 z4 = {0, 0, 0, 0};
    float br[8], bi[8];
    sfor<0, 2>([&](auto TT) {
      f32x4 P = mmh(uu[0][TT][0], tF64[0][0], z4); P = mmh(uu[0][TT][1], tF64[0][1], P);
      f32x4 Q = mmh(uu[1][TT][0], tF64[1][0], z4); Q = mmh(uu[1][TT][1], tF64[1][1], Q);
      f32x4 I = mmh(uu[0][TT][0], tF64[1][0], z4); I = mmh(uu[0][TT][1], tF64[1][1], I);
      I = mmh(uu[1][TT][0], tF64[0][0], I); I = mmh(uu[1][TT][1], tF64[0][1], I);
      sfor<0, 4>([&](auto J) { const float an = (float)((16 * TT + 4 * gq + J) * (16 * g + i16)) * (1.0f / 2048.0f);
        const float c = __builtin_amdgcn_cosf(an), s = __builtin_amdgcn_sinf(an), re = P[(int)J] - Q[(int)J], im = I[(int)J];
        br[4 * TT + J] = re * c + im * s; bi[4 * TT + J] = im * c - re * s; });
    });
    const u32x4 Br = {pk_h(br[0], br[1]), pk_h(br[2], br[3]), pk_h(br[4], br[5]), pk_h(br[6], br[7])};
    const u32x4 Bi = {pk_h(bi[0], bi[1]), pk_h(bi[2], bi[3]), pk_h(bi[4], bi[5]), pk_h(bi[6], bi[7])};
    float yr[8], yi[8];
    waitv<11>(); if (hf == 0) { tif<0>(); waitv<4>(); } tend<0>();
    sfor<0, 2>([&](auto C) { sfor<0, 2>([&](auto A) { pin(tF32[C][A]); pin(tG32[C][A]); }); pin(wt[C]); });
    sfor<0, 2>([&](auto TT) {
      f32x4 P = mmh(tF32[0][TT], Br, z4), Q = mmh(tF32[1][TT], Bi, z4);
      f32x4 I = mmh(tF32[1][TT], Br, z4); I = mmh(tF32[0][TT], Bi, I);
      sfor<0, 4>([&](auto J) { const unsigned a = wt[TT][J >> 1], c = wt[TT][2 + (J >> 1)];
        const float p = (J & 1) ? hi_f(a) : lo_f(a), q = (J & 1) ? hi_f(c) : lo_f(c), re = P[(int)J] - Q[(int)J], im = I[(int)J];
        yr[4 * TT + J] = re * p - im * q; yi[4 * TT + J] = re * q + im * p; });
    });
    const u32x4 Yr = {pk_bf(yr[0], yr[1]), pk_bf(yr[2], yr[3]), pk_bf(yr[4], yr[5]), pk_bf(yr[6], yr[7])};
    const u32x4 Yi = {pk_bf(yi[0], yi[1]), pk_bf(yi[2], yi[3]), pk_bf(yi[4], yi[5]), pk_bf(yi[6], yi[7])};
    sfor<0, 2>([&](auto TT) {
      f32x4 P = mmb(Yr, tG32[0][TT], z4), Q = mmb(Yi, tG32[1][TT], z4);
      f32x4 I = mmb(Yr, tG32[1][TT], z4); I = mmb(Yi, tG32[0][TT], I);
      float cr[4], ci[4];
      sfor<0, 4>([&](auto J) { const float an = (float)((16 * TT + i16) * (16 * g + 4 * gq + J)) * (1.0f / 2048.0f);
        const float c = __builtin_amdgcn_cosf(an), s = __builtin_amdgcn_sinf(an), re = P[(int)J] - Q[(int)J], im = I[(int)J];
        cr[J] = re * c - im * s; ci[J] = im * c + re * s; });
      LAS char* p = cx + (16 * TT + i16) * 128 + (((2 * g + (gq >> 1)) ^ (i16 >> 1)) * 16) + (gq & 1) * 8;
      *(LAS u32x2*)p = u32x2{pk_bf(cr[0], cr[1]), pk_bf(cr[2], cr[3])};
      *(LAS u32x2*)(p + 4096) = u32x2{pk_bf(ci[0], ci[1]), pk_bf(ci[2], ci[3])};
    });
  }
  tend<2>();
  if (hf == 0) { tif<0>();
    Gelu ge, g2;
    blocks(IC<4>{}, F0{}, F0{}, F0{}, T1{}, T1{}, 2 * BUF, 0, [&](auto B, auto TT) { return hown[B][TT]; },
      [&](auto S) { if constexpr (S < 16) { ge.run<S>(p0, hown[3][0]); g2.run<S>(p1, hown[3][1]); }
        if constexpr (S == 10) { if (!fft_on) bar0(); }
        if constexpr (S == 16) st16c<0>(hown[3][0], rs, v16, sown); if constexpr (S == 17) st16c<1024>(hown[3][1], rs, v16, sown);
        if constexpr (S >= 18 && S < 28 && !(S & 1)) dma2(IC<3>{}, IC<(S - 18) / 2>{}); });
  }
  tend<0>();
  ST_(15);
  waitv_lb<5>();
  ST_(16);
  if (tid == 0) setflag(ws, WS_FH + b * 128, tag + 3);
  check1(pl, 2);
  u32x4 hr[3][2][2];
  auto ldh = [&](auto J, auto K) { const uint32_t s = sh + fblk(J, 2 * hf + K) * 8192; ld16c<0>(hr[J % 3][K][0], rs, vho, s); ld16c<0>(hr[J % 3][K][1], rs, vhs, s); };
  if (fft_on) { tif<2>();
    const f32x4 z4 = {0, 0, 0, 0};
    sfor<0, 2>([&](auto C) { sfor<0, 2>([&](auto A) { pin(tG64[C][A]); }); });
    const uint32_t vz = (i16 >> 1) * 147456 + (i16 & 1) * 64 + (gq & 1) * 32 + (gq >> 1) * 16, sz = WS_ZT + (T == 288 ? 32 : T * 512) + g * 128;
    sfor<0, 2>([&](auto TT) {
      LAS char* p0 = cx + (16 * TT + i16) * 128 + ((gq ^ (i16 >> 1)) * 16); LAS char* p1 = cx + (16 * TT + i16) * 128 + (((gq + 4) ^ (i16 >> 1)) * 16);
      const u32x4 r0 = *(LAS u32x4*)p0, r1_ = *(LAS u32x4*)p1, i0 = *(LAS u32x4*)(p0 + 4096), i1 = *(LAS u32x4*)(p1 + 4096);
      f32x4 P = mmb(tG64[0][0], r0, z4); P = mmb(tG64[0][1], r1_, P);
      f32x4 Q = mmb(tG64[1][0], i0, z4); Q = mmb(tG64[1][1], i1, Q);
      f32x4 I = mmb(tG64[1][0], r0, z4); I = mmb(tG64[1][1], r1_, I); I = mmb(tG64[0][0], i0, I); I = mmb(tG64[0][1], i1, I);
      unsigned ra = pk_bf(P.x - Q.x, P.y - Q.y), rb = pk_bf(P.z - Q.z, P.w - Q.w), ia = pk_bf(I.x, I.y), ib = pk_bf(I.z, I.w);
      auto sa = __builtin_amdgcn_permlane16_swap(ra, ia, false, false); auto sb = __builtin_amdgcn_permlane16_swap(rb, ib, false, false);
      const unsigned sa0 = sa[0], sa1 = sa[1], sb0 = sb[0], sb1 = sb[1];
      const u32x4 o = {sa0, sb0, sa1, sb1};
      if (!(special && (gq & 1))) st16c<0>(o, rs, vz, sz + TT * 8 * 147456);
    });
  }
  tend<2>();
  { auto fill = [&](auto S) {
      if constexpr (S < 10 && !(S & 1)) dma2(IC<4>{}, IC<S / 2>{});
      if constexpr (S == 10 || S == 12) ldh(IC<3>{}, IC<(S - 10) / 2>{});
      if constexpr (S == 20) ld8c(pl, rs, vfh, 0);
      if constexpr (S == 22 || S == 24) ldh(IC<4>{}, IC<(S - 22) / 2>{}); };
    if (hf == 0) { tif<0>(); sfor<0, 4>([&](auto I) { pin(hc[I][0]); pin(hc[I][1]); });
      blocks(IC<4>{}, F0{}, F0{}, F0{}, F0{}, T1{}, 0, 0, [&](auto B, auto TT) { return hc[B][TT]; }, fill); }
    else { tif<1>(); blocks(IC<4>{}, F0{}, F0{}, F0{}, T1{}, T1{}, 3 * BUF, 0, [&](auto B, auto TT) { return hown[B][TT ^ 1]; }, fill); }
    tend<0>(); tend<1>();
  }
  const int zb0 = ks < 2 ? 5 * ks : 4 * ks + 2;
  u32x4 zr[5], xv, bv;
  u32x2 xv8, bv8;
  uint32_t vta = 0, vtb = 0;
  auto dmat = [&](auto I) {
    if constexpr (I == 0) { sw = oq_s(w); auto f = [&](int c) { const int r = c / 18, k = c % 18; return (uint32_t)(r * 288 + (k < 16 ? ((k + 4 * ks) & 15) : k) * 16); }; vta = f(w * 64 + oq_v(l)); vtb = f(512 + oq_v(l)); }
    if constexpr (I < 4) dma(rt, vta, TAB_TI + ((zb0 + I) * 4 + nt) * 9216, 2 * BUF + I * 9216 + sw * 1024);
    else if constexpr (I == 4) dmah(rt, vtb, TAB_TI + ((zb0 + (sw >> 1)) * 4 + nt) * 9216, 2 * BUF + (sw >> 1) * 9216 + 8192, hmask);
    else if (ks < 2) { dma(rt, vta, TAB_TI + ((zb0 + 4) * 4 + nt) * 9216, L_SP + sw * 1024); if (w == 0) dma(rt, vtb, TAB_TI + ((zb0 + 4) * 4 + nt) * 9216, L_SP + 8192); }
  };
  const uint32_t azw = (4 * w + 2 * hh + ((l >> 1) & 1)) * 288 + (((l >> 4) & 1) + 2 * ((l >> 2) & 1)) * 64 + ((l >> 3) & 1) * 32 + (l & 1) * 16;
  const uint32_t srow = sx + hf * 294912 + 288 * nt, vrow = (l >> 2) * 36864;
  sfor<3, 8>([&](auto J) {
    waitv_b<(J == 3 ? 4 : J == 4 ? 9 : J == 5 ? 9 : J == 6 ? 9 : 10)>();
    ST_(14 + J);
    if constexpr (J == 3) { if (tid == 0) setflag(ws, WS_FZ + b * 8, tag); check1(pl, 3); }
    if constexpr (J == 5) { ld16c<0>(pu[0], rs, v16, WS_FZ); ld16c<1024>(pu[1], rs, v16, WS_FZ); }
    if constexpr (J == 6) check4(pu, WS_FZ);
    sfor<0, 2>([&](auto K) { pin(hr[J % 3][K][0]); pin(hr[J % 3][K][1]); });
    SB();
    blocks(IC<2>{}, IC<18 * J>{}, IC<(J > 3)>{}, T1{}, F0{}, T1{}, ((J + 2) & 3) * BUF + hf * 18432, ((J + 3) & 3) * BUF + hf * 18432,
      [&](auto B, auto TT) { return hr[J % 3][B][TT]; },
      [&](auto S) {
        if constexpr (J == 3) { if constexpr (S < 5) dma2(IC<5>{}, S); if constexpr (S == 6 || S == 8) ldh(IC<5>{}, IC<(S - 6) / 2>{}); if constexpr (S >= 10 && S < 15) dma2(IC<6>{}, IC<S - 10>{}); }
        if constexpr (J == 4) { if constexpr (S < 10 && !(S & 1)) dma2(IC<7>{}, IC<S / 2>{}); if constexpr (S == 11 || S == 13) ldh(IC<6>{}, IC<(S - 11) / 2>{}); }
        if constexpr (J == 5) { if constexpr (S < 12 && !(S & 1)) dmat(IC<S / 2>{}); if constexpr (S == 11 || S == 13) ldh(IC<7>{}, IC<(S - 11) / 2>{}); }
        if constexpr (J == 6) { if constexpr (S < 5) ld16c<0>(zr[S], rs, v16, WS_ZT + mt * 147456 + (zb0 + (S == 4 && ks >= 2 ? 0 : (int)S)) * 8192 + w * 1024);
          if constexpr (S >= 5 && S < 15 && (S & 1)) dma2(IC<9>{}, IC<(S - 5) / 2>{}); }
        if constexpr (J == 7) { if constexpr (S == 0 || S == 2) ldh(IC<9>{}, IC<S / 2>{});
          if constexpr (S == 8) waitv<9>();
          if constexpr (S >= 8 && S < 12) { pin(zr[S - 8]); *(LAS u32x4*)(smem + azw + (S - 8) * 9216) = zr[S - 8]; }
          if constexpr (S == 13) { ld16<0>(xv, rx, vrow + (l & 3) * 16, srow + 64 * ks); ld8<256>(xv8, rx, vrow + (l & 3) * 8, srow); }
          if constexpr (S == 15) { const u32x4 rb = mksrd(b2, 1152); ld16<0>(bv, rb, (l & 3) * 16, 288 * nt + 64 * ks); ld8<256>(bv8, rb, (l & 3) * 8, 288 * nt); } }
      });
  });
  { waitv_lb<8>();
    ST_(22);
    pin(zr[4]); *(LAS u32x4*)(smem + BUF + azw) = zr[4];
    const uint32_t az = oq_v(a2 + hf * 18432 + g * 64);
    u32x4 z[2][2];
    sfor<0, 2>([&](auto K) { z[K][0] = tr2(smem + az + K * 9216 + hf * 32, 4608); z[K][1] = tr2(smem + az + K * 9216 + (hf ^ 1) * 32, 4608); });
    SB();
    blocks(IC<2>{}, IC<144>{}, T1{}, F0{}, F0{}, T1{}, 0, 0, [&](auto B, auto TT) { return z[B][TT]; }, [&](auto S) {});
  }
  { waitv_lb<4>();
    ST_(23);
    if (ks < 2) { const u32x4 z = tr2(smem + BUF + a2 + g * 64 + hf * 32, 4608);
      blocks(IC<1>{}, F0{}, F0{}, F0{}, F0{}, F0{}, L_SP, 0, [&](auto B, auto TT) { return z; }, [&](auto S) {}); }
    sfor<0, 2>([&](auto K) { pin(hr[0][K][0]); pin(hr[0][K][1]); });
    const uint32_t aw = oq_v((hf ? 0 : 2 * BUF) + g * 9216 + v16);
    blocks(IC<2>{}, F0{}, F0{}, F0{}, F0{}, T1{}, 3 * BUF + hf * 18432, 0, [&](auto B, auto TT) { return hr[0][B][TT]; },
      [&](auto S) { if constexpr (S >= 10) *(LAS f32x4*)(smem + aw + (S - 10) * 1024) = os[S - 10]; });
    *(LAS f32x4*)(smem + aw + 8192) = os[8];
    barl();
    ST_(24);
    const uint32_t ar = oq_v((hf ? 2 * BUF : 0) + g * 9216 + v16);
    sfor<0, 9>([&](auto DT) { oo[(DT + 2) % 9] += *(LAS f32x4*)(smem + ar + ((DT + 2) % 9) * 1024); });
  }
  const uint32_t sp = WS_PART + (mt * 4 + nt) * 294912 + w * 3072;
  sfor<1, 4>([&](auto K) { const uint32_t s = sp + (((ks + K) & 3) * 3 + K - 1) * 24576;
    st16c<0>(__builtin_bit_cast(u32x4, oo[2 * K]), rs, v16, s); st16c<1024>(__builtin_bit_cast(u32x4, oo[2 * K + 1]), rs, v16, s); });
  if (g != ks) st16c<2048>(__builtin_bit_cast(u32x4, oo[8]), rs, v16, sp + (g * 3 + ((g - ks) & 3) - 1) * 24576);
  waitv_b<0>();
  ST_(25);
  if (tid == 0) setflag(ws, WS_FP + b * 128, tag);
  spin<1>(ws, WS_FP + ((b & ~6) + gq * 2) * 128, tag, 0);
  ST_(26);
  { u32x4 pp[3][3];
    sfor<0, 3>([&](auto K) { const uint32_t s = sp + (ks * 3 + K) * 24576; ld16c<0>(pp[K][0], rs, v16, s); ld16c<1024>(pp[K][1], rs, v16, s); ld16c<2048>(pp[K][2], rs, v16, s); });
    waitv<0>();
    LAS char* pt = smem + BUF + w * 3072;
    sfor<0, 3>([&](auto Q) { f32x4 r = oo[Q < 2 ? (int)Q : 8];
      sfor<0, 3>([&](auto K) { pin(pp[K][Q]); r += __builtin_bit_cast(f32x4, pp[K][Q]); });
      *(LAS f32x4*)(pt + Q * 1024 + i16 * 64 + gq * 16) = r; });
    pin(xv); pin(bv); pin(xv8); pin(bv8);
    LAS char* pq = pt + ((l >> 1) & 1) * 1024 + (l >> 2) * 64 + (l & 1) * 32;
    const f32x4 ra = *(LAS f32x4*)pq, rb = *(LAS f32x4*)(pq + 16), rc = *(LAS f32x4*)(pt + 2048 + v16);
    auto fin = [&](float a, float c, unsigned xx, unsigned bb) { return pk_bf(a + lo_f(xx) + lo_f(bb), c + hi_f(xx) + hi_f(bb)); };
    char* op = out + srow + vrow;
    *(u32x4*)(op + 64 * ks + (l & 3) * 16) = u32x4{fin(ra.x, ra.y, xv.x, bv.x), fin(ra.z, ra.w, xv.y, bv.y), fin(rb.x, rb.y, xv.z, bv.z), fin(rb.z, rb.w, xv.w, bv.w)};
    if (g == ks) *(u32x2*)(op + 256 + (l & 3) * 8) = u32x2{fin(rc.x, rc.y, xv8.x, bv8.x), fin(rc.z, rc.w, xv8.y, bv8.y)};
  }
  if (b == 0 && tid == 0) setflag(ws, WS_EPOCH, tag);
}

extern "C" void kernel_launch(void* const* d_in, const int* in_sizes, int n_in, void* d_out, int out_size, void* d_ws, size_t ws_size, hipStream_t stream) {
  (void)hipFuncSetAttribute((const void*)fused_kernel, hipFuncAttributeMaxDynamicSharedMemorySize, LDS_BYTES);
  fused_kernel<<<256, 512, LDS_BYTES, stream>>>((const char*)d_in[0], (const char*)d_in[1], (const char*)d_in[2], (const char*)d_in[4], (const char*)d_in[5],
                                                (const char*)d_in[6], (const char*)d_in[7], (char*)d_out, (char*)d_ws);
}
```

```cpp
#include <hip/hip_runtime.h>
#include <stdint.h>
#include <type_traits>
#define NB2 2

typedef unsigned short u16;
typedef short s16x4 __attribute__((ext_vector_type(4)));
typedef short s16x8 __attribute__((ext_vector_type(8)));
typedef __bf16 bf16x8 __attribute__((ext_vector_type(8)));
typedef __bf16 bf16x2 __attribute__((ext_vector_type(2)));
typedef _Float16 f16x8 __attribute__((ext_vector_type(8)));
typedef _Float16 f16x2 __attribute__((ext_vector_type(2)));
typedef float f32x4 __attribute__((ext_vector_type(4)));
typedef float f32x2 __attribute__((ext_vector_type(2)));
typedef unsigned u32x4 __attribute__((ext_vector_type(4)));
typedef unsigned u32x2 __attribute__((ext_vector_type(2)));
typedef unsigned long long u64;
#define LAS __attribute__((address_space(3)))
#define GAS __attribute__((address_space(1)))
#define DI __device__ __forceinline__

namespace tab {
constexpr double PI = 3.14159265358979323846;
constexpr double ccos(int a, int n) {
  a %= n; if (a < 0) a += n;
  if (2 * a > n) a = n - a;
  bool neg = false;
  if (4 * a > n) { a = n / 2 - a; neg = true; }
  if (4 * a == n) return 0.0;
  double x = 2 * PI * a / n, t = 1, s = 1;
  for (int i = 1; i <= 13; i++) { t *= -x * x / ((2 * i - 1) * (2 * i)); s += t; }
  return neg ? -s : s;
}
constexpr u16 to_bf(double v) { float f = (float)v; uint32_t u = __builtin_bit_cast(uint32_t, f); u += 0x7fffu + ((u >> 16) & 1u); return (u16)(u >> 16); }
constexpr u16 to_h(double v) { _Float16 h = (_Float16)v; return __builtin_bit_cast(u16, h); }
struct C576 { u16 b1[576]; u16 b2[576]; };
constexpr C576 gen576() { C576 r{}; for (int i = 0; i < 576; i++) { double c = ccos(i, 576) / 24.0; r.b1[i] = to_bf(c); r.b2[i] = to_bf(2 * c); } return r; }
constexpr C576 c576 = gen576();
constexpr int colk(int c) { return c == 0 ? 0 : c == 1 ? 288 : c < 288 ? (c & ~1) : ((c - 288) & ~1) + 1; }
constexpr int colp(int c) { return c < 2 ? 0 : (c & 1); }
struct Chunk { u16 v[9216]; };
constexpr Chunk genTF(int u) {
  Chunk r{};
  for (int i = 0; i < 1152; i++) { int t = i / 576, s = (i % 576) >> 6, l = i & 63, c = 32 * u + 16 * t + (l & 15), k = colk(c), o = 144 * colp(c), d = 32 * s + 8 * (l >> 4);
    for (int j = 0; j < 8; j++) r.v[i * 8 + j] = c576.b1[((d + j) * k + o) % 576]; }
  return r;
}
constexpr Chunk genTI(int q) {
  Chunk r{};
  for (int i = 0; i < 64; i++) { int c = 32 * (q >> 1) + (i & 31), k = colk(c), o = 144 * colp(c), d0 = 144 * (2 * (q & 1) + (i >> 5));
    if (c < 2) { for (int d = 0; d < 144; d++) r.v[i * 144 + d] = c576.b1[((d0 + d) * k + o) % 576]; }
    else { for (int d = 0; d < 144; d++) r.v[i * 144 + d] = c576.b2[((d0 + d) * k + o) % 576]; } }
  return r;
}
template <int I> constexpr Chunk cTF = genTF(I);
template <int I> constexpr Chunk cTI = genTI(I);
struct Small { u16 f64[4][2][2][512]; u16 f32[2][2][512]; u16 g32[2][2][512]; u16 g64[4][2][2][512]; };
constexpr Small genSmall() {
  Small r{};
  double c64[64] = {}; for (int i = 0; i < 64; i++) c64[i] = ccos(i, 64);
  const double r32 = 0.17677669529663688110;
  for (int l = 0; l < 64; l++) for (int j = 0; j < 8; j++) {
    int rr = l & 15, gq = l >> 4;
    int p2 = 16 * (j >> 2) + 4 * gq + (j & 3);
    for (int w = 0; w < 4; w++) for (int a = 0; a < 2; a++) {
      int e = (16 * w + rr) * (32 * a + 8 * gq + j), e1 = (16 * w + rr) * (32 * a + p2);
      r.f64[w][0][a][l * 8 + j] = to_h(c64[e1 & 63] / 8); r.f64[w][1][a][l * 8 + j] = to_h(c64[(e1 + 16) & 63] / 8);
      r.g64[w][0][a][l * 8 + j] = to_bf(c64[e & 63] / 8); r.g64[w][1][a][l * 8 + j] = to_bf(c64[(e + 48) & 63] / 8);
    }
    for (int t = 0; t < 2; t++) {
      int e = 2 * ((16 * t + rr) * p2);
      r.f32[0][t][l * 8 + j] = to_h(c64[e & 63] * r32); r.f32[1][t][l * 8 + j] = to_h(c64[(e + 16) & 63] * r32);
      r.g32[0][t][l * 8 + j] = to_bf(c64[e & 63] * r32); r.g32[1][t][l * 8 + j] = to_bf(c64[(e + 48) & 63] * r32);
    }
  }
  return r;
}
struct alignas(256) All { Chunk tf[18]; Chunk ti[36]; Small sm; };
template <int... I, int... J> constexpr All mk(std::integer_sequence<int, I...>, std::integer_sequence<int, J...>) { return All{{cTF<I>...}, {cTI<J>...}, genSmall()}; }
}
static __device__ const tab::All g_tab = tab::mk(std::make_integer_sequence<int, 18>{}, std::make_integer_sequence<int, 36>{});

constexpr uint32_t BUF = 36864, L_SP = 147456, L_B1 = L_SP, L_JUNK = L_SP + 2048, L_W = L_SP + 4096, LDS_BYTES = 163840;
constexpr uint32_t WS_EPOCH = 0, WS_FU = 4096, WS_FZ = 8192, WS_FH = 16384;
constexpr uint32_t WS_H = 1u << 20, WS_UT = 20u << 20, WS_WT = 23u << 20, WS_ZT = 26u << 20, WS_PART = 29u << 20, WS_END = 48u << 20;
constexpr uint32_t TAB_TF = 0, TAB_TI = 18 * 18432, TAB_F64 = 54 * 18432, TAB_F32 = TAB_F64 + 16384, TAB_G32 = TAB_F32 + 4096, TAB_G64 = TAB_G32 + 4096, TAB_END = TAB_G64 + 16384;

template <int I, int N, class Fn> DI void sfor(Fn&& f) { if constexpr (I < N) { f(std::integral_constant<int, I>{}); sfor<I + 1, N>(f); } }
template <int V> using IC = std::integral_constant<int, V>;

DI u32x4 mksrd(const void* p, uint32_t n) { return u32x4{(unsigned)(size_t)p, (unsigned)((size_t)p >> 32) & 0xffffu, n, 0x00020000u}; }
template <int IMM> DI void ld16(u32x4& d, u32x4 srd, uint32_t voff, uint32_t soff) { asm volatile("buffer_load_dwordx4 %0, %1, %2, %3 offen offset:%4" : "=v"(d) : "v"(voff), "s"(srd), "s"(soff), "i"(IMM) : "memory"); }
template <int IMM> DI void ld16c(u32x4& d, u32x4 srd, uint32_t voff, uint32_t soff) { asm volatile("buffer_load_dwordx4 %0, %1, %2, %3 offen offset:%4 sc1" : "=v"(d) : "v"(voff), "s"(srd), "s"(soff), "i"(IMM) : "memory"); }
template <int IMM> DI void ld8(u32x2& d, u32x4 srd, uint32_t voff, uint32_t soff) { asm volatile("buffer_load_dwordx2 %0, %1, %2, %3 offen offset:%4" : "=v"(d) : "v"(voff), "s"(srd), "s"(soff), "i"(IMM) : "memory"); }
DI void ld8c(u32x2& d, u32x4 srd, uint32_t voff, uint32_t soff) { asm volatile("buffer_load_dwordx2 %0, %1, %2, %3 offen sc1" : "=v"(d) : "v"(voff), "s"(srd), "s"(soff) : "memory"); }
DI void ld4(unsigned& d, u32x4 srd, uint32_t voff, uint32_t soff) { asm volatile("buffer_load_dword %0, %1, %2, %3 offen" : "=v"(d) : "v"(voff), "s"(srd), "s"(soff) : "memory"); }
DI void dma(u32x4 srd, uint32_t voff, uint32_t soff, uint32_t lds) { asm volatile("s_mov_b32 m0, %3\n\ts_nop 0\n\tbuffer_load_dwordx4 %0, %1, %2 offen lds" :: "v"(voff), "s"(srd), "s"(soff), "s"(lds) : "memory"); }
DI void dmah(u32x4 srd, uint32_t voff, uint32_t soff, uint32_t lds, u64 mask) {
  asm volatile("s_mov_b32 m0, %3\n\ts_mov_b64 exec, %4\n\tbuffer_load_dwordx4 %0, %1, %2 offen lds\n\ts_mov_b64 exec, -1" :: "v"(voff), "s"(srd), "s"(soff), "s"(lds), "s"(mask) : "memory"); }
DI void touch(u32x4 srd, uint32_t voff, uint32_t soff, u64 mask) {
  asm volatile("s_mov_b32 m0, %3\n\ts_mov_b64 exec, %4\n\tbuffer_load_dword %0, %1, %2 offen lds\n\ts_mov_b64 exec, -1" :: "v"(voff), "s"(srd), "s"(soff), "s"(L_JUNK), "s"(mask) : "memory"); }
DI void code_touch(int part, int l) {
  u64 pc; uint32_t n;
  asm volatile("s_getpc_b64 %0\n.Lfk_begin:\n\ts_mov_b32 %1, .Lfk_end-.Lfk_begin" : "=s"(pc), "=s"(n));
  touch(u32x4{(unsigned)pc, (unsigned)(pc >> 32) & 0xffffu, n, 0x00020000u}, l * 128, part * 2048, 0xffffull); }
template <int IMM> DI void st16c(u32x4 d, u32x4 srd, uint32_t voff, uint32_t soff) {
  asm volatile("buffer_store_dwordx4 %0, %1, %2, %3 offen offset:%4 sc1\n\ts_nop 1" :: "v"(d), "v"(voff), "s"(srd), "s"(soff), "i"(IMM) : "memory"); }
DI void st8c(u32x2 d, u32x4 srd, uint32_t voff, uint32_t soff) {
  asm volatile("buffer_store_dwordx2 %0, %1, %2, %3 offen sc1\n\ts_nop 1" :: "v"(d), "v"(voff), "s"(srd), "s"(soff) : "memory"); }
template <int N> DI void waitv() { asm volatile("s_waitcnt vmcnt(%0)" :: "i"(N) : "memory"); }
template <int N> DI void waitv_b() { asm volatile("s_waitcnt vmcnt(%0)\n\ts_barrier" :: "i"(N) : "memory"); }
template <int N> DI void waitv_lb() { asm volatile("s_waitcnt vmcnt(%0) lgkmcnt(0)\n\ts_barrier" :: "i"(N) : "memory"); }
DI void bar0() { asm volatile("s_barrier" ::: "memory"); }
DI void barl() { asm volatile("s_waitcnt lgkmcnt(0)\n\ts_barrier" ::: "memory"); }
DI void pin(u32x4& v) { asm volatile("" : "+v"(v)); }
DI void pin(u32x2& v) { asm volatile("" : "+v"(v)); }
DI void pin(unsigned& v) { asm volatile("" : "+v"(v)); }
DI uint32_t oq_s(uint32_t v) { asm volatile("" : "+s"(v)); return v; }
DI uint32_t oq_v(uint32_t v) { asm volatile("" : "+v"(v)); return v; }
#define SB() __builtin_amdgcn_sched_barrier(0)
template <int C> DI void tif() { asm volatile("; @IF %0" :: "i"(C)); }
template <int C> DI void tend() { asm volatile("; @END %0" :: "i"(C)); }

DI float asf(unsigned u) { return __builtin_bit_cast(float, u); }
DI unsigned asu(float f) { return __builtin_bit_cast(unsigned, f); }
DI unsigned pk_bf(float a, float b) { f32x2 p = {a, b}; bf16x2 r = __builtin_convertvector(p, bf16x2); return __builtin_bit_cast(unsigned, r); }
DI unsigned pk_h(float a, float b) { f32x2 p = {a, b}; f16x2 r = __builtin_convertvector(p, f16x2); return __builtin_bit_cast(unsigned, r); }
DI float bfr(float a) { return asf(pk_bf(a, a) << 16); }
DI float lo_f(unsigned u) { return asf(u << 16); }
DI float hi_f(unsigned u) { return asf(u & 0xffff0000u); }
DI float dot2(unsigned a, unsigned b, float c) { return __builtin_amdgcn_fdot2_f32_bf16(__builtin_bit_cast(bf16x2, a), __builtin_bit_cast(bf16x2, b), c, false); }
DI float vfma(float a, float b, float c) { float d; asm("v_fma_f32 %0, %1, %2, %3" : "=v"(d) : "v"(a), "v"(b), "v"(c)); return d; }
template <int CTRL> DI float dpp_add(float v) { return v + asf(__builtin_amdgcn_update_dpp(0, asu(v), CTRL, 0xf, 0xf, true)); }
DI float sum8(float v) { return dpp_add<0x141>(dpp_add<0x4E>(dpp_add<0xB1>(v))); }
DI f32x4 mmb(u32x4 a, u32x4 b, f32x4 c) { return __builtin_amdgcn_mfma_f32_16x16x32_bf16(__builtin_bit_cast(bf16x8, a), __builtin_bit_cast(bf16x8, b), c, 0, 0, 0); }
DI f32x4 mmh(u32x4 a, u32x4 b, f32x4 c) { return __builtin_amdgcn_mfma_f32_16x16x32_f16(__builtin_bit_cast(f16x8, a), __builtin_bit_cast(f16x8, b), c, 0, 0, 0); }
DI u32x4 tr2(LAS char* p, int second) {
  s16x4 a = __builtin_amdgcn_ds_read_tr16_b64_v4i16((LAS s16x4*)p);
  s16x4 b = __builtin_amdgcn_ds_read_tr16_b64_v4i16((LAS s16x4*)(p + second));
  s16x8 c = __builtin_shufflevector(a, b, 0, 1, 2, 3, 4, 5, 6, 7);
  return __builtin_bit_cast(u32x4, c);
}
struct Gelu { float a0, a1;
  template <int Q> DI void step(float v0, float v1, unsigned& o) {
    if constexpr (Q == 0) { a0 = __builtin_fmaf(v0 * v0, -0.10294324f, -2.3022082f); a1 = __builtin_fmaf(v1 * v1, -0.10294324f, -2.3022082f); }
    if constexpr (Q == 1) { a0 = __builtin_amdgcn_exp2f(v0 * a0); a1 = __builtin_amdgcn_exp2f(v1 * a1); }
    if constexpr (Q == 2) { a0 = __builtin_amdgcn_rcpf(1.0f + a0); a1 = __builtin_amdgcn_rcpf(1.0f + a1); }
    if constexpr (Q == 3) { o = pk_bf(v0 * a0, __builtin_fmaf(v1, a1, 0.0f)); }
  }
  template <int K> DI void run(const f32x4 (&p)[2], u32x4& o) {
    constexpr int P = K >> 2; const float v0 = p[P >> 1][2 * (P & 1)], v1 = p[P >> 1][2 * (P & 1) + 1];
    unsigned t = 0; step<K & 3>(v0, v1, t); if constexpr ((K & 3) == 3) o[P] = t; }
};
#define ST_(i) do {} while (0)
#define RLX_AGENT __ATOMIC_RELAXED, __HIP_MEMORY_SCOPE_AGENT
DI bool lvl_ok(unsigned lo, unsigned hi, u64 tag, unsigned lvl) { return (hi == (unsigned)(tag >> 32)) & (lo - (unsigned)tag - lvl <= 7u - lvl); }
template <int N> DI void spin(const char* base, uint32_t off, u64 tag, unsigned lvl) {
#pragma nounroll
  for (unsigned spins = 0; spins < (1u << 17); ++spins) {
    bool ok = true;
#pragma unroll
    for (int k = 0; k < N; ++k) { u64 v = __hip_atomic_load((GAS u64*)(base + off + (k & 1) * 8 + (k >> 1) * 1024), RLX_AGENT); ok &= lvl_ok((unsigned)v, (unsigned)(v >> 32), tag, lvl); }
    if (__all(ok)) break;
    __builtin_amdgcn_s_sleep(1);
  }
}
DI void setflag(char* ws, uint32_t off, u64 v) { __hip_atomic_store((GAS u64*)(ws + off), v, RLX_AGENT); }

__global__ __launch_bounds__(512, 2) void fused_kernel(const char* __restrict__ x, const char* __restrict__ wr, const char* __restrict__ wi,
    const char* __restrict__ w1, const char* __restrict__ b1, const char* __restrict__ w2, const char* __restrict__ b2, char* out, char* ws) {
  extern __shared__ __attribute__((aligned(16))) char smem_[];
  LAS char* smem = (LAS char*)smem_;
  const int tid = threadIdx.x, l = tid & 63;
  const int w = __builtin_amdgcn_readfirstlane(tid >> 6), g = w & 3, hf = w >> 2;
  const int b = blockIdx.x, xcd = b & 7, jj = b >> 3;
  const int ks = xcd >> 1, mt = (xcd & 1) * 8 + (jj >> 2), nt = jj & 3, fj = ks * 4 + nt;
  const u32x4 rs = mksrd(ws, WS_END), rx = mksrd(x, 2048 * 1152), r1 = mksrd(w1, 576 * 9216), r2 = mksrd(w2, 4608 * 1152), rt = mksrd(&g_tab, TAB_END);
  const int i16 = l & 15, q4 = i16 >> 2, p4 = i16 & 3, hh = l >> 5, gq = l >> 4;
  const u64 hmask = (w & 1) ? 0xffffffff00000000ull : 0xffffffffull;
  const uint32_t v16 = l * 16;
  asm volatile("" :: "s"(ws), "s"(out), "s"(b1), "s"(b2), "s"(w2), "s"(wr), "s"(wi));
  ST_(0);
  u32x4 x0[18], x1[18];
  const uint32_t vx = (l >> 3) * 36864 + (l & 7) * 16;
  const uint32_t sx = (uint32_t)((32 * (g & 1) + 16 * hf) * 32 + 2 * mt + (g >> 1)) * 1152;
  const int odd = fj & 1, lone = odd ? 0 : 8;
  const int tileA = 18 * odd + 2 * (fj >> 1), tileC = 18 * odd + 16 + (nt >> 1);
  u32x2 ep; unsigned b1v;
  const uint32_t vo1 = (8 * hh + ((l >> 3) & 3)) * 9216 + (((l & 7) ^ (((l >> 4) & 3) << 1)) * 16);
  uint32_t sw = 0;
  auto dma1 = [&](auto ST, auto I, int wv) {
    if constexpr (I == 0) sw = oq_s(wv);
    const uint32_t so1 = (288 * (ST & 1)) * 9216 + (288 * fj + 32 * (odd + 2 * ((ST >> 1) - 1))) * 2;
    constexpr uint32_t lb = (ST & 3) * BUF;
    if constexpr (I < 4) dma(r1, vo1, so1 + (32 * sw + 16 * (I >> 1) + 4 * (I & 1)) * 9216, lb + (4 * sw + I) * 1024);
    else dmah(r1, vo1, so1 + (256 + 16 * (sw >> 2) + 4 * ((sw >> 1) & 1)) * 9216, lb + (32 + (sw >> 1)) * 1024, hmask);
  };
  const uint32_t axb = 2 * BUF + g * 18432;
  const uint32_t axw = axb + ((l >> 2) & 1) * 1024 + (l & 3) * 256 + (((l >> 3) ^ (l & 7)) * 16);
  const uint32_t axr0 = axb + gq * 256 + ((i16 ^ gq) * 16), axr1 = axr0 ^ 64;
  auto rdx = [&](u32x4 (&d)[18], auto S) { d[S] = *(LAS u32x4*)(smem + ((S & 1) ? axr1 : axr0) + S * 1024); };
  u32x4 xr[2][9];
  f32x2 rn2[2], mb2[2];
  auto ldx = [&]() { sfor<0, 18>([&](auto C) { ld16<(C % 9) * 128>(xr[C / 9][C % 9], rx, vx, sx + (C / 9) * 294912); }); ld8c(ep, rs, 0, WS_EPOCH); };
  auto stats = [&](auto RB, auto NV, auto&& fill) {
    float sa[4] = {}, qa[4] = {};
    sfor<0, 9>([&](auto C) { fill(C);
      waitv<NV + 17 - 9 * RB - C>(); pin(xr[RB][C]);
      sfor<0, 4>([&](auto D) { const unsigned u = xr[RB][C][(int)D]; sa[D] = dot2(u, 0x3f803f80u, sa[D]); qa[D] = dot2(u, u, qa[D]); });
      SB(); });
    rn2[RB] = f32x2{(sa[0] + sa[1]) + (sa[2] + sa[3]), (qa[0] + qa[1]) + (qa[2] + qa[3])}; };
  auto chain = [&](auto RB) {
    const float mean = sum8(rn2[RB].x) * (1.0f / 576.0f);
    const float var = fmaxf(sum8(rn2[RB].y) * (1.0f / 576.0f) - mean * mean, 0.0f);
    const float sd = bfr(__builtin_amdgcn_sqrtf(bfr(bfr(var) + 1e-5f)));
    const float rn = __builtin_amdgcn_rcpf(sd), mb = -bfr(mean) * rn;
    rn2[RB] = f32x2{rn, rn}; mb2[RB] = f32x2{mb, mb}; };
  auto put = [&](auto RB, auto C) { *(LAS u32x4*)(smem + axw + RB * 128 + C * 2048) = xr[RB][C]; };
  auto norm = [&](auto RB, auto&& fill) {
    sfor<0, 9>([&](auto C) { f32x2 f[4];
      sfor<0, 4>([&](auto D) { const unsigned u = xr[RB][C][(int)D]; f[D] = f32x2{lo_f(u), hi_f(u)}; });
      SB();
      sfor<0, 4>([&](auto D) { f[D] = __builtin_elementwise_fma(f[D], rn2[RB], mb2[RB]); });
      SB();
      xr[RB][C] = u32x4{pk_bf(f[0].x, f[0].y), pk_bf(f[1].x, f[1].y), pk_bf(f[2].x, f[2].y), pk_bf(f[3].x, f[3].y)};
      fill(C);
      SB(); }); };
  using T1 = IC<1>; using F0 = IC<0>;
  u64 tag;
  auto mktag = [&]() { pin(ep);
    tag = ((u64)(unsigned)__builtin_amdgcn_readfirstlane(ep.y) << 32) | (unsigned)__builtin_amdgcn_readfirstlane(ep.x);
    tag += 0x9E3779B97F4A7C15ull; tag = (tag ^ (tag >> 30)) * 0xBF58476D1CE4E5B9ull; tag = (tag ^ (tag >> 27)) * 0x94D049BB133111EBull; tag = (tag ^ (tag >> 31)) & ~7ull; };
  f32x4 c0[2], c1[2], p0[2], p1[2];
  auto bias = [&](int unit, auto FT) { return *(LAS f32x4*)(smem + L_B1 + (32 * unit + 16 * FT + 4 * gq) * 4); };
  const int rho = 16 * (gq >> 1) + 4 * (gq & 1) + q4;
  uint32_t vo2a, vo2b;
  { auto f = [&](int c, int stride) { const int r = c / 18, k = c % 18; return (uint32_t)(r * stride + (k < 16 ? ((k + 4 * ks) & 15) : k) * 16); };
    vo2a = f(w * 64 + l, 1152); vo2b = f(512 + l, 1152); }
  auto fblk = [&](auto J, int i) {
    int pn, u;
    if constexpr (J < 2) { pn = nt; u = odd + 2 * i + J; }
    else if constexpr (J == 9) { pn = (nt + i) & 3; u = (pn & 1) ? 0 : 8; }
    else { const int q = 4 * (J - 2) + i; const int r = q / 6, m = q - 6 * r; pn = (nt + 1 + (m >> 1)) & 3; u = (pn & 1) + 2 * r + (m & 1); }
    return __builtin_amdgcn_readfirstlane(36 * ks + 9 * pn + u); };
  auto dma2 = [&](auto J, auto I) {
    if constexpr (I == 0) sw = oq_s(w);
    constexpr uint32_t lb = ((J + 2) & 3) * BUF;
    if constexpr (I < 4) dma(r2, vo2a, fblk(J, I) * 36864 + 288 * nt, lb + I * 9216 + sw * 1024);
    else dmah(r2, vo2b, fblk(J, sw >> 1) * 36864 + 288 * nt, lb + (sw >> 1) * 9216 + 8192, hmask);
  };
  const uint32_t sh = WS_H + (mt * 144 * 4 + g) * 2048;
  const uint32_t vho = v16 + hf * 1024, vhs = v16 + (hf ^ 1) * 1024;
  const uint32_t vfh = WS_FH + ((b & ~24) + gq * 8) * 128;
  auto check1 = [&](u32x2& p, unsigned lvl) { pin(p); if (!__all(lvl_ok(p.x, p.y, tag, lvl))) spin<1>(ws, vfh, tag, lvl); };
  auto check4 = [&](u32x4 (&p)[2], uint32_t base) { pin(p[0]); pin(p[1]);
    if (!__all(lvl_ok(p[0].x, p[0].y, tag, 0) & lvl_ok(p[0].z, p[0].w, tag, 0) & lvl_ok(p[1].x, p[1].y, tag, 0) & lvl_ok(p[1].z, p[1].w, tag, 0))) spin<4>(ws, base + v16, tag, 0); };
  const int T = hf ? b : 256 + (xcd & 1) * 32 + jj;
  const bool fft_on = hf || (ks == 3 && T <= 288);
  const bool special = T == 0 || T == 288;
  u32x4 tF64[2][2], tF32[2][2], tG32[2][2], tG64[2][2], uu[2][2][2], ur[2], wt[2], pu[2], hc[4][2], hown[4][2];
  u32x2 pl;

  const uint32_t a1 = rho * 128 + (((2 * hf) ^ ((rho >> 1) & 3)) * 32) + p4 * 8;
  uint32_t pa0 = 0, pa1 = 0;
  u32x4 fr[4];
  auto rd1 = [&](auto ST, auto S) {
    if constexpr (S == 0) { pa0 = oq_v(a1 + (ST & 3) * BUF); pa1 = oq_v(pa0 ^ 32); }
    return tr2(smem + ((S & 1) ? pa1 : pa0) + (S >> 1) * 4096, 1024); };
  LAS char* cx = smem + L_SP + hf * 8192;
  auto wtask = [&](int cl) {
    LAS char* p = smem + L_W + (l & 7) * 656 + (l & 1) * 2 + cl * 2 + (b >= 192 ? 12 : 0); unsigned v[8];
    sfor<0, 8>([&](auto J) { v[J] = *(LAS u16*)(p + (J >> 2) * 5248 + (J & 3) * 160); });
    const int kc = 72 * (b >> 6) + cl;
    st16c<0>(u32x4{v[0] | (v[1] << 16), v[2] | (v[3] << 16), v[4] | (v[5] << 16), v[6] | (v[7] << 16)}, rs,
             (uint32_t)(kc == 288 ? 288 : (kc & 1) ? 144 + (kc >> 1) : (kc >> 1)) * 8192 + (l & 7) * 16,
             WS_WT + ((b >> 1) & 3) * 2048 + ((b >> 5) & 1) * 1024 + ((b >> 3) & 3) * 256 + (b & 1) * 128); };
  auto stage = [&](auto ST, auto HF) {
    constexpr int R = ST >> 1, KB = 9 * (ST & 1);
    waitv_b<(ST == 2 ? 8 + 2 * HF : ST == 3 ? 12 + HF : ST == 4 ? 8 : ST == 5 || ST == 9 ? 6 : 7)>();
    ST_(3 + ST);
    if constexpr (ST == 5) { if (tid == 0) setflag(ws, WS_FU + b * 8, tag); }
    if constexpr (ST == 7 || ST == 9) { if (tid == 0) setflag(ws, WS_FH + b * 128, tag + (ST - 5) / 2); }
    if constexpr (ST <= 4) sfor<0, 3>([&](auto S) { fr[(18 * ST + S) & 3] = rd1(ST, S); });
    f32x4 bi[2];
    if constexpr ((ST & 1) == 0) { const int u = odd + 2 * (R - 1) + hf; bi[0] = bias(u, IC<0>{}); bi[1] = bias(u, IC<1>{}); }
    if constexpr (ST == 8) { ld8c(pl, rs, vfh, 0); ld16c<0>(pu[0], rs, v16, WS_FU); ld16c<1024>(pu[1], rs, v16, WS_FU); }
    if constexpr (ST == 9) {
      check1(pl, 1);
      if (fft_on) { tif<2>(); check4(pu, WS_FU);
        sfor<0, 2>([&](auto Q) { ld16c<Q * 1024>(ur[Q], rs, v16, WS_UT + (T == 288 ? 0 : T * 8192) + g * 2048); });
        sfor<0, 2>([&](auto C) { sfor<0, 2>([&](auto A) { ld16<C * 2048 + A * 1024>(tF64[C][A], rt, v16, TAB_F64 + g * 4096); }); }); }
      tend<2>();
      if (hf == 0) { tif<0>(); sfor<0, 4>([&](auto I) { const uint32_t s = sh + fblk(IC<2>{}, I) * 8192; ld16c<0>(hc[I][0], rs, v16, s); ld16c<1024>(hc[I][1], rs, v16, s); }); }
      tend<0>();
    }
    Gelu ge; u32x4 o;
    SB();
    sfor<0, 18>([&](auto S) {
      constexpr int G = 18 * ST + S, FT = S & 1;
      const u32x4 a = fr[G & 3];
      if constexpr ((ST & 1) == 0 && S < 2) c0[FT] = mmb(a, x0[KB + (S >> 1)], bi[FT]); else c0[FT] = mmb(a, x0[KB + (S >> 1)], c0[FT]);
      SB();
      if constexpr (S + 3 < 18) fr[(G + 3) & 3] = rd1(ST, IC<S + 3>{});
      else if constexpr (ST > 3 && ST != 9) fr[(G + 3) & 3] = rd1(IC<ST + 1>{}, IC<S + 3 - 18>{});
      SB();
      if constexpr ((ST & 1) == 0 && S < 2) c1[FT] = mmb(a, x1[KB + (S >> 1)], bi[FT]); else c1[FT] = mmb(a, x1[KB + (S >> 1)], c1[FT]);
      SB();
      if constexpr (HF != 1 && S >= 1 && S < 17) { if constexpr (ST & 1) ge.run<S - 1>(p1, o); else ge.run<S - 1>(p0, o); }
      if constexpr (ST == 2 && S % 3 == 2 && S / 3 < 5) dma1(IC<5>{}, IC<S / 3>{}, w);
      if constexpr (ST == 2 && S == 15) touch(r2, (jj * 324 + w * 41 + l) * 128, ks * 1152 * 1152, (1ull << 41) - 1);
      if constexpr (S % 3 == 1 && S / 3 < 5) { if constexpr (ST < 7) dma1(IC<(ST == 2 ? 4 : ST + 3)>{}, IC<S / 3>{}, w); else dma2(IC<ST - 7>{}, IC<S / 3>{}); }
      if constexpr (HF == 1 && ST == 3 && (S == 3 || S == 9)) wtask(8 * g + (S == 3 ? 0 : 32) + (l >> 3));
      if constexpr (ST >= 5 && ST < 8 && S == 15) { st16c<0>(u32x4{~0u, ~0u, ~0u, ~0u}, rs, v16, WS_PART + b * 32768 + ((ST - 5) * 8 + w) * 1024);
        if constexpr (ST == 5) { if (w < 3) st16c<0>(u32x4{~0u, ~0u, ~0u, ~0u}, rs, v16, WS_PART + b * 32768 + (24 + w) * 1024); } }
      if constexpr (HF == 1 && ST == 3 && S == 15) { if (g == 0 || (g == 1 && b >= 192)) wtask(g == 0 ? 64 + (l >> 3) : 72); }
      if constexpr (ST == 9 && S == 15) {
        if (fft_on) { tif<2>(); waitv<9>();
          sfor<0, 2>([&](auto Q) { pin(ur[Q]); *(LAS u32x4*)(cx + (g >> 1) * 4096 + ((l >> 2) & 1) * 2048 + ((l >> 3) & 1) * 1024 + (l & 3) * 256 + (((8 * (g & 1) + 4 * Q + gq) ^ (l & 7)) * 16)) = ur[Q]; }); }
        tend<2>(); }
      if constexpr (HF != 1 && S == 17) { st16c<(ST & 1) * 1024>(o, rs, v16, sh + (9 * fj + (R == 1 ? lone : odd + 2 * (R - 2) + hf)) * 8192); if constexpr (R > 1) hown[R - 2][ST & 1] = o; }
      SB();
    });
    if constexpr (ST & 1) { p0[0] = c0[0]; p0[1] = c0[1]; p1[0] = c1[0]; p1[1] = c1[1]; }
  };
  auto fill0 = [&](auto S) {
    if constexpr (S == 0) { ST_(30); barl(); ST_(31); }
    else if constexpr (S < 6) dma1(IC<2>{}, IC<S - 1>{}, w);
    else if constexpr (S < 11) dma1(IC<3>{}, IC<S - 6>{}, w);
    else if constexpr (S == 11) { const int i = jj * 216 + w * 27 + l, r = i / 12, m = i - 12 * r, q = m / 3;
      touch(r1, r * 9216 + (q >> 1) * 1152 + (q & 1) * 640 + (m - 3 * q + 1) * 128, ks * 2304, (1ull << 27) - 1); }
    else if constexpr (S < 14) {
      const int j = (l * 13) >> 7;
      dmah(mksrd(S == 13 ? wi : wr, 2048 * 578), j * 36992 + (l - 10 * j) * 16, ((8 * (b & 7) + w + 256 * ((b >> 3) & 7)) * 578 + (b >= 192 ? 420 : 144 * (b >> 6))) & ~3u,
           L_W + (S - 12) * 5248 + w * 656, (1ull << 40) - 1); } };
  const f32x4 z4 = {0, 0, 0, 0};
  const uint32_t su = WS_UT + (2 * mt + (g >> 1)) * 256 + (g & 1) * 128;
  auto ust = [&](const f32x4 (&u)[4], uint32_t so) { f32x4 a, c; const float sg = odd ? -1.0f : 1.0f;
    sfor<0, 4>([&](auto J) { a[(int)J] = __builtin_fmaf(u[2][(int)J], sg, u[0][(int)J]); c[(int)J] = __builtin_fmaf(u[3][(int)J], sg, u[1][(int)J]); });
    st16c<0>(u32x4{pk_h(a.x, a.y), pk_h(a.z, a.w), pk_h(c.x, c.y), pk_h(c.z, c.w)}, rs, (i16 >> 1) * 8192 + (i16 & 1) * 64 + gq * 16,
             so); };
  if (hf == 0) { tif<0>();
    ldx(); ld4(b1v, mksrd(b1, 9216), tid * 4, 576 * fj);
    bar0();
    sfor<0, 2>([&](auto RB) { stats(RB, IC<2>{}, [&](auto) {}); ST_(1 + RB); chain(RB); norm(RB, [&](auto C) { put(RB, C); }); });
    waitv<0>(); pin(b1v);
    if (tid < 144) *(LAS f32x2*)(smem + L_B1 + tid * 8) = f32x2{lo_f(b1v), hi_f(b1v)};
    ST_(28);
    barl();
    sfor<0, 18>([&](auto S) { rdx(x0, S); });
    const uint32_t vo = (8 * ((l >> 4) & 1) + 4 * hh + ((l >> 2) & 3)) * 9216 + (((l & 3) ^ (((l >> 4) & 1) << 1)) * 16), so = (288 * fj + 32 * lone) * 2 + 16 * g * 9216;
    sfor<0, 9>([&](auto Q) { if constexpr (Q == NB2) { barl(); ST_(3); }
      dma(r1, vo, so + 64 * Q * 9216, (g + 4 * Q) * 1024);
      if constexpr (Q < 4) dma(rt, v16, TAB_TF + tileA * 9216 + (g + 4 * Q) * 1024, BUF + (g + 4 * Q) * 1024); });
    if (g < 2) dma(rt, v16, TAB_TF + tileA * 9216 + (g + 16) * 1024, BUF + (g + 16) * 1024);
    if (ks == 2) { sfor<0, 2>([&](auto Q) { dma(rt, v16, TAB_TF + tileC * 9216 + (g + 4 * Q) * 1024, BUF + 18432 + (g + 4 * Q) * 1024); });
      if (g == 3) dma(rt, v16, TAB_TF + tileC * 9216 + 8192, BUF + 18432 + 8192); }
    { const int i = jj * 72 + g * 18 + l; touch(r1, (i >> 2) * 9216 + ((i >> 1) & 1) * 1152 + (i & 1) * 640, ks * 2304, (1ull << 18) - 1); }
    mktag();
    const f32x4 bi[2] = {bias(lone, F0{}), bias(lone, T1{})};
    const uint32_t al0 = oq_v(rho * 64 + (gq & 1) * 32 + p4 * 8), al1 = oq_v(al0 ^ 32);
    auto rd = [&](auto S) { return tr2(smem + ((S & 1) ? al1 : al0) + (S >> 1) * 2048, 512); };
    waitv<1>(); ST_(29); bar0(); ST_(4);
    sfor<0, 3>([&](auto S) { fr[S] = rd(S); });
    sfor<0, 18>([&](auto S) { rdx(x1, S); }); SB();
    sfor<0, 36>([&](auto S) { const u32x4 a = fr[S & 3];
      p0[S & 1] = mmb(a, x0[S >> 1], S < 2 ? bi[S & 1] : p0[S & 1]); SB();
      if constexpr (S + 3 < 36) fr[(S + 3) & 3] = rd(IC<S + 3>{});
      SB();
      p1[S & 1] = mmb(a, x1[S >> 1], S < 2 ? bi[S & 1] : p1[S & 1]); SB();
      if constexpr (S >= 4 && !(S & 1)) fill0(IC<(S - 4) / 2>{});
      SB(); });
    ST_(32); ST_(33);
    stage(IC<2>{}, IC<0>{}); stage(IC<3>{}, IC<0>{});
  } else { tif<1>();
    if (w == 7) code_touch(jj, l);
    if (w == 6) { const int i = jj * 9 + l - 18;
      touch(rt, l < 18 ? (18 * (l & 1) + 4 * ks) * 9216 + (jj * 9 + (l >> 1)) * 128 : 16 * 9216 + (i >= 144 ? 147456 : 0) + i * 128, TAB_TF, ks == 2 ? (1ull << 27) - 1 : (1ull << 18) - 1); }
    if (w == 5) touch(r1, (jj * 18 + (l >> 1)) * 9216 + 512 + (l & 1) * 1152, ks * 2304, (1ull << 36) - 1);
    bar0();
    ldx();
    stats(F0{}, T1{}, [&](auto) {}); chain(F0{}); SB();
    ST_(1);
    bar0();
    ST_(2);
    norm(F0{}, [&](auto C) { rdx(x0, IC<2 * C>{}); rdx(x0, IC<2 * C + 1>{}); });
    ST_(28);
    barl();
    ST_(3);
    stats(T1{}, T1{}, [&](auto C) { put(F0{}, C); }); chain(T1{}); norm(T1{}, [&](auto C) { put(T1{}, C); });
    waitv<0>(); mktag();
    ST_(29); barl(); ST_(4);
    const uint32_t at = oq_v(BUF + v16), suA = oq_s(su + tileA * 65536), suC = oq_s(su + tileC * 65536);
    f32x4 ua[2][4];
    auto rd = [&](auto S) { return *(LAS u32x4*)(smem + at + (S & 1) * 9216 + (S >> 1) * 1024); };
    sfor<0, 2>([&](auto S) { fr[S] = rd(S); });
    sfor<0, 9>([&](auto S) { rdx(x1, S); rdx(x1, IC<S + 9>{}); }); SB();
    sfor<0, 18>([&](auto S) { constexpr int I = S >> 1, TT = S & 1; const u32x4 t = fr[S & 3];
      ua[TT][0] = mmb(x0[I], t, I ? ua[TT][0] : z4); SB();
      if constexpr (S + 2 < 18) fr[(S + 2) & 3] = rd(IC<S + 2>{});
      SB();
      ua[TT][2] = mmb(x0[I + 9], t, I ? ua[TT][2] : z4); SB();
      if constexpr (S >= 2) fill0(IC<S - 2>{});
      SB();
      ua[TT][1] = mmb(x1[I], t, I ? ua[TT][1] : z4); ua[TT][3] = mmb(x1[I + 9], t, I ? ua[TT][3] : z4); SB(); });
    ST_(32);
    if (ks == 2) { f32x4 uc[4];
      sfor<0, 9>([&](auto I) { const u32x4 t = *(LAS u32x4*)(smem + at + 18432 + I * 1024);
        uc[0] = mmb(x0[I], t, I ? uc[0] : z4); uc[2] = mmb(x0[I + 9], t, I ? uc[2] : z4); uc[1] = mmb(x1[I], t, I ? uc[1] : z4); uc[3] = mmb(x1[I + 9], t, I ? uc[3] : z4); });
      ust(uc, suC); }
    ust(ua[0], suA); ust(ua[1], suA + 65536);
    ST_(33);
    stage(IC<2>{}, IC<1>{}); stage(IC<3>{}, IC<1>{});
  }
  tend<0>(); tend<1>();
  sfor<4, 10>([&](auto ST) { stage(ST, IC<2>{}); });

  f32x4 oo[9], os[9];
  const uint32_t a2 = (4 * gq + q4) * 288 + p4 * 8;
  uint32_t pb = 0;
  auto blocks = [&](auto NB, auto G0, auto PRE, auto NEXT, auto ZERO, auto BOTH, uint32_t cur, uint32_t nxt, auto&& hfrag, auto&& fill) {
    auto rd = [&](auto S) { return tr2(smem + pb + (S / 9) * 9216 + (S % 9) * 32, 4608); };
    if constexpr (!PRE) { pb = oq_v(a2 + cur); sfor<0, 3>([&](auto S) { fr[(G0 + S) & 3] = rd(S); }); SB(); }
    sfor<0, 9 * NB>([&](auto S) {
      constexpr int G = G0 + S, B = S / 9, DT = S % 9;
      const u32x4 a = fr[G & 3];
      oo[DT] = mmb(a, hfrag(IC<B>{}, IC<0>{}), (ZERO && B == 0) ? z4 : oo[DT]);
      SB();
      if constexpr (S + 3 < 9 * NB) fr[(G + 3) & 3] = rd(IC<S + 3>{});
      else if constexpr (NEXT) { if constexpr (S + 3 == 9 * NB) pb = oq_v(a2 + nxt); fr[(G + 3) & 3] = rd(IC<S + 3 - 9 * NB>{}); }
      SB();
      if constexpr (BOTH) os[DT] = mmb(a, hfrag(IC<B>{}, IC<1>{}), (ZERO && B == 0) ? z4 : os[DT]);
      SB();
      fill(S);
      SB();
    }); };
  const uint32_t sown = sh + (9 * fj + odd + 6 + hf) * 8192;
  barl();
  ST_(13);
  ld8c(pl, rs, vfh, 0);
  if (fft_on) { tif<2>();
    { LAS char* pi = cx + 2048 + gq * 256 + ((i16 ^ (4 + gq)) * 16); LAS char* pr = T == 288 ? pi : cx + gq * 256 + ((i16 ^ gq) * 16);
      sfor<0, 2>([&](auto TT) { sfor<0, 2>([&](auto A) { uu[0][TT][A] = *(LAS u32x4*)(pr + TT * 4096 + A * 1024); uu[1][TT][A] = *(LAS u32x4*)(pi + TT * 4096 + A * 1024); }); }); }
    sfor<0, 2>([&](auto C) { sfor<0, 2>([&](auto A) { ld16<C * 2048 + A * 1024>(tF32[C][A], rt, v16, TAB_F32); }); });
    sfor<0, 2>([&](auto P) { ld16c<P * 1024>(wt[P], rs, v16, WS_WT + T * 8192 + g * 2048); });
    sfor<0, 2>([&](auto C) { sfor<0, 2>([&](auto A) { ld16<C * 2048 + A * 1024>(tG32[C][A], rt, v16, TAB_G32); }); });
    sfor<0, 2>([&](auto C) { sfor<0, 2>([&](auto A) { ld16<C * 2048 + A * 1024>(tG64[C][A], rt, v16, TAB_G64 + g * 4096); }); });
  }
  tend<2>();
  if (hf) { tif<1>();
    Gelu ge, g2;
    sfor<0, 16>([&](auto S) { ge.run<S>(p0, hown[3][0]); g2.run<S>(p1, hown[3][1]); });
    st16c<0>(hown[3][0], rs, v16, sown); st16c<1024>(hown[3][1], rs, v16, sown);
    sfor<0, 5>([&](auto I) { dma2(IC<3>{}, I); });
  }
  tend<1>();
  ST_(14);
  if (fft_on) { tif<2>();
    waitv_lb<28>();
    sfor<0, 2>([&](auto C) { sfor<0, 2>([&](auto A) { pin(tF64[C][A]); }); });
    if (special) sfor<0, 2>([&](auto TT) { sfor<0, 2>([&](auto A) { uu[1][TT][A] = u32x4{0, 0, 0, 0}; }); });
    float br[8], bi[8];
    sfor<0, 2>([&](auto TT) {
      f32x4 P = mmh(uu[0][TT][0], tF64[0][0], z4); P = mmh(uu[0][TT][1], tF64[0][1], P);
      f32x4 Q = mmh(uu[1][TT][0], tF64[1][0], z4); Q = mmh(uu[1][TT][1], tF64[1][1], Q);
      f32x4 I = mmh(uu[0][TT][0], tF64[1][0], z4); I = mmh(uu[0][TT][1], tF64[1][1], I);
      I = mmh(uu[1][TT][0], tF64[0][0], I); I = mmh(uu[1][TT][1], tF64[0][1], I);
      sfor<0, 4>([&](auto J) { const float an = (float)((16 * TT + 4 * gq + J) * (16 * g + i16)) * (1.0f / 2048.0f);
        const float c = __builtin_amdgcn_cosf(an), s = __builtin_amdgcn_sinf(an), re = P[(int)J] - Q[(int)J], im = I[(int)J];
        br[4 * TT + J] = re * c + im * s; bi[4 * TT + J] = im * c - re * s; });
    });
    const u32x4 Br = {pk_h(br[0], br[1]), pk_h(br[2], br[3]), pk_h(br[4], br[5]), pk_h(br[6], br[7])};
    const u32x4 Bi = {pk_h(bi[0], bi[1]), pk_h(bi[2], bi[3]), pk_h(bi[4], bi[5]), pk_h(bi[6], bi[7])};
    float yr[8], yi[8];
    waitv<11>(); if (hf == 0) { tif<0>(); waitv<4>(); } tend<0>();
    sfor<0, 2>([&](auto C) { sfor<0, 2>([&](auto A) { pin(tF32[C][A]); pin(tG32[C][A]); }); pin(wt[C]); });
    sfor<0, 2>([&](auto TT) {
      f32x4 P = mmh(tF32[0][TT], Br, z4), Q = mmh(tF32[1][TT], Bi, z4);
      f32x4 I = mmh(tF32[1][TT], Br, z4); I = mmh(tF32[0][TT], Bi, I);
      sfor<0, 4>([&](auto J) { const unsigned a = wt[TT][J >> 1], c = wt[TT][2 + (J >> 1)];
        const float p = (J & 1) ? hi_f(a) : lo_f(a), q = (J & 1) ? hi_f(c) : lo_f(c), re = P[(int)J] - Q[(int)J], im = I[(int)J];
        yr[4 * TT + J] = re * p - im * q; yi[4 * TT + J] = re * q + im * p; });
    });
    const u32x4 Yr = {pk_bf(yr[0], yr[1]), pk_bf(yr[2], yr[3]), pk_bf(yr[4], yr[5]), pk_bf(yr[6], yr[7])};
    const u32x4 Yi = {pk_bf(yi[0], yi[1]), pk_bf(yi[2], yi[3]), pk_bf(yi[4], yi[5]), pk_bf(yi[6], yi[7])};
    sfor<0, 2>([&](auto TT) {
      f32x4 P = mmb(Yr, tG32[0][TT], z4), Q = mmb(Yi, tG32[1][TT], z4);
      f32x4 I = mmb(Yr, tG32[1][TT], z4); I = mmb(Yi, tG32[0][TT], I);
      float cr[4], ci[4];
      sfor<0, 4>([&](auto J) { const float an = (float)((16 * TT + i16) * (16 * g + 4 * gq + J)) * (1.0f / 2048.0f);
        const float c = __builtin_amdgcn_cosf(an), s = __builtin_amdgcn_sinf(an), re = P[(int)J] - Q[(int)J], im = I[(int)J];
        cr[J] = re * c - im * s; ci[J] = im * c + re * s; });
      LAS char* p = cx + (16 * TT + i16) * 128 + (((2 * g + (gq >> 1)) ^ (i16 >> 1)) * 16) + (gq & 1) * 8;
      *(LAS u32x2*)p = u32x2{pk_bf(cr[0], cr[1]), pk_bf(cr[2], cr[3])};
      *(LAS u32x2*)(p + 4096) = u32x2{pk_bf(ci[0], ci[1]), pk_bf(ci[2], ci[3])};
    });
  }
  tend<2>();
  if (hf == 0) { tif<0>();
    Gelu ge, g2;
    blocks(IC<4>{}, F0{}, F0{}, F0{}, T1{}, T1{}, 2 * BUF, 0, [&](auto B, auto TT) { return hown[B][TT]; },
      [&](auto S) { if constexpr (S < 16) { ge.run<S>(p0, hown[3][0]); g2.run<S>(p1, hown[3][1]); }
        if constexpr (S == 10) { if (!fft_on) bar0(); }
        if constexpr (S == 16) st16c<0>(hown[3][0], rs, v16, sown); if constexpr (S == 17) st16c<1024>(hown[3][1], rs, v16, sown);
        if constexpr (S >= 18 && S < 28 && !(S & 1)) dma2(IC<3>{}, IC<(S - 18) / 2>{}); });
  }
  tend<0>();
  ST_(15);
  waitv_lb<5>();
  ST_(16);
  if (tid == 0) setflag(ws, WS_FH + b * 128, tag + 3);
  check1(pl, 2);
  u32x4 hr[3][2][2];
  auto ldh = [&](auto J, auto K) { const uint32_t s = sh + fblk(J, 2 * hf + K) * 8192; ld16c<0>(hr[J % 3][K][0], rs, vho, s); ld16c<0>(hr[J % 3][K][1], rs, vhs, s); };
  if (fft_on) { tif<2>();
    sfor<0, 2>([&](auto C) { sfor<0, 2>([&](auto A) { pin(tG64[C][A]); }); });
    const uint32_t vz = (i16 >> 1) * 147456 + (i16 & 1) * 64 + (gq & 1) * 32 + (gq >> 1) * 16, sz = WS_ZT + (T == 288 ? 32 : T * 512) + g * 128;
    sfor<0, 2>([&](auto TT) {
      LAS char* p0 = cx + (16 * TT + i16) * 128 + ((gq ^ (i16 >> 1)) * 16); LAS char* p1 = cx + (16 * TT + i16) * 128 + (((gq + 4) ^ (i16 >> 1)) * 16);
      const u32x4 r0 = *(LAS u32x4*)p0, r1_ = *(LAS u32x4*)p1, i0 = *(LAS u32x4*)(p0 + 4096), i1 = *(LAS u32x4*)(p1 + 4096);
      f32x4 P = mmb(tG64[0][0], r0, z4); P = mmb(tG64[0][1], r1_, P);
      f32x4 Q = mmb(tG64[1][0], i0, z4); Q = mmb(tG64[1][1], i1, Q);
      f32x4 I = mmb(tG64[1][0], r0, z4); I = mmb(tG64[1][1], r1_, I); I = mmb(tG64[0][0], i0, I); I = mmb(tG64[0][1], i1, I);
      unsigned ra = pk_bf(P.x - Q.x, P.y - Q.y), rb = pk_bf(P.z - Q.z, P.w - Q.w), ia = pk_bf(I.x, I.y), ib = pk_bf(I.z, I.w);
      auto sa = __builtin_amdgcn_permlane16_swap(ra, ia, false, false); auto sb = __builtin_amdgcn_permlane16_swap(rb, ib, false, false);
      const unsigned sa0 = sa[0], sa1 = sa[1], sb0 = sb[0], sb1 = sb[1];
      const u32x4 o = {sa0, sb0, sa1, sb1};
      if (!(special && (gq & 1))) st16c<0>(o, rs, vz, sz + TT * 8 * 147456);
    });
  }
  tend<2>();
  { auto fill = [&](auto S) {
      if constexpr (S < 10 && !(S & 1)) dma2(IC<4>{}, IC<S / 2>{});
      if constexpr (S == 10 || S == 12) ldh(IC<3>{}, IC<(S - 10) / 2>{});
      if constexpr (S == 20) ld8c(pl, rs, vfh, 0);
      if constexpr (S == 22 || S == 24) ldh(IC<4>{}, IC<(S - 22) / 2>{}); };
    if (hf == 0) { tif<0>(); sfor<0, 4>([&](auto I) { pin(hc[I][0]); pin(hc[I][1]); });
      blocks(IC<4>{}, F0{}, F0{}, F0{}, F0{}, T1{}, 0, 0, [&](auto B, auto TT) { return hc[B][TT]; }, fill); }
    else { tif<1>(); blocks(IC<4>{}, F0{}, F0{}, F0{}, T1{}, T1{}, 3 * BUF, 0, [&](auto B, auto TT) { return hown[B][TT ^ 1]; }, fill); }
    tend<0>(); tend<1>();
  }
  const int zb0 = ks < 2 ? 5 * ks : 4 * ks + 2;
  u32x4 zr[5], xv, bv;
  u32x2 xv8, bv8;
  uint32_t vta = 0, vtb = 0;
  auto dmat = [&](auto I) {
    if constexpr (I == 0) { sw = oq_s(w); auto f = [&](int c) { const int r = c / 18, k = c % 18; return (uint32_t)(r * 288 + (k < 16 ? ((k + 4 * ks) & 15) : k) * 16); }; vta = f(w * 64 + oq_v(l)); vtb = f(512 + oq_v(l)); }
    if constexpr (I < 4) dma(rt, vta, TAB_TI + ((zb0 + I) * 4 + nt) * 9216, 2 * BUF + I * 9216 + sw * 1024);
    else if constexpr (I == 4) dmah(rt, vtb, TAB_TI + ((zb0 + (sw >> 1)) * 4 + nt) * 9216, 2 * BUF + (sw >> 1) * 9216 + 8192, hmask);
    else if (ks < 2) { dma(rt, vta, TAB_TI + ((zb0 + 4) * 4 + nt) * 9216, L_SP + sw * 1024); if (w == 0) dma(rt, vtb, TAB_TI + ((zb0 + 4) * 4 + nt) * 9216, L_SP + 8192); }
  };
  const uint32_t azw = (4 * w + 2 * hh + ((l >> 1) & 1)) * 288 + (((l >> 4) & 1) + 2 * ((l >> 2) & 1)) * 64 + ((l >> 3) & 1) * 32 + (l & 1) * 16;
  const uint32_t srow = sx + 288 * nt, vrow = (l >> 2) * 36864;
  sfor<3, 8>([&](auto J) {
    waitv_b<(J == 3 ? 4 : J == 4 ? 9 : J == 5 ? 9 : J == 6 ? 9 : 10)>();
    ST_(14 + J);
    if constexpr (J == 3) { if (tid == 0) setflag(ws, WS_FZ + b * 8, tag); check1(pl, 3); }
    if constexpr (J == 5) { ld16c<0>(pu[0], rs, v16, WS_FZ); ld16c<1024>(pu[1], rs, v16, WS_FZ); }
    if constexpr (J == 6) check4(pu, WS_FZ);
    sfor<0, 2>([&](auto K) { pin(hr[J % 3][K][0]); pin(hr[J % 3][K][1]); });
    SB();
    blocks(IC<2>{}, IC<18 * J>{}, IC<(J > 3)>{}, T1{}, F0{}, T1{}, ((J + 2) & 3) * BUF + hf * 18432, ((J + 3) & 3) * BUF + hf * 18432,
      [&](auto B, auto TT) { return hr[J % 3][B][TT]; },
      [&](auto S) {
        if constexpr (J == 3) { if constexpr (S < 5) dma2(IC<5>{}, S); if constexpr (S == 6 || S == 8) ldh(IC<5>{}, IC<(S - 6) / 2>{}); if constexpr (S >= 10 && S < 15) dma2(IC<6>{}, IC<S - 10>{}); }
        if constexpr (J == 4) { if constexpr (S < 10 && !(S & 1)) dma2(IC<7>{}, IC<S / 2>{}); if constexpr (S == 11 || S == 13) ldh(IC<6>{}, IC<(S - 11) / 2>{}); }
        if constexpr (J == 5) { if constexpr (S < 12 && !(S & 1)) dmat(IC<S / 2>{}); if constexpr (S == 11 || S == 13) ldh(IC<7>{}, IC<(S - 11) / 2>{}); }
        if constexpr (J == 6) { if constexpr (S < 5) ld16c<0>(zr[S], rs, v16, WS_ZT + mt * 147456 + (zb0 + (S == 4 && ks >= 2 ? 0 : (int)S)) * 8192 + w * 1024);
          if constexpr (S >= 5 && S < 15 && (S & 1)) dma2(IC<9>{}, IC<(S - 5) / 2>{}); }
        if constexpr (J == 7) { if constexpr (S == 0 || S == 2) ldh(IC<9>{}, IC<S / 2>{});
          if constexpr (S == 8) waitv<9>();
          if constexpr (S >= 8 && S < 12) { pin(zr[S - 8]); *(LAS u32x4*)(smem + azw + (S - 8) * 9216) = zr[S - 8]; }
          if constexpr (S == 13) { ld16<0>(xv, rx, vrow + (l & 3) * 16, srow + 64 * ks); ld8<256>(xv8, rx, vrow + (l & 3) * 8, srow); }
          if constexpr (S == 15) { const u32x4 rb = mksrd(b2, 1152); ld16<0>(bv, rb, (l & 3) * 16, 288 * nt + 64 * ks); ld8<256>(bv8, rb, (l & 3) * 8, 288 * nt); } }
      });
  });
  { waitv_lb<8>();
    ST_(22);
    pin(zr[4]); *(LAS u32x4*)(smem + BUF + azw) = zr[4];
    const uint32_t az = oq_v(a2 + hf * 18432 + g * 64);
    u32x4 z[2][2];
    sfor<0, 2>([&](auto K) { z[K][0] = tr2(smem + az + K * 9216 + hf * 32, 4608); z[K][1] = tr2(smem + az + K * 9216 + (hf ^ 1) * 32, 4608); });
    SB();
    blocks(IC<2>{}, IC<144>{}, T1{}, F0{}, F0{}, T1{}, 0, 0, [&](auto B, auto TT) { return z[B][TT]; }, [&](auto S) {});
  }
  { waitv_lb<4>();
    ST_(23);
    if (ks < 2) { const u32x4 z = tr2(smem + BUF + a2 + g * 64 + hf * 32, 4608);
      blocks(IC<1>{}, F0{}, F0{}, F0{}, F0{}, F0{}, L_SP, 0, [&](auto B, auto TT) { return z; }, [&](auto S) {}); }
    sfor<0, 2>([&](auto K) { pin(hr[0][K][0]); pin(hr[0][K][1]); });
    const uint32_t aw = oq_v((hf ? 0 : 2 * BUF) + g * 9216 + v16);
    blocks(IC<2>{}, F0{}, F0{}, F0{}, F0{}, T1{}, 3 * BUF + hf * 18432, 0, [&](auto B, auto TT) { return hr[0][B][TT]; },
      [&](auto S) { if constexpr (S >= 10) *(LAS f32x4*)(smem + aw + (S - 10) * 1024) = os[S - 10]; });
    *(LAS f32x4*)(smem + aw + 8192) = os[8];
    barl();
    ST_(24);
    const uint32_t ar = oq_v((hf ? 2 * BUF : 0) + g * 9216 + v16);
    sfor<0, 9>([&](auto DT) { oo[(DT + 2) % 9] += *(LAS f32x4*)(smem + ar + ((DT + 2) % 9) * 1024); });
  }
  auto pk1 = [&](float p, float q) { const unsigned v = pk_bf(p, q); return v < ~1u ? v : ~1u; };
  sfor<1, 4>([&](auto K) { const f32x4 p = oo[2 * K], q = oo[2 * K + 1];
    st16c<0>(u32x4{pk1(p.x, p.y), pk1(p.z, p.w), pk1(q.x, q.y), pk1(q.z, q.w)}, rs, v16, WS_PART + ((b & ~6) + ((ks + K) & 3) * 2) * 32768 + ((K - 1) * 8 + w) * 1024); });
  if (g != ks) st8c(u32x2{pk1(oo[8].x, oo[8].y), pk1(oo[8].z, oo[8].w)}, rs, l * 8, WS_PART + ((b & ~6) + g * 2) * 32768 + 24576 + (((g - ks) & 3) - 1) * 1024 + hf * 512);
  ST_(25);
  { u32x4 pp[3]; u32x2 p8[3];
    const uint32_t s8 = WS_PART + b * 32768 + (g == ks ? 24576 + hf * 512 : w * 1024);
#pragma nounroll
    for (unsigned spins = 0; spins < (1u << 16); ++spins) {
      sfor<0, 3>([&](auto K) { ld16c<0>(pp[K], rs, v16, WS_PART + b * 32768 + (K * 8 + w) * 1024); ld8c(p8[K], rs, l * 8, s8 + (g == ks ? K * 1024 : 0)); });
      waitv<0>();
      unsigned m = 0;
      sfor<0, 3>([&](auto K) { pin(pp[K]); pin(p8[K]); const unsigned a = pp[K].x, c = pp[K].y, d = pp[K].z, e = pp[K].w, f = p8[K].x, h = p8[K].y;
        m = max(max(max(m, a), max(c, d)), max(max(e, f), h)); });
      if (__all(m != ~0u)) break;
      __builtin_amdgcn_s_sleep(2);
    }
    ST_(26);
    LAS char* pt = smem + BUF + w * 3072;
    sfor<0, 3>([&](auto Q) { f32x4 r = oo[Q < 2 ? (int)Q : 8];
      sfor<0, 3>([&](auto K) { const unsigned a = Q == 2 ? p8[K].x : pp[K][2 * (Q & 1)], c = Q == 2 ? p8[K].y : pp[K][2 * (Q & 1) + 1];
        r += f32x4{lo_f(a), hi_f(a), lo_f(c), hi_f(c)}; });
      *(LAS f32x4*)(pt + Q * 1024 + i16 * 64 + gq * 16) = r; });
    pin(xv); pin(bv); pin(xv8); pin(bv8);
    LAS char* pq = pt + ((l >> 1) & 1) * 1024 + (l >> 2) * 64 + (l & 1) * 32;
    const f32x4 ra = *(LAS f32x4*)pq, rb = *(LAS f32x4*)(pq + 16), rc = *(LAS f32x4*)(pt + 2048 + v16);
    auto fin = [&](float a, float c, unsigned xx, unsigned bb) { return pk_bf(a + lo_f(xx) + lo_f(bb), c + hi_f(xx) + hi_f(bb)); };
    char* op = out + srow + vrow;
    *(u32x4*)(op + 64 * ks + (l & 3) * 16) = u32x4{fin(ra.x, ra.y, xv.x, bv.x), fin(ra.z, ra.w, xv.y, bv.y), fin(rb.x, rb.y, xv.z, bv.z), fin(rb.z, rb.w, xv.w, bv.w)};
    if (g == ks) *(u32x2*)(op + 256 + (l & 3) * 8) = u32x2{fin(rc.x, rc.y, xv8.x, bv8.x), fin(rc.z, rc.w, xv8.y, bv8.y)};
  }
  if (b == 0 && tid == 0) setflag(ws, WS_EPOCH, tag);
  asm volatile(".Lfk_end:");
}

extern "C" void kernel_launch(void* const* d_in, const int* in_sizes, int n_in, void* d_out, int out_size, void* d_ws, size_t ws_size, hipStream_t stream) {
  (void)hipFuncSetAttribute((const void*)fused_kernel, hipFuncAttributeMaxDynamicSharedMemorySize, LDS_BYTES);
  fused_kernel<<<256, 512, LDS_BYTES, stream>>>((const char*)d_in[0], (const char*)d_in[1], (const char*)d_in[2], (const char*)d_in[4], (const char*)d_in[5],
                                                (const char*)d_in[6], (const char*)d_in[7], (char*)d_out, (char*)d_ws);
}
```

```cpp
#include <hip/hip_runtime.h>
#include <stdint.h>
#include <type_traits>
#define LAD 3
#define TP 1
#define PRIO 0

typedef unsigned short u16;
typedef short s16x4 __attribute__((ext_vector_type(4)));
typedef short s16x8 __attribute__((ext_vector_type(8)));
typedef __bf16 bf16x8 __attribute__((ext_vector_type(8)));
typedef __bf16 bf16x2 __attribute__((ext_vector_type(2)));
typedef _Float16 f16x8 __attribute__((ext_vector_type(8)));
typedef _Float16 f16x2 __attribute__((ext_vector_type(2)));
typedef float f32x4 __attribute__((ext_vector_type(4)));
typedef float f32x2 __attribute__((ext_vector_type(2)));
typedef unsigned u32x4 __attribute__((ext_vector_type(4)));
typedef unsigned u32x2 __attribute__((ext_vector_type(2)));
typedef unsigned long long u64;
#define LAS __attribute__((address_space(3)))
#define GAS __attribute__((address_space(1)))
#define DI __device__ __forceinline__

namespace tab {
constexpr double PI = 3.14159265358979323846;
constexpr double ccos(int a, int n) {
  a %= n; if (a < 0) a += n;
  if (2 * a > n) a = n - a;
  bool neg = false;
  if (4 * a > n) { a = n / 2 - a; neg = true; }
  if (4 * a == n) return 0.0;
  double x = 2 * PI * a / n, t = 1, s = 1;
  for (int i = 1; i <= 13; i++) { t *= -x * x / ((2 * i - 1) * (2 * i)); s += t; }
  return neg ? -s : s;
}
constexpr u16 to_bf(double v) { float f = (float)v; uint32_t u = __builtin_bit_cast(uint32_t, f); u += 0x7fffu + ((u >> 16) & 1u); return (u16)(u >> 16); }
constexpr u16 to_h(double v) { _Float16 h = (_Float16)v; return __builtin_bit_cast(u16, h); }
struct C576 { u16 b1[576]; u16 b2[576]; };
constexpr C576 gen576() { C576 r{}; for (int i = 0; i < 576; i++) { double c = ccos(i, 576) / 24.0; r.b1[i] = to_bf(c); r.b2[i] = to_bf(2 * c); } return r; }
constexpr C576 c576 = gen576();
constexpr int colk(int c) { return c == 0 ? 0 : c == 1 ? 288 : c < 288 ? (c & ~1) : ((c - 288) & ~1) + 1; }
constexpr int colp(int c) { return c < 2 ? 0 : (c & 1); }
struct Chunk { u16 v[9216]; };
constexpr Chunk genTF(int u) {
  Chunk r{};
  for (int i = 0; i < 1152; i++) { int t = i / 576, s = (i % 576) >> 6, l = i & 63, c = 32 * u + 16 * t + (l & 15), k = colk(c), o = 144 * colp(c), d = 32 * s + 8 * (l >> 4);
    for (int j = 0; j < 8; j++) r.v[i * 8 + j] = c576.b1[((d + j) * k + o) % 576]; }
  return r;
}
constexpr Chunk genTI(int q) {
  Chunk r{};
  for (int i = 0; i < 64; i++) { int c = 32 * (q >> 1) + (i & 31), k = colk(c), o = 144 * colp(c), d0 = 144 * (2 * (q & 1) + (i >> 5));
    if (c < 2) { for (int d = 0; d < 144; d++) r.v[i * 144 + d] = c576.b1[((d0 + d) * k + o) % 576]; }
    else { for (int d = 0; d < 144; d++) r.v[i * 144 + d] = c576.b2[((d0 + d) * k + o) % 576]; } }
  return r;
}
template <int I> constexpr Chunk cTF = genTF(I);
template <int I> constexpr Chunk cTI = genTI(I);
struct Small { u16 f64[4][2][2][512]; u16 f32[2][2][512]; u16 g32[2][2][512]; u16 g64[4][2][2][512]; };
constexpr Small genSmall() {
  Small r{};
  double c64[64] = {}; for (int i = 0; i < 64; i++) c64[i] = ccos(i, 64);
  const double r32 = 0.17677669529663688110;
  for (int l = 0; l < 64; l++) for (int j = 0; j < 8; j++) {
    int rr = l & 15, gq = l >> 4;
    int p2 = 16 * (j >> 2) + 4 * gq + (j & 3);
    for (int w = 0; w < 4; w++) for (int a = 0; a < 2; a++) {
      int e = (16 * w + rr) * (32 * a + 8 * gq + j), e1 = (16 * w + rr) * (32 * a + p2);
      r.f64[w][0][a][l * 8 + j] = to_h(c64[e1 & 63] / 8); r.f64[w][1][a][l * 8 + j] = to_h(c64[(e1 + 16) & 63] / 8);
      r.g64[w][0][a][l * 8 + j] = to_bf(c64[e & 63] / 8); r.g64[w][1][a][l * 8 + j] = to_bf(c64[(e + 48) & 63] / 8);
    }
    for (int t = 0; t < 2; t++) {
      int e = 2 * ((16 * t + rr) * p2);
      r.f32[0][t][l * 8 + j] = to_h(c64[e & 63] * r32); r.f32[1][t][l * 8 + j] = to_h(c64[(e + 16) & 63] * r32);
      r.g32[0][t][l * 8 + j] = to_bf(c64[e & 63] * r32); r.g32[1][t][l * 8 + j] = to_bf(c64[(e + 48) & 63] * r32);
    }
  }
  return r;
}
struct C2048 { u16 v[2048]; };
constexpr C2048 gen2048() { C2048 r{}; for (int i = 0; i < 2048; i++) r.v[i] = to_h(ccos(i, 2048)); return r; }
constexpr C2048 c2048 = gen2048();
struct Tw { u16 v[2][4][2][512]; };
constexpr Tw genTw() {
  Tw r{};
  for (int w = 0; w < 4; w++) for (int t = 0; t < 2; t++) for (int l = 0; l < 64; l++) for (int j = 0; j < 4; j++) {
    int e0 = (16 * t + 4 * (l >> 4) + j) * (16 * w + (l & 15)), e1 = (16 * t + (l & 15)) * (16 * w + 4 * (l >> 4) + j);
    r.v[0][w][t][l * 8 + 2 * j] = c2048.v[e0 & 2047]; r.v[0][w][t][l * 8 + 2 * j + 1] = c2048.v[(e0 + 1536) & 2047];
    r.v[1][w][t][l * 8 + 2 * j] = c2048.v[e1 & 2047]; r.v[1][w][t][l * 8 + 2 * j + 1] = c2048.v[(e1 + 1536) & 2047]; }
  return r;
}
constexpr Tw ctw = genTw();
constexpr int fblk(int nt, int J, int i) {
  if (J == 0) return 9 * nt + (nt & 1) + 2 * i;
  if (J == 1) { int pn = (nt + i) & 3; return 9 * pn + ((pn & 1) ? 0 : 8); }
  int q = 4 * (J - 2) + i, r = q / 7, m = q - 7 * r + 1, pn = (nt + (m >> 1)) & 3;
  return 9 * pn + (pn & 1) + 2 * r + (m & 1);
}
struct Adr { uint32_t w[4][4][64]; uint32_t h[4][2][64]; uint32_t v[4][576]; };
constexpr Adr genAdr() {
  Adr r{};
  for (int nt = 0; nt < 4; nt++) {
    for (int k = 0; k < 4; k++) for (int l = 0; l < 45; l++) r.w[nt][k][l] = fblk(nt, l / 5, l % 5 < 4 ? l % 5 : k) * 36864 + 288 * nt;
    for (int k = 0; k < 2; k++) for (int l = 0; l < 36; l++) r.h[nt][k][l] = fblk(nt, l / 4, (l + 2 * k) & 3) * 8192;
    for (int c = 0; c < 576; c++) { int k = c % 18; r.v[nt][c] = (c / 18) * 1152 + (k < 16 ? ((k + 4 * nt) & 15) : k) * 16 + nt * 36 * 36864; } }
  return r;
}
struct alignas(256) All { Chunk tf[18]; Chunk ti[36]; Small sm; Tw tw; Adr adr; };
template <int... I, int... J> constexpr All mk(std::integer_sequence<int, I...>, std::integer_sequence<int, J...>) { return All{{cTF<I>...}, {cTI<J>...}, genSmall(), ctw, genAdr()}; }
}
static __device__ const tab::All g_tab = tab::mk(std::make_integer_sequence<int, 18>{}, std::make_integer_sequence<int, 36>{});

constexpr uint32_t BUF = 36864, L_SP = 147456, L_B1 = L_SP, L_JUNK = L_SP + 2048, L_W = L_SP + 4096, LDS_BYTES = 163840;
constexpr uint32_t WS_EPOCH = 0, WS_FU = 4096, WS_FZ = 8192, WS_FH = 16384;
constexpr uint32_t WS_H = 1u << 20, WS_UT = 20u << 20, WS_WT = 23u << 20, WS_ZT = 26u << 20, WS_PART = 29u << 20, WS_END = 48u << 20;
constexpr uint32_t TAB_TF = 0, TAB_TI = 18 * 18432, TAB_F64 = 54 * 18432, TAB_F32 = TAB_F64 + 16384, TAB_G32 = TAB_F32 + 4096, TAB_G64 = TAB_G32 + 4096, TAB_TW = TAB_G64 + 16384, TAB_AW = TAB_TW + 16384, TAB_AH = TAB_AW + 4096, TAB_AV = TAB_AH + 2048, TAB_END = TAB_AV + 9216;

template <int I, int N, class Fn> DI void sfor(Fn&& f) { if constexpr (I < N) { f(std::integral_constant<int, I>{}); sfor<I + 1, N>(f); } }
template <int V> using IC = std::integral_constant<int, V>;

DI u32x4 mksrd(const void* p, uint32_t n) { return u32x4{(unsigned)(size_t)p, (unsigned)((size_t)p >> 32) & 0xffffu, n, 0x00020000u}; }
template <int IMM> DI void ld16(u32x4& d, u32x4 srd, uint32_t voff, uint32_t soff) { asm volatile("buffer_load_dwordx4 %0, %1, %2, %3 offen offset:%4" : "=v"(d) : "v"(voff), "s"(srd), "s"(soff), "i"(IMM) : "memory"); }
template <int IMM> DI void ld16c(u32x4& d, u32x4 srd, uint32_t voff, uint32_t soff) { asm volatile("buffer_load_dwordx4 %0, %1, %2, %3 offen offset:%4 sc1" : "=v"(d) : "v"(voff), "s"(srd), "s"(soff), "i"(IMM) : "memory"); }
template <int IMM> DI void ld8(u32x2& d, u32x4 srd, uint32_t voff, uint32_t soff) { asm volatile("buffer_load_dwordx2 %0, %1, %2, %3 offen offset:%4" : "=v"(d) : "v"(voff), "s"(srd), "s"(soff), "i"(IMM) : "memory"); }
DI void ld8c(u32x2& d, u32x4 srd, uint32_t voff, uint32_t soff) { asm volatile("buffer_load_dwordx2 %0, %1, %2, %3 offen sc1" : "=v"(d) : "v"(voff), "s"(srd), "s"(soff) : "memory"); }
DI void ld4(unsigned& d, u32x4 srd, uint32_t voff, uint32_t soff) { asm volatile("buffer_load_dword %0, %1, %2, %3 offen" : "=v"(d) : "v"(voff), "s"(srd), "s"(soff) : "memory"); }
DI void dma(u32x4 srd, uint32_t voff, uint32_t soff, uint32_t lds) { asm volatile("s_mov_b32 m0, %3\n\ts_nop 0\n\tbuffer_load_dwordx4 %0, %1, %2 offen lds" :: "v"(voff), "s"(srd), "s"(soff), "s"(lds) : "memory"); }
DI void dmah(u32x4 srd, uint32_t voff, uint32_t soff, uint32_t lds, u64 mask) {
  asm volatile("s_mov_b32 m0, %3\n\ts_mov_b64 exec, %4\n\tbuffer_load_dwordx4 %0, %1, %2 offen lds\n\ts_mov_b64 exec, -1" :: "v"(voff), "s"(srd), "s"(soff), "s"(lds), "s"(mask) : "memory"); }
template <uint32_t LDS, uint32_t SO> DI void dmak(u32x4 srd, uint32_t voff, uint32_t base) { uint32_t t;
  asm volatile("s_add_u32 m0, %3, %4\n\ts_mov_b32 %0, %5\n\tbuffer_load_dwordx4 %1, %2, %0 offen lds" : "=&s"(t) : "v"(voff), "s"(srd), "s"(base), "i"(LDS), "i"(SO) : "memory", "scc"); }
template <uint32_t LDS, uint32_t SO> DI void dmakh(u32x4 srd, uint32_t voff, uint32_t base, u64 mask) { uint32_t t;
  asm volatile("s_add_u32 m0, %3, %4\n\ts_mov_b32 %0, %5\n\ts_mov_b64 exec, %6\n\tbuffer_load_dwordx4 %1, %2, %0 offen lds\n\ts_mov_b64 exec, -1"
               : "=&s"(t) : "v"(voff), "s"(srd), "s"(base), "i"(LDS), "i"(SO), "s"(mask) : "memory", "scc"); }
template <int L> DI uint32_t lane_of(uint32_t t) { uint32_t r; asm volatile("v_readlane_b32 %0, %1, %2" : "=s"(r) : "v"(t), "i"(L)); return r; }
template <uint32_t LDS> DI void setm0(uint32_t base) { asm volatile("s_add_u32 m0, %0, %1" :: "s"(base), "i"(LDS) : "scc"); }
DI void dmago(u32x4 srd, uint32_t voff, uint32_t soff) { asm volatile("buffer_load_dwordx4 %0, %1, %2 offen lds" :: "v"(voff), "s"(srd), "s"(soff) : "memory"); }
DI void dmagoh(u32x4 srd, uint32_t voff, uint32_t soff, u64 mask) {
  asm volatile("s_mov_b64 exec, %3\n\tbuffer_load_dwordx4 %0, %1, %2 offen lds\n\ts_mov_b64 exec, -1" :: "v"(voff), "s"(srd), "s"(soff), "s"(mask) : "memory"); }
DI void nop4() { asm volatile("s_nop 4"); }
DI void nop0() { asm volatile("s_nop 0"); }
DI void touch(u32x4 srd, uint32_t voff, uint32_t soff, u64 mask) {
  asm volatile("s_mov_b32 m0, %3\n\ts_mov_b64 exec, %4\n\tbuffer_load_dword %0, %1, %2 offen lds\n\ts_mov_b64 exec, -1" :: "v"(voff), "s"(srd), "s"(soff), "s"(L_JUNK), "s"(mask) : "memory"); }
DI void code_touch(int part, int l) {
  u64 pc; uint32_t n;
  asm volatile("s_getpc_b64 %0\n.Lfk_begin:\n\ts_mov_b32 %1, .Lfk_end-.Lfk_begin" : "=s"(pc), "=s"(n));
  touch(u32x4{(unsigned)pc, (unsigned)(pc >> 32) & 0xffffu, n, 0x00020000u}, l * 128, part * 2048, 0xffffull); }
template <int IMM> DI void st16c(u32x4 d, u32x4 srd, uint32_t voff, uint32_t soff) {
  asm volatile("buffer_store_dwordx4 %0, %1, %2, %3 offen offset:%4 sc1\n\ts_nop 1" :: "v"(d), "v"(voff), "s"(srd), "s"(soff), "i"(IMM) : "memory"); }
DI void st16m(u32x4 d, u32x4 srd, uint32_t voff, uint32_t soff, u64 mask) {
  asm volatile("s_mov_b64 exec, %4\n\tbuffer_store_dwordx4 %0, %1, %2, %3 offen sc1\n\ts_mov_b64 exec, -1\n\ts_nop 0" :: "v"(d), "v"(voff), "s"(srd), "s"(soff), "s"(mask) : "memory"); }
DI void st8c(u32x2 d, u32x4 srd, uint32_t voff, uint32_t soff) {
  asm volatile("buffer_store_dwordx2 %0, %1, %2, %3 offen sc1\n\ts_nop 1" :: "v"(d), "v"(voff), "s"(srd), "s"(soff) : "memory"); }
template <int N> DI void waitv() { asm volatile("s_waitcnt vmcnt(%0)" :: "i"(N) : "memory"); }
template <int N> DI void waitv_b() { asm volatile("s_waitcnt vmcnt(%0)\n\ts_barrier" :: "i"(N) : "memory"); }
template <int N> DI void waitv_lb() { asm volatile("s_waitcnt vmcnt(%0) lgkmcnt(0)\n\ts_barrier" :: "i"(N) : "memory"); }
DI void bar0() { asm volatile("s_barrier" ::: "memory"); }
DI void barl() { asm volatile("s_waitcnt lgkmcnt(0)\n\ts_barrier" ::: "memory"); }
DI void pin(u32x4& v) { asm volatile("" : "+v"(v)); }
DI void pin(u32x2& v) { asm volatile("" : "+v"(v)); }
DI void pin(unsigned& v) { asm volatile("" : "+v"(v)); }
DI void fix(float& v) { asm volatile("" : "+v"(v)); }
DI void kill(u32x4& v) { asm volatile("" : "=v"(v)); }
DI void kill(f32x4& v) { asm volatile("" : "=v"(v)); }
DI uint32_t oq_s(uint32_t v) { asm volatile("" : "+s"(v)); return v; }
DI uint32_t oq_v(uint32_t v) { asm volatile("" : "+v"(v)); return v; }
template <int P> DI void prio() { asm volatile("s_setprio %0" :: "i"(P)); }
#define SB() __builtin_amdgcn_sched_barrier(0)
template <int C> DI void tif() { asm volatile("; @IF %0" :: "i"(C)); }
template <int C> DI void tend() { asm volatile("; @END %0" :: "i"(C)); }

DI float asf(unsigned u) { return __builtin_bit_cast(float, u); }
DI unsigned asu(float f) { return __builtin_bit_cast(unsigned, f); }
DI unsigned pk_bf(float a, float b) { f32x2 p = {a, b}; bf16x2 r = __builtin_convertvector(p, bf16x2); return __builtin_bit_cast(unsigned, r); }
DI unsigned pk_h(float a, float b) { f32x2 p = {a, b}; f16x2 r = __builtin_convertvector(p, f16x2); return __builtin_bit_cast(unsigned, r); }
DI float bfr(float a) { return asf(pk_bf(a, a) << 16); }
DI float lo_f(unsigned u) { return asf(u << 16); }
DI float hi_f(unsigned u) { return asf(u & 0xffff0000u); }
DI float dot2(unsigned a, unsigned b, float c) { return __builtin_amdgcn_fdot2_f32_bf16(__builtin_bit_cast(bf16x2, a), __builtin_bit_cast(bf16x2, b), c, false); }
DI float vfma(float a, float b, float c) { float d; asm("v_fma_f32 %0, %1, %2, %3" : "=v"(d) : "v"(a), "v"(b), "v"(c)); return d; }
DI float vmul(float a, float b) { float d; asm("v_mul_f32 %0, %1, %2" : "=v"(d) : "v"(a), "v"(b)); return d; }
DI float vfms(float a, float b, float c) { float d; asm("v_fma_f32 %0, -%1, %2, %3" : "=v"(d) : "v"(a), "v"(b), "v"(c)); return d; }
template <int H, int N> DI float xmul(float a, unsigned t) { float d;
  if constexpr (N) asm("v_fma_mix_f32 %0, -%1, %2, 0 op_sel:[0,%3,0] op_sel_hi:[0,1,0]" : "=v"(d) : "v"(a), "v"(t), "i"(H));
  else asm("v_fma_mix_f32 %0, %1, %2, 0 op_sel:[0,%3,0] op_sel_hi:[0,1,0]" : "=v"(d) : "v"(a), "v"(t), "i"(H));
  return d; }
template <int H, int N> DI float xfma(float a, unsigned t, float c) { float d;
  if constexpr (N) asm("v_fma_mix_f32 %0, -%1, %2, %3 op_sel:[0,%4,0] op_sel_hi:[0,1,0]" : "=v"(d) : "v"(a), "v"(t), "v"(c), "i"(H));
  else asm("v_fma_mix_f32 %0, %1, %2, %3 op_sel:[0,%4,0] op_sel_hi:[0,1,0]" : "=v"(d) : "v"(a), "v"(t), "v"(c), "i"(H));
  return d; }
template <int H, int D> DI void xfmah(unsigned& d, float a, unsigned t, float c) {
  if constexpr (D == 0) asm("v_fma_mixlo_f16 %0, %1, %2, %3 op_sel:[0,%4,0] op_sel_hi:[0,1,0]" : "=v"(d) : "v"(a), "v"(t), "v"(c), "i"(H));
  else asm("v_fma_mixhi_f16 %0, %1, %2, %3 op_sel:[0,%4,0] op_sel_hi:[0,1,0]" : "+v"(d) : "v"(a), "v"(t), "v"(c), "i"(H)); }
template <int CTRL> DI float dpp_add(float v) { return v + asf(__builtin_amdgcn_update_dpp(0, asu(v), CTRL, 0xf, 0xf, true)); }
DI float sum8(float v) { return dpp_add<0x141>(dpp_add<0x4E>(dpp_add<0xB1>(v))); }
DI f32x4 mmb(u32x4 a, u32x4 b, f32x4 c) { return __builtin_amdgcn_mfma_f32_16x16x32_bf16(__builtin_bit_cast(bf16x8, a), __builtin_bit_cast(bf16x8, b), c, 0, 0, 0); }
DI f32x4 mmh(u32x4 a, u32x4 b, f32x4 c) { return __builtin_amdgcn_mfma_f32_16x16x32_f16(__builtin_bit_cast(f16x8, a), __builtin_bit_cast(f16x8, b), c, 0, 0, 0); }
DI u32x4 tr2(LAS char* p, int second) {
  s16x4 a = __builtin_amdgcn_ds_read_tr16_b64_v4i16((LAS s16x4*)p);
  s16x4 b = __builtin_amdgcn_ds_read_tr16_b64_v4i16((LAS s16x4*)(p + second));
  s16x8 c = __builtin_shufflevector(a, b, 0, 1, 2, 3, 4, 5, 6, 7);
  return __builtin_bit_cast(u32x4, c);
}
struct Gelu { float a0, a1;
  template <int Q> DI void step(float v0, float v1, unsigned& o) {
    if constexpr (Q == 0) { a0 = __builtin_fmaf(v0 * v0, -0.10294324f, -2.3022082f); a1 = __builtin_fmaf(v1 * v1, -0.10294324f, -2.3022082f); }
    if constexpr (Q == 1) { a0 = __builtin_amdgcn_exp2f(v0 * a0); a1 = __builtin_amdgcn_exp2f(v1 * a1); }
    if constexpr (Q == 2) { a0 = __builtin_amdgcn_rcpf(1.0f + a0); a1 = __builtin_amdgcn_rcpf(1.0f + a1); }
    if constexpr (Q == 3) { o = pk_bf(v0 * a0, __builtin_fmaf(v1, a1, 0.0f)); }
  }
  template <int K> DI void run(const f32x4 (&p)[2], u32x4& o) {
    constexpr int P = K >> 2; const float v0 = p[P >> 1][2 * (P & 1)], v1 = p[P >> 1][2 * (P & 1) + 1];
    unsigned t = 0; step<K & 3>(v0, v1, t); if constexpr ((K & 3) == 3) o[P] = t; }
};
#define ST_(i) do {} while (0)
#define RLX_AGENT __ATOMIC_RELAXED, __HIP_MEMORY_SCOPE_AGENT
DI bool all_of(bool c) { return __builtin_amdgcn_ballot_w64(c) == ~0ull; }
DI bool lvl_ok(unsigned lo, unsigned hi, u64 tag, unsigned lvl) { return (hi == (unsigned)(tag >> 32)) & (lo - (unsigned)tag - lvl <= 7u - lvl); }
template <int N> DI void spin(const char* base, uint32_t off, u64 tag, unsigned lvl) {
#pragma nounroll
  for (unsigned spins = 0; spins < (1u << 17); ++spins) {
    bool ok = true;
#pragma unroll
    for (int k = 0; k < N; ++k) { u64 v = __hip_atomic_load((GAS u64*)(base + off + (k & 1) * 8 + (k >> 1) * 1024), RLX_AGENT); ok &= lvl_ok((unsigned)v, (unsigned)(v >> 32), tag, lvl); }
    if (__all(ok)) break;
    __builtin_amdgcn_s_sleep(1);
  }
}
DI void setflag(char* ws, uint32_t off, u64 v) { __hip_atomic_store((GAS u64*)(ws + off), v, RLX_AGENT); }

__global__ __launch_bounds__(512, 2) void fused_kernel(const char* __restrict__ x, const char* __restrict__ wr, const char* __restrict__ wi,
    const char* __restrict__ w1, const char* __restrict__ b1, const char* __restrict__ w2, const char* __restrict__ b2, char* out, char* ws) {
  extern __shared__ __attribute__((aligned(16))) char smem_[];
  LAS char* smem = (LAS char*)smem_;
  const int tid = threadIdx.x, l = tid & 63;
  const int w = __builtin_amdgcn_readfirstlane(tid >> 6), g = w & 3, hf = w >> 2;
  const int b = blockIdx.x, xcd = b & 7, jj = b >> 3;
  const int ks = xcd >> 1, mt = (xcd & 1) * 8 + (jj >> 2), nt = jj & 3, fj = ks * 4 + nt;
  const u32x4 rs = mksrd(ws, WS_END), rx = mksrd(x, 2048 * 1152), r1 = mksrd(w1, 576 * 9216), r2 = mksrd(w2, 4608 * 1152), rt = mksrd(&g_tab, TAB_END);
  const int i16 = l & 15, q4 = i16 >> 2, p4 = i16 & 3, hh = l >> 5, gq = l >> 4;
  const u64 hmask = (w & 1) ? 0xffffffff00000000ull : 0xffffffffull;
  const uint32_t v16 = l * 16;
  asm volatile("" :: "s"(ws), "s"(out), "s"(b1), "s"(b2), "s"(w2), "s"(wr), "s"(wi));
  ST_(0);
  u32x4 x0[18], x1[18];
  const uint32_t vx = (l >> 3) * 36864 + (l & 7) * 16;
  const uint32_t sx = (uint32_t)((32 * (g & 1) + 16 * hf) * 32 + 2 * mt + (g >> 1)) * 1152;
  const int odd = fj & 1, lone = odd ? 0 : 8;
  const int tileA = 18 * odd + 2 * (fj >> 1), tileC = 18 * odd + 16 + (nt >> 1);
  u32x2 ep; unsigned b1v;
  const uint32_t vo1 = (8 * hh + ((l >> 3) & 3)) * 9216 + (((l & 7) ^ (((l >> 4) & 3) << 1)) * 16);
  uint32_t v1a = 0, v1b = 0, sown = 0, sbox = 0;
  const uint32_t sw4 = oq_s(w * 4096), sw1 = oq_s(w * 1024), sh1 = oq_s((w >> 1) * 1024), sh9 = oq_s((w >> 1) * 9216);
  auto dma1 = [&](auto ST, auto I) {
    constexpr uint32_t so = 288 * (ST & 1) * 9216 + 128 * ((ST >> 1) - 1), lb = (ST & 3) * BUF;
    if constexpr (I < 4) dmak<lb + I * 1024, so + (16 * (I >> 1) + 4 * (I & 1)) * 9216>(r1, v1a, sw4);
    else dmakh<lb + 32768, so>(r1, v1b, sh1, hmask);
  };
  const uint32_t axb = 2 * BUF + g * 18432;
  const uint32_t axw = axb + ((l >> 2) & 1) * 1024 + (l & 3) * 256 + (((l >> 3) ^ (l & 7)) * 16);
  const uint32_t axr0 = axb + gq * 256 + ((i16 ^ gq) * 16), axr1 = axr0 ^ 64;
  auto rdx = [&](u32x4 (&d)[18], auto S) { d[S] = *(LAS u32x4*)(smem + ((S & 1) ? axr1 : axr0) + S * 1024); };
  u32x4 xr[2][9];
  f32x2 rn2[2], mb2[2];
  auto ldx = [&]() { sfor<0, 18>([&](auto C) { ld16<(C % 9) * 128>(xr[C / 9][C % 9], rx, vx, sx + (C / 9) * 294912); }); ld8c(ep, rs, 0, WS_EPOCH); };
  auto stats = [&](auto RB, auto NV, auto&& fill) {
    float sa[4] = {}, qa[4] = {};
    sfor<0, 9>([&](auto C) { fill(C);
      waitv<NV + 17 - 9 * RB - C>(); pin(xr[RB][C]);
      sfor<0, 4>([&](auto D) { const unsigned u = xr[RB][C][(int)D]; sa[D] = dot2(u, 0x3f803f80u, sa[D]); qa[D] = dot2(u, u, qa[D]); });
      SB(); });
    rn2[RB] = f32x2{(sa[0] + sa[1]) + (sa[2] + sa[3]), (qa[0] + qa[1]) + (qa[2] + qa[3])}; };
  auto chain = [&](auto RB) {
    const float mean = sum8(rn2[RB].x) * (1.0f / 576.0f);
    const float var = fmaxf(sum8(rn2[RB].y) * (1.0f / 576.0f) - mean * mean, 0.0f);
    const float sd = bfr(__builtin_amdgcn_sqrtf(bfr(bfr(var) + 1e-5f)));
    const float rn = __builtin_amdgcn_rcpf(sd), mb = -bfr(mean) * rn;
    rn2[RB] = f32x2{rn, rn}; mb2[RB] = f32x2{mb, mb}; };
  auto put = [&](auto RB, auto C) { *(LAS u32x4*)(smem + axw + RB * 128 + C * 2048) = xr[RB][C]; };
  auto norm = [&](auto RB, auto&& fill) {
    sfor<0, 9>([&](auto C) { f32x2 f[4];
      sfor<0, 4>([&](auto D) { const unsigned u = xr[RB][C][(int)D]; f[D] = f32x2{lo_f(u), hi_f(u)}; });
      SB();
      sfor<0, 4>([&](auto D) { f[D] = __builtin_elementwise_fma(f[D], rn2[RB], mb2[RB]); });
      SB();
      xr[RB][C] = u32x4{pk_bf(f[0].x, f[0].y), pk_bf(f[1].x, f[1].y), pk_bf(f[2].x, f[2].y), pk_bf(f[3].x, f[3].y)};
      fill(C);
      SB(); }); };
  using T1 = IC<1>; using F0 = IC<0>;
  u64 tag;
  auto mktag = [&]() { pin(ep);
    tag = ((u64)(unsigned)__builtin_amdgcn_readfirstlane(ep.y) << 32) | (unsigned)__builtin_amdgcn_readfirstlane(ep.x);
    tag += 0x9E3779B97F4A7C15ull; tag = (tag ^ (tag >> 30)) * 0xBF58476D1CE4E5B9ull; tag = (tag ^ (tag >> 27)) * 0x94D049BB133111EBull; tag = (tag ^ (tag >> 31)) & ~7ull; };
  f32x4 c0[2], c1[2], p0[2], p1[2];
  auto bias = [&](int unit, auto FT) { return *(LAS f32x4*)(smem + L_B1 + (32 * unit + 16 * FT + 4 * gq) * 4); };
  const int rho = 16 * (gq >> 1) + 4 * (gq & 1) + q4;
  uint32_t vo2a, vo2b;
  const uint32_t sh = WS_H + (mt * 144 * 4 + g) * 2048;
  uint32_t tabw, tabh;
  uint32_t sq = 0;
  auto dma2p = [&](auto J, auto I) { constexpr uint32_t lb = ((J + 2) & 3) * BUF;
    sq = lane_of<5 * J + I>(tabw); if constexpr (I < 4) setm0<lb + I * 9216>(sw1); else setm0<lb + 8192>(sh9); };
  auto dma2g = [&](auto I) { if constexpr (I < 4) dmago(r2, vo2a, sq); else dmagoh(r2, vo2b, sq, hmask); };
  const uint32_t vho = v16 + hf * 1024, vhs = v16 + (hf ^ 1) * 1024;
  const uint32_t vfh = WS_FH + ((b & ~24) + gq * 8) * 128;
  auto check1 = [&](u32x2& p, unsigned lvl) { pin(p); if (!all_of(lvl_ok(p.x, p.y, tag, lvl))) spin<1>(ws, vfh, tag, lvl); };
  auto check4 = [&](u32x4 (&p)[2], uint32_t base) { pin(p[0]); pin(p[1]);
    if (!all_of(lvl_ok(p[0].x, p[0].y, tag, 0) & lvl_ok(p[0].z, p[0].w, tag, 0) & lvl_ok(p[1].x, p[1].y, tag, 0) & lvl_ok(p[1].z, p[1].w, tag, 0))) spin<4>(ws, base + v16, tag, 0); };
  const int zb0 = ks < 2 ? 5 * ks : 4 * ks + 2;
  const int T2 = 256 + (xcd & 1) * 32 + jj;
  const bool dbl = ks == 3 && T2 <= 288;
  u32x4 tF64[2][2], tF32[2][2], tG32[2][2], tG64[2][2], tw[2][2], ur[2], wt[2], pu[2], hc[4][2], hown[4][2];
  u32x2 pl;

  const uint32_t a1 = rho * 128 + (((2 * hf) ^ ((rho >> 1) & 3)) * 32) + p4 * 8;
  uint32_t pa0 = 0, pa1 = 0;
  u32x4 fr[4];
  auto rd1 = [&](auto ST, auto S) {
    if constexpr (S == 0) { pa0 = oq_v(a1 + (ST & 3) * BUF); pa1 = oq_v(pa0 ^ 32); }
    return tr2(smem + ((S & 1) ? pa1 : pa0) + (S >> 1) * 4096, 1024); };
  auto ldu = [&](int T) { sfor<0, 2>([&](auto Q) { ld16c<Q * 1024>(ur[Q], rs, v16, WS_UT + (T == 288 ? 0 : T * 8192) + g * 2048); }); };
  auto putu = [&]() { sfor<0, 2>([&](auto Q) { pin(ur[Q]);
    *(LAS u32x4*)(smem + L_SP + (g >> 1) * 4096 + ((l >> 2) & 1) * 2048 + ((l >> 3) & 1) * 1024 + (l & 3) * 256 + (((8 * (g & 1) + 4 * Q + gq) ^ (l & 7)) * 16)) = ur[Q]; }); };
  auto ldt = [&](u32x4 (&t)[2][2], uint32_t so) { sfor<0, 2>([&](auto C) { sfor<0, 2>([&](auto A) { ld16<C * 2048 + A * 1024>(t[C][A], rt, v16, so); }); }); };
  auto ld2h = [&](u32x4 (&h)[2], uint32_t so) { ld16c<0>(h[0], rs, v16, so); ld16c<1024>(h[1], rs, v16, so); };
  uint32_t s4[4] = {};
  f32x4 bi[2];
  auto wtask = [&](int cl) {
    LAS char* p = smem + L_W + (l & 7) * 656 + (l & 1) * 2 + cl * 2 + (b >= 192 ? 12 : 0); unsigned v[8];
    sfor<0, 8>([&](auto J) { v[J] = *(LAS u16*)(p + (J >> 2) * 5248 + (J & 3) * 160); });
    const int kc = 72 * (b >> 6) + cl;
    st16c<0>(u32x4{v[0] | (v[1] << 16), v[2] | (v[3] << 16), v[4] | (v[5] << 16), v[6] | (v[7] << 16)}, rs,
             (uint32_t)(kc == 288 ? 288 : (kc & 1) ? 144 + (kc >> 1) : (kc >> 1)) * 8192 + (l & 7) * 16,
             WS_WT + ((b >> 1) & 3) * 2048 + ((b >> 5) & 1) * 1024 + ((b >> 3) & 3) * 256 + (b & 1) * 128); };
  auto stage = [&](auto ST, auto HF) {
    constexpr int R = ST >> 1, KB = 9 * (ST & 1);
    if constexpr (ST == 9) bar0(); else waitv_b<(ST == 2 ? 11 + 2 * HF : ST == 3 ? 11 + HF : ST == 5 ? 6 : ST == 8 ? 9 : 7)>();
    ST_(3 + ST);
    if constexpr (ST == 3) { pin(tabw); pin(tabh); pin(vo2a); pin(vo2b); tabh += sh + 36 * 8192 * ks; }
    if constexpr (ST == 5) { if (tid == 0) setflag(ws, WS_FU + b * 8, tag); }
    if constexpr (ST == 7 || ST == 9) { if (tid == 0) setflag(ws, WS_FH + b * 128, tag + (ST - 5) / 2); }
    if constexpr (ST <= 4) sfor<0, 3>([&](auto S) { fr[(18 * ST + S) & 3] = rd1(ST, S); });
    if constexpr (ST == 2) { bi[0] = bias(odd + hf, IC<0>{}); bi[1] = bias(odd + hf, IC<1>{}); }
    if constexpr (ST == 8) ld8c(pl, rs, vfh, 0);
    if constexpr (ST == 9) {
      if (hf) { tif<1>(); ldt(tF64, TAB_F64 + g * 4096); ld16<0>(tw[0][0], rt, v16, TAB_TW + g * 2048); ld16<1024>(tw[0][1], rt, v16, TAB_TW + g * 2048); }
      else { tif<0>(); sfor<0, 4>([&](auto I) { ld2h(hc[I], s4[I]); }); }
      tend<0>(); tend<1>();
    }
    Gelu ge; u32x4 o;
    SB();
    sfor<0, 18>([&](auto S) {
      constexpr int G = 18 * ST + S, FT = S & 1;
      if constexpr (LAD & 1) { if constexpr (S == 0) prio<3>(); if constexpr (S == 5) prio<2>(); if constexpr (S == 10) prio<1>(); if constexpr (S == 14) prio<0>(); }
      if constexpr (S >= 2 && !(S & 1)) { __builtin_amdgcn_s_waitcnt(0xC27F); SB(); }
      const u32x4 a = fr[G & 3];
      if constexpr ((ST & 1) == 0 && S < 2) c0[FT] = mmb(a, x0[KB + (S >> 1)], bi[FT]); else c0[FT] = mmb(a, x0[KB + (S >> 1)], c0[FT]);
      SB();
      if constexpr (S + 3 < 18) fr[(G + 3) & 3] = rd1(ST, IC<S + 3>{});
      else if constexpr (ST > 3 && ST != 9) fr[(G + 3) & 3] = rd1(IC<ST + 1>{}, IC<S + 3 - 18>{});
      SB();
      if constexpr ((ST & 1) == 0 && S < 2) c1[FT] = mmb(a, x1[KB + (S >> 1)], bi[FT]); else c1[FT] = mmb(a, x1[KB + (S >> 1)], c1[FT]);
      SB();
      if constexpr (HF != 1 && S >= 1 && S < 17) { if constexpr (ST & 1) ge.run<S - 1>(p1, o); else ge.run<S - 1>(p0, o); }
      if constexpr (ST == 2 && S % 3 == 2 && S / 3 < 5) dma1(IC<5>{}, IC<S / 3>{});
      if constexpr (S % 3 == 1 && S / 3 < 5) { if constexpr (ST < 7) dma1(IC<(ST == 2 ? 4 : ST + 3)>{}, IC<S / 3>{}); else dma2g(IC<S / 3>{}); }
      if constexpr (ST >= 7 && S % 3 == 0 && S / 3 < 5) dma2p(IC<ST - 7>{}, IC<S / 3>{});
      if constexpr (HF == 1 && ST == 3 && (S == 3 || S == 9)) wtask(8 * g + (S == 3 ? 0 : 32) + (l >> 3));
      if constexpr (ST >= 5 && ST < 8 && S == 15) { st16c<0>(u32x4{~0u, ~0u, ~0u, ~0u}, rs, v16, sbox + (ST - 5) * 8192);
        if constexpr (ST == 5) { if (w < 3) st16c<0>(u32x4{~0u, ~0u, ~0u, ~0u}, rs, v16, sbox + 24576); } }
      if constexpr (HF == 1 && ST == 3 && S == 15) { if (g == 0 || (g == 1 && b >= 192)) wtask(g == 0 ? 64 + (l >> 3) : 72); }
      if constexpr (ST == 7 && S == 14) { ld16c<0>(pu[0], rs, v16, WS_FU); ld16c<1024>(pu[1], rs, v16, WS_FU); }
      if constexpr (ST == 8 && S == 8) { waitv<6>(); if (hf) { tif<1>(); check4(pu, WS_FU); ldu(b); } tend<1>(); }
      if constexpr (ST == 8 && S == 17) { waitv<5>(); check1(pl, 1); }
      if constexpr (ST == 9 && S == 15) { if (hf) { tif<1>(); waitv<12>(); putu(); } tend<1>(); }
      if constexpr (HF != 1 && S == 17) { st16c<(ST & 1) * 1024>(o, rs, v16, R == 1 ? sh + (9 * fj + lone) * 8192 : sown + 2 * (R - 2) * 8192); if constexpr (R > 1) hown[R - 2][ST & 1] = o; }
      if constexpr ((ST & 1) && ST < 9 && S == 17) { bi[0] = bias(odd + 2 * R + hf, IC<0>{}); bi[1] = bias(odd + 2 * R + hf, IC<1>{}); }
      if constexpr (ST == 8 && S == 17) sfor<0, 4>([&](auto I) { s4[I] = lane_of<4 + I>(tabh); });
      SB();
    });
    if constexpr (ST & 1) { p0[0] = c0[0]; p0[1] = c0[1]; p1[0] = c1[0]; p1[1] = c1[1]; }
  };
  auto fill0 = [&](auto S) {
    if constexpr (S == 0) { v1a = oq_v(vo1 + 32 * w * 9216 + (288 * fj + 32 * odd) * 2); v1b = oq_v(vo1 + (256 + 16 * hf + 4 * ((w >> 1) & 1)) * 9216 + (288 * fj + 32 * odd) * 2);
      ST_(30); barl(); ST_(31); }
    else if constexpr (S < 6) dma1(IC<2>{}, IC<S - 1>{});
    else if constexpr (S < 11) dma1(IC<3>{}, IC<S - 6>{});
    else if constexpr (S == 11) {}
    else if constexpr (S < 14) {
      const int j = (l * 13) >> 7;
      dmah(mksrd(S == 13 ? wi : wr, 2048 * 578), j * 36992 + (l - 10 * j) * 16, ((8 * (b & 7) + w + 256 * ((b >> 3) & 7)) * 578 + (b >= 192 ? 420 : 144 * (b >> 6))) & ~3u,
           L_W + (S - 12) * 5248 + w * 656, (1ull << 40) - 1); }
    else if constexpr (S == 14) { ld4(tabw, rt, l * 4, TAB_AW + nt * 1024 + (w >> 1) * 256); ld4(tabh, rt, l * 4, TAB_AH + nt * 512 + hf * 256); }
    else { ld4(vo2a, rt, l * 4, TAB_AV + ks * 2304 + w * 256); ld4(vo2b, rt, l * 4, TAB_AV + ks * 2304 + 2048); } };
  const f32x4 z4 = {0, 0, 0, 0};
  const uint32_t su = WS_UT + (2 * mt + (g >> 1)) * 256 + (g & 1) * 128;
  auto ust = [&](const f32x4 (&u)[4], uint32_t so) { f32x4 a, c; const float sg = odd ? -1.0f : 1.0f;
    sfor<0, 4>([&](auto J) { a[(int)J] = __builtin_fmaf(u[2][(int)J], sg, u[0][(int)J]); c[(int)J] = __builtin_fmaf(u[3][(int)J], sg, u[1][(int)J]); });
    st16c<0>(u32x4{pk_h(a.x, a.y), pk_h(a.z, a.w), pk_h(c.x, c.y), pk_h(c.z, c.w)}, rs, (i16 >> 1) * 8192 + (i16 & 1) * 64 + gq * 16,
             so); };
  if (hf == 0) { tif<0>();
    ldx(); ld4(b1v, mksrd(b1, 9216), tid * 4, 576 * fj);
    bar0();
    sfor<0, 2>([&](auto RB) { stats(RB, IC<(RB > TP ? 4 : 2)>{}, [&](auto) {}); ST_(1 + RB);
      if constexpr (RB == TP) { const int i = jj * 288 + g * 72 + l; const uint32_t t = (i >> 4) * 9216 + ((i >> 3) & 1) * 1152 + ((i >> 2) & 1) * 640 + (i & 3) * 128;
        touch(r1, t, ks * 2304, ~0ull); touch(r1, t, ks * 2304 + 36864, 0xffull); }
      chain(RB); norm(RB, [&](auto C) { put(RB, C); }); });
    waitv<2>(); pin(b1v);
    if (tid < 144) *(LAS f32x2*)(smem + L_B1 + tid * 8) = f32x2{lo_f(b1v), hi_f(b1v)};
    ST_(28);
    barl();
    sfor<0, 18>([&](auto S) { rdx(x0, S); });
    const uint32_t vo = (8 * ((l >> 4) & 1) + 4 * hh + ((l >> 2) & 3)) * 9216 + (((l & 3) ^ (((l >> 4) & 1) << 1)) * 16), so = (288 * fj + 32 * lone) * 2 + 16 * g * 9216;
    sfor<0, 9>([&](auto Q) { if constexpr (Q == 2) { barl(); ST_(3); }
      dma(r1, vo, so + 64 * Q * 9216, (g + 4 * Q) * 1024);
      if constexpr (Q < 4) dma(rt, v16, TAB_TF + tileA * 9216 + (g + 4 * Q) * 1024, BUF + (g + 4 * Q) * 1024); });
    if (g < 2) dma(rt, v16, TAB_TF + tileA * 9216 + (g + 16) * 1024, BUF + (g + 16) * 1024);
    if (ks == 2) { sfor<0, 2>([&](auto Q) { dma(rt, v16, TAB_TF + tileC * 9216 + (g + 4 * Q) * 1024, BUF + 18432 + (g + 4 * Q) * 1024); });
      if (g == 3) dma(rt, v16, TAB_TF + tileC * 9216 + 8192, BUF + 18432 + 8192); }
    { const uint32_t t = (jj * 324 + g * 81 + l) * 128;
      touch(r2, t, ks * 1152 * 1152, ~0ull); touch(r2, t, ks * 1152 * 1152 + 8192, (1ull << 17) - 1);
      touch(rt, l < 12 ? TAB_TI + zb0 * 36864 + ((jj * 4 + g) * 12 + l) * 128 : TAB_F64 + ((jj * 4 + g) * 4 + l - 12) * 128, 0, 0xffffull); }
    mktag();
    const f32x4 bl[2] = {bias(lone, F0{}), bias(lone, T1{})};
    const uint32_t al0 = oq_v(rho * 64 + (gq & 1) * 32 + p4 * 8), al1 = oq_v(al0 ^ 32);
    auto rd = [&](auto S) { return tr2(smem + ((S & 1) ? al1 : al0) + (S >> 1) * 2048, 512); };
    waitv<3>(); ST_(29); bar0(); ST_(4);
    sfor<0, 3>([&](auto S) { fr[S] = rd(S); });
    sfor<0, 18>([&](auto S) { rdx(x1, S); }); SB();
    sfor<0, 36>([&](auto S) { const u32x4 a = fr[S & 3];
      p0[S & 1] = mmb(a, x0[S >> 1], S < 2 ? bl[S & 1] : p0[S & 1]); SB();
      if constexpr (S + 3 < 36) fr[(S + 3) & 3] = rd(IC<S + 3>{});
      SB();
      p1[S & 1] = mmb(a, x1[S >> 1], S < 2 ? bl[S & 1] : p1[S & 1]); SB();
      if constexpr (S >= 4 && !(S & 1)) fill0(IC<(S - 4) / 2>{});
      SB(); });
    ST_(32);
    if (ks == 2) { f32x4 uc[4]; const uint32_t at = oq_v(BUF + 18432 + v16), suC = oq_s(su + tileC * 65536);
      u32x4 tc[9];
      sfor<0, 9>([&](auto I) { tc[I] = *(LAS u32x4*)(smem + at + I * 1024); });
      SB();
      sfor<0, 9>([&](auto I) { const u32x4 t = tc[I];
        uc[0] = mmb(x0[I], t, I ? uc[0] : z4); uc[2] = mmb(x0[I + 9], t, I ? uc[2] : z4); uc[1] = mmb(x1[I], t, I ? uc[1] : z4); uc[3] = mmb(x1[I + 9], t, I ? uc[3] : z4); });
      ust(uc, suC); }
    ST_(33);
    stage(IC<2>{}, IC<0>{}); stage(IC<3>{}, IC<0>{});
  } else { tif<1>();
    if (w == 7) code_touch(jj, l);
    if (w == 6) { const int i = jj * 9 + l - 18;
      touch(rt, l < 18 ? (18 * (l & 1) + 4 * ks) * 9216 + (jj * 9 + (l >> 1)) * 128 : 16 * 9216 + (i >= 144 ? 147456 : 0) + i * 128, TAB_TF, ks == 2 ? (1ull << 27) - 1 : (1ull << 18) - 1); }
    if (w == 5) touch(r1, (jj * 18 + (l >> 1)) * 9216 + 512 + (l & 1) * 1152, ks * 2304, (1ull << 36) - 1);
    bar0();
    ldx();
    stats(F0{}, T1{}, [&](auto) {}); chain(F0{}); SB();
    ST_(1);
    bar0();
    ST_(2);
    norm(F0{}, [&](auto C) { rdx(x0, IC<2 * C>{}); rdx(x0, IC<2 * C + 1>{}); });
    ST_(28);
    barl();
    ST_(3);
    stats(T1{}, T1{}, [&](auto C) { put(F0{}, C); }); chain(T1{}); norm(T1{}, [&](auto C) { put(T1{}, C); });
    waitv<0>(); mktag();
    ST_(29); barl(); ST_(4);
    const uint32_t at = oq_v(BUF + v16), suA = oq_s(su + tileA * 65536);
    f32x4 ua[2][4];
    auto rd = [&](auto S) { return *(LAS u32x4*)(smem + at + (S & 1) * 9216 + (S >> 1) * 1024); };
    sfor<0, 2>([&](auto S) { fr[S] = rd(S); });
    sfor<0, 9>([&](auto S) { rdx(x1, S); rdx(x1, IC<S + 9>{}); }); SB();
    sfor<0, 18>([&](auto S) { constexpr int I = S >> 1, TT = S & 1; const u32x4 t = fr[S & 3];
      ua[TT][0] = mmb(x0[I], t, I ? ua[TT][0] : z4); SB();
      if constexpr (S + 2 < 18) fr[(S + 2) & 3] = rd(IC<S + 2>{});
      SB();
      ua[TT][2] = mmb(x0[I + 9], t, I ? ua[TT][2] : z4); SB();
      if constexpr (S >= 2) fill0(IC<S - 2>{});
      SB();
      ua[TT][1] = mmb(x1[I], t, I ? ua[TT][1] : z4); ua[TT][3] = mmb(x1[I + 9], t, I ? ua[TT][3] : z4); SB(); });
    ST_(32);
    ust(ua[0], suA); ust(ua[1], suA + 65536);
    ST_(33);
    if (PRIO & 1) __builtin_amdgcn_s_setprio(1);
    stage(IC<2>{}, IC<1>{}); stage(IC<3>{}, IC<1>{});
  }
  tend<0>(); tend<1>();
  sown = oq_s(sh + (9 * fj + odd + hf) * 8192); sbox = oq_s(WS_PART + b * 32768 + w * 1024);
  sfor<4, 10>([&](auto ST) { stage(ST, IC<2>{}); });

  f32x4 oo[9], os[9];
  u32x4 f6[6];
  const uint32_t a2 = (4 * gq + q4) * 288 + p4 * 8;
  uint32_t pb = 0;
  auto rd2 = [&](auto S) { f6[S % 6] = tr2(smem + pb + (S / 9) * 9216 + (S % 9) * 32, 4608); };
  auto pre = [&](uint32_t cur) { pb = oq_v(a2 + cur); sfor<0, 4>(rd2); SB(); };
  auto blocks = [&](auto NB, auto NEXT, auto ZERO, auto BOTH, uint32_t nxt, auto&& hfrag, auto&& fill) {
    sfor<0, 9 * NB>([&](auto S) {
      constexpr int B = S / 9, DT = S % 9;
      if constexpr (!(S & 1) && (NEXT || S + 4 < 9 * NB)) { __builtin_amdgcn_s_waitcnt(0xC47F); SB(); }
      const u32x4 a = f6[S % 6];
      oo[DT] = mmb(a, hfrag(IC<B>{}, F0{}), (ZERO && B == 0) ? z4 : oo[DT]);
      SB();
      if constexpr (S + 4 < 9 * NB) rd2(IC<S + 4>{});
      else if constexpr (NEXT) { if constexpr (S + 4 == 9 * NB) pb = oq_v(a2 + nxt); rd2(IC<S + 4 - 9 * NB>{}); }
      SB();
      if constexpr (BOTH) os[DT] = mmb(a, hfrag(IC<B>{}, T1{}), (ZERO && B == 0) ? z4 : os[DT]);
      SB();
      fill(S);
      SB();
    }); };
  auto lbuf = [&](auto J) { return (uint32_t)(((J + 2) & 3) * BUF); };
  const uint32_t sow4 = sown + 6 * 8192;
  u32x4 hr[3][2][2], hx[2][2][2];
  uint32_t sa = 0, sb = 0;
  auto ldh = [&](auto J, auto K, auto TT, uint32_t so) { ld16c<0>(hr[J % 3][K][TT], rs, TT ? vhs : vho, so); };
  auto ldh4 = [&](auto J) { sa = lane_of<4 * J>(tabh); sb = lane_of<4 * J + 1>(tabh); nop4(); sfor<0, 4>([&](auto I) { ldh(J, IC<I / 2>{}, IC<I & 1>{}, I < 2 ? sa : sb); }); };
  auto solo = [&](auto J, auto S) {
    if constexpr (S % 4 == 0 && S < 20) dma2p(IC<J + 3>{}, IC<S / 4>{});
    if constexpr (S % 4 == 1 && S < 20) dma2g(IC<S / 4>{});
    if constexpr (S == 19 || S == 28) { sa = lane_of<4 * (J + 2) + (S - 19) / 4>(tabh); sb = lane_of<4 * (J + 2) + (S - 19) / 4 + 1>(tabh); }
    if constexpr (S >= 21 && S < 29 && (S & 1)) ldh(IC<J + 2>{}, IC<(S - 21) / 4>{}, IC<((S - 21) / 2) & 1>{}, S < 25 ? sa : sb);
    if constexpr (S == 30) { if (dbl) { tif<2>(); ld2h(hx[J & 1][0], sa); ld2h(hx[J & 1][1], sb); } tend<2>(); } };
  auto pin4 = [&](u32x4 (&t)[2][2]) { pin(t[0][0]); pin(t[0][1]); pin(t[1][0]); pin(t[1][1]); };
  auto kill4 = [&](u32x4 (&t)[2][2]) { kill(t[0][0]); kill(t[0][1]); kill(t[1][0]); kill(t[1][1]); };
  auto hflag = [&](int lvl) { if (tid == 0) setflag(ws, WS_FH + b * 128, tag + lvl); };
  u32x4 uu[2][2][2], Br, Bi;
  auto rdu = [&](int T) { LAS char* pi = smem + L_SP + 2048 + gq * 256 + ((i16 ^ (4 + gq)) * 16); LAS char* pr = T == 288 ? pi : smem + L_SP + gq * 256 + ((i16 ^ gq) * 16);
    sfor<0, 2>([&](auto TT) { sfor<0, 2>([&](auto A) { uu[0][TT][A] = *(LAS u32x4*)(pr + TT * 4096 + A * 1024); uu[1][TT][A] = *(LAS u32x4*)(pi + TT * 4096 + A * 1024); }); }); };
  auto ldw = [&](int T) { sfor<0, 2>([&](auto P) { ld16c<P * 1024>(wt[P], rs, v16, WS_WT + T * 8192 + g * 2048); }); };
  auto real = [&](f32x4 (&P)[2], const f32x4 (&Q)[2]) { SB(); sfor<0, 8>([&](auto E) { float r = P[E >> 2][E & 3] - Q[E >> 2][E & 3]; fix(r); P[E >> 2][E & 3] = r; }); SB(); };
  auto segA = [&](int T) {
    if (T == 0 || T == 288) sfor<0, 2>([&](auto TT) { sfor<0, 2>([&](auto A) { uu[1][TT][A] = u32x4{0, 0, 0, 0}; }); });
    unsigned br[4], bq[4]; f32x4 P[2], Q[2], I[2]; float ta[8], tb[8];
    sfor<0, 2>([&](auto TT) { I[TT] = mmh(uu[0][TT][0], tF64[1][0], z4); I[TT] = mmh(uu[0][TT][1], tF64[1][1], I[TT]);
      I[TT] = mmh(uu[1][TT][0], tF64[0][0], I[TT]); I[TT] = mmh(uu[1][TT][1], tF64[0][1], I[TT]); });
    SB();
    sfor<0, 2>([&](auto TT) { P[TT] = mmh(uu[0][TT][0], tF64[0][0], z4); P[TT] = mmh(uu[0][TT][1], tF64[0][1], P[TT]);
      Q[TT] = mmh(uu[1][TT][0], tF64[1][0], z4); Q[TT] = mmh(uu[1][TT][1], tF64[1][1], Q[TT]); });
    real(P, Q);
    sfor<0, 8>([&](auto E) { const unsigned t = tw[0][E >> 2][E & 3]; ta[E] = xmul<0, 0>(P[E >> 2][E & 3], t); tb[E] = xmul<1, 1>(P[E >> 2][E & 3], t); });
    SB();
    sfor<0, 8>([&](auto X) { constexpr int E = (X & 3) * 2 + (X >> 2); const unsigned t = tw[0][E >> 2][E & 3];
      xfmah<1, E & 1>(br[E / 2], I[E >> 2][E & 3], t, ta[E]); xfmah<0, E & 1>(bq[E / 2], I[E >> 2][E & 3], t, tb[E]); if constexpr (X == 3) SB(); });
    Br = u32x4{br[0], br[1], br[2], br[3]}; Bi = u32x4{bq[0], bq[1], bq[2], bq[3]}; pin(Br); pin(Bi); };
  auto segB = [&](auto&& mid) {
    float ta[8], tb[8], wp[8], wq[8]; f32x4 P[2], Q[2], I[2];
    pin(wt[0]); pin(wt[1]);
    sfor<0, 2>([&](auto TT) { I[TT] = mmh(tF32[1][TT], Br, z4); I[TT] = mmh(tF32[0][TT], Bi, I[TT]); });
    SB();
    sfor<0, 2>([&](auto TT) { P[TT] = mmh(tF32[0][TT], Br, z4); Q[TT] = mmh(tF32[1][TT], Bi, z4); });
    SB();
    sfor<0, 8>([&](auto E) { const unsigned a = wt[E >> 2][(E >> 1) & 1], c = wt[E >> 2][2 + ((E >> 1) & 1)]; wp[E] = (E & 1) ? hi_f(a) : lo_f(a); wq[E] = (E & 1) ? hi_f(c) : lo_f(c); });
    real(P, Q);
    sfor<0, 8>([&](auto E) { ta[E] = vmul(P[E >> 2][E & 3], wp[E]); tb[E] = vmul(P[E >> 2][E & 3], wq[E]); });
    SB();
    sfor<0, 8>([&](auto E) { ta[E] = vfms(I[E >> 2][E & 3], wq[E], ta[E]); tb[E] = vfma(I[E >> 2][E & 3], wp[E], tb[E]); });
    SB();
    const u32x4 Yr = {pk_bf(ta[0], ta[1]), pk_bf(ta[2], ta[3]), pk_bf(ta[4], ta[5]), pk_bf(ta[6], ta[7])};
    const u32x4 Yi = {pk_bf(tb[0], tb[1]), pk_bf(tb[2], tb[3]), pk_bf(tb[4], tb[5]), pk_bf(tb[6], tb[7])};
    SB();
    sfor<0, 2>([&](auto TT) { I[TT] = mmb(Yr, tG32[1][TT], z4); I[TT] = mmb(Yi, tG32[0][TT], I[TT]); });
    SB();
    sfor<0, 2>([&](auto TT) { P[TT] = mmb(Yr, tG32[0][TT], z4); Q[TT] = mmb(Yi, tG32[1][TT], z4); });
    SB(); mid();
    real(P, Q);
    sfor<0, 8>([&](auto E) { const unsigned t = tw[1][E >> 2][E & 3]; ta[E] = xmul<0, 0>(P[E >> 2][E & 3], t); tb[E] = xmul<1, 0>(P[E >> 2][E & 3], t); });
    SB();
    sfor<0, 8>([&](auto E) { const unsigned t = tw[1][E >> 2][E & 3]; ta[E] = xfma<1, 1>(I[E >> 2][E & 3], t, ta[E]); tb[E] = xfma<0, 0>(I[E >> 2][E & 3], t, tb[E]); });
    SB();
    sfor<0, 2>([&](auto TT) {
      LAS char* p = smem + L_SP + 8192 + (16 * TT + i16) * 128 + (((2 * g + (gq >> 1)) ^ (i16 >> 1)) * 16) + (gq & 1) * 8;
      *(LAS u32x2*)p = u32x2{pk_bf(ta[4 * TT], ta[4 * TT + 1]), pk_bf(ta[4 * TT + 2], ta[4 * TT + 3])};
      *(LAS u32x2*)(p + 4096) = u32x2{pk_bf(tb[4 * TT], tb[4 * TT + 1]), pk_bf(tb[4 * TT + 2], tb[4 * TT + 3])};
    }); };
  auto segC = [&](int T, auto&& mid) {
    const uint32_t vz = (i16 >> 1) * 147456 + (i16 & 1) * 64 + (gq & 1) * 32 + (gq >> 1) * 16, sz = oq_s(WS_ZT + (T == 288 ? 32 : T * 512) + g * 128);
    const u64 zmask = (T == 0 || T == 288) ? 0x0000ffff0000ffffull : ~0ull;
    sfor<0, 2>([&](auto TT) {
      LAS char* q0 = smem + L_SP + 8192 + (16 * TT + i16) * 128 + ((gq ^ (i16 >> 1)) * 16); LAS char* q1 = smem + L_SP + 8192 + (16 * TT + i16) * 128 + (((gq + 4) ^ (i16 >> 1)) * 16);
      uu[0][TT][0] = *(LAS u32x4*)q0; uu[0][TT][1] = *(LAS u32x4*)q1; uu[1][TT][0] = *(LAS u32x4*)(q0 + 4096); uu[1][TT][1] = *(LAS u32x4*)(q1 + 4096); });
    SB(); mid(); SB();
    sfor<0, 2>([&](auto TT) {
      const u32x4 r0 = uu[0][TT][0], r1_ = uu[0][TT][1], i0 = uu[1][TT][0], i1 = uu[1][TT][1];
      f32x4 P = mmb(tG64[0][0], r0, z4); P = mmb(tG64[0][1], r1_, P);
      f32x4 Q = mmb(tG64[1][0], i0, z4); Q = mmb(tG64[1][1], i1, Q);
      f32x4 I = mmb(tG64[1][0], r0, z4); I = mmb(tG64[1][1], r1_, I); I = mmb(tG64[0][0], i0, I); I = mmb(tG64[0][1], i1, I);
      unsigned ra = pk_bf(P.x - Q.x, P.y - Q.y), rb = pk_bf(P.z - Q.z, P.w - Q.w), ia = pk_bf(I.x, I.y), ib = pk_bf(I.z, I.w);
      auto sa = __builtin_amdgcn_permlane16_swap(ra, ia, false, false); auto sb = __builtin_amdgcn_permlane16_swap(rb, ib, false, false);
      const unsigned sa0 = sa[0], sa1 = sa[1], sb0 = sb[0], sb1 = sb[1];
      st16m(u32x4{sa0, sb0, sa1, sb1}, rs, vz, sz + TT * 8 * 147456, zmask);
    }); };
  auto dmas = [&](auto J) { uint32_t t[5]; constexpr uint32_t lb = ((J + 2) & 3) * BUF;
    sfor<0, 5>([&](auto I) { t[I] = lane_of<5 * J + I>(tabw); });
    sfor<0, 4>([&](auto I) { setm0<lb + I * 9216>(sw1); nop0(); dmago(r2, vo2a, t[I]); });
    setm0<lb + 8192>(sh9); dmagoh(r2, vo2b, t[4], hmask); };
  waitv_lb<15>();
  ST_(13);
  ld8c(pl, rs, vfh, 0);
  if (hf == 0) { tif<0>();
    u32x4 hd[4][2];
    Gelu ge, g2;
    pre(lbuf(F0{}));
    blocks(IC<4>{}, T1{}, T1{}, T1{}, lbuf(T1{}), [&](auto B, auto TT) { return hown[B][TT]; },
      [&](auto S) { if constexpr (S < 16) { ge.run<S>(p0, hown[3][0]); g2.run<S>(p1, hown[3][1]); }
        if constexpr (S == 16) st16c<0>(hown[3][0], rs, v16, sow4); if constexpr (S == 17) st16c<1024>(hown[3][1], rs, v16, sow4);
        if constexpr (S >= 17 && S < 25 && (S & 1)) sa = lane_of<8 + (S - 17) / 2>(tabh);
        if constexpr (S >= 18 && S < 26 && !(S & 1)) ld2h(hd[(S - 18) / 2], sa);
        if constexpr (S >= 25 && S < 35 && (S & 1)) dma2p(IC<3>{}, IC<(S - 25) / 2>{});
        if constexpr (S >= 26 && !(S & 1)) dma2g(IC<(S - 26) / 2>{}); });
    ST_(34);
    waitv_b<15>();
    ST_(14);
    hflag(3); check1(pl, 2);
    sfor<0, 4>([&](auto I) { pin(hc[I][0]); pin(hc[I][1]); });
    blocks(IC<4>{}, T1{}, F0{}, T1{}, lbuf(IC<2>{}), [&](auto B, auto TT) { return hc[B][TT]; },
      [&](auto S) { solo(T1{}, S); });
    ST_(35);
    waitv_b<9>();
    ST_(15);
    hflag(4); ld8c(pl, rs, vfh, 0);
    sfor<0, 4>([&](auto I) { pin(hd[I][0]); pin(hd[I][1]); });
    blocks(IC<4>{}, T1{}, F0{}, T1{}, lbuf(IC<3>{}), [&](auto B, auto TT) { return hd[B][TT]; },
      [&](auto S) { solo(IC<2>{}, S); });
    ST_(36);
    kill4(tF64); kill(tw[0][0]); kill(tw[0][1]); kill(p0[0]); kill(p0[1]); kill(p1[0]); kill(p1[1]);
  }
  tend<0>();
  if (oq_s(hf)) { tif<1>();
    if (PRIO & 2) __builtin_amdgcn_s_setprio(1);
    rdu(b);
    { Gelu ge, g2; u32x4 o0, o1;
      const uint32_t g2k = oq_s(g * 2048);
      sfor<0, 16>([&](auto S) { constexpr int C = (S >> 1) & 1, A = S & 1;
        if constexpr (S < 2) ld16c<A * 1024>(wt[A], rs, v16, WS_WT + b * 8192 + g2k);
        else if constexpr (S < 4) ld16<A * 1024>(tw[1][A], rt, v16, TAB_TW + 8192 + g2k);
        else ld16<C * 2048 + A * 1024>((S < 8 ? tF32 : S < 12 ? tG32 : tG64)[C][A], rt, v16, S < 8 ? TAB_F32 : S < 12 ? TAB_G32 : TAB_G64 + 2 * g2k);
        ge.run<S>(p0, o0); g2.run<S>(p1, o1); SB(); });
      if (dbl) { tif<2>(); ldu(T2); } tend<2>();
      st16c<0>(o0, rs, v16, sow4); st16c<1024>(o1, rs, v16, sow4); }
    waitv<25>(); pin4(tF64); pin(tw[0][0]); pin(tw[0][1]);
    segA(b); SB();
    dmas(IC<3>{});
    ST_(34);
    waitv_b<11>();
    ST_(14);
    check1(pl, 2); pin4(tF32); pin4(tG32); pin(tw[1][0]); pin(tw[1][1]);
    segB([&]() { dmas(IC<4>{}); });
    if (dbl) { tif<2>(); waitv<12>(); putu(); ldw(T2); kill4(hr[0]); } else { tif<3>(); ldh4(IC<3>{}); }
    tend<2>(); tend<3>();
    ST_(35);
    waitv_lb<7>();
    ST_(15);
    ld8c(pl, rs, vfh, 0); pin4(tG64);
    segC(b, [&]() { dmas(IC<5>{}); });
    ST_(36);
    if (dbl) { tif<2>();
      rdu(T2); segA(T2); SB();
      waitv_b<7>();
      check1(pl, 3);
      segB([&]() { dmas(IC<6>{}); });
      ldh4(IC<5>{});
      waitv_lb<9>();
      ld8c(pl, rs, vfh, 0);
      segC(T2, [&]() {});
      dmas(IC<7>{});
      ldh4(IC<6>{});
      kill4(hr[1]);
    } else { tif<3>(); ldh4(IC<4>{}); }
    tend<2>(); tend<3>();
    pre((dbl ? lbuf(IC<5>{}) : lbuf(IC<3>{})) + 18432);
    sfor<0, 9>([&](auto DT) { oo[DT] = z4; os[DT] = z4; });
    kill4(hx[0]); kill4(hx[1]);
    if (PRIO & 4) __builtin_amdgcn_s_setprio(0);
  }
  tend<1>();
  u32x4 zr[5], xv, bv;
  u32x2 xv8, bv8;
  uint32_t vta = 0, vtb = 0;
  auto dmat = [&](auto I) {
    if constexpr (I == 0) { const uint32_t t = TAB_TI + (zb0 * 4 + nt) * 9216 - ks * 36 * 36864, c = oq_v(l);
      vta = vo2a - 864 * (((w * 64 + c) * 3641) >> 16) + t; vtb = vo2b - 864 * (((512 + c) * 3641) >> 16) + t + 4 * sh9; }
    if constexpr (I < 4) dmak<3 * BUF + I * 9216, I * 36864>(rt, vta, sw1);
    else if constexpr (I == 4) dmakh<3 * BUF + 8192, 0>(rt, vtb, sh9, hmask);
    else if (ks < 2) { dmak<L_SP, 4 * 36864>(rt, vta, sw1); if (w == 0) dmak<L_SP + 8192, 4 * 36864>(rt, vtb, sh9); }
  };
  const uint32_t azw = (4 * w + 2 * hh + ((l >> 1) & 1)) * 288 + (((l >> 4) & 1) + 2 * ((l >> 2) & 1)) * 64 + ((l >> 3) & 1) * 32 + (l & 1) * 16;
  const uint32_t srow = sx + 288 * nt, vrow = (l >> 2) * 36864;
  auto shared = [&](auto J) {
    if constexpr (J == 8) bar0(); else waitv_b<9>();
    ST_(13 + J);
    if constexpr (J == 3) check1(pl, 3);
    if constexpr (J == 4) { if (tid == 0 && !dbl) setflag(ws, WS_FZ + b * 8, tag); ld8c(pl, rs, vfh, 0); }
    if constexpr (J == 5) check1(pl, 4);
    if constexpr (J == 5) { if (tid == 0 && dbl) setflag(ws, WS_FZ + b * 8, tag); }
    if constexpr (J == 7) { ld16c<0>(pu[0], rs, v16, WS_FZ); ld16c<1024>(pu[1], rs, v16, WS_FZ); }
    pin4(hr[J % 3]);
    SB();
    const uint32_t nxt = lbuf(IC<J + 1>{}) + hf * 18432;
    blocks(IC<2>{}, T1{}, F0{}, T1{}, (J < 5 && dbl) ? lbuf(J) + 18432 : nxt, [&](auto B, auto TT) { return hr[J % 3][B][TT]; },
      [&](auto S) {
        if constexpr (LAD & 2) { if constexpr (S == 17) prio<3>(); if constexpr (S == 4) prio<2>(); if constexpr (S == 9) prio<1>(); if constexpr (S == 13) prio<0>(); }
        if constexpr (J < 6) { if constexpr (S % 4 == 0) dma2p(IC<J + 3>{}, IC<S / 4>{}); if constexpr (S % 4 == 1) dma2g(IC<S / 4>{}); }
        if constexpr (J == 6) { if constexpr (S % 4 == 1 && S < 16) dmat(IC<S / 4>{}); if constexpr (S >= 16) dmat(IC<S - 12>{}); }
        if constexpr (J < 7) { if constexpr (S % 8 == 1) sa = lane_of<4 * (J + 2) + S / 8>(tabh); if constexpr (S % 4 == 3) ldh(IC<J + 2>{}, IC<S / 8>{}, IC<(S / 4) & 1>{}, sa); }
        if constexpr (J == 7) {
          if constexpr (S == 8) { waitv<0>(); check4(pu, WS_FZ); }
          if constexpr (S >= 8 && S < 13) { if constexpr (S == 8) sa = oq_s(WS_ZT + mt * 147456 + zb0 * 8192 + sw1); ld16c<0>(zr[S - 8], rs, v16, sa + (S == 12 && ks >= 2 ? 0 : (int)S - 8) * 8192); }
          if constexpr (S == 14) { ld16<0>(xv, rx, vrow + (l & 3) * 16, srow + 64 * ks); ld8<256>(xv8, rx, vrow + (l & 3) * 8, srow); }
          if constexpr (S == 16) { const u32x4 rb = mksrd(b2, 1152); ld16<0>(bv, rb, (l & 3) * 16, 288 * nt + 64 * ks); ld8<256>(bv8, rb, (l & 3) * 8, 288 * nt); } }
        if constexpr (J == 8 && S >= 13) { waitv<21 - S>(); pin(zr[S - 13]); *(LAS u32x4*)(smem + azw + (S - 13) * 9216) = zr[S - 13]; }
      });
    if constexpr (J < 5) { if (dbl) { tif<2>(); pin4(hx[J & 1]); blocks(IC<2>{}, T1{}, F0{}, T1{}, nxt, [&](auto B, auto TT) { return hx[J & 1][B][TT]; }, [&](auto) {}); } tend<2>(); }
  };
  if (!(oq_s(hf) && dbl)) { tif<5>(); shared(IC<3>{}); shared(IC<4>{}); }
  tend<5>();
  sfor<5, 9>(shared);
  { barl();
    ST_(22);
    const uint32_t az = oq_v(a2 + hf * 18432 + g * 64);
    u32x4 z[2][2];
    sfor<0, 2>([&](auto K) { z[K][0] = tr2(smem + az + K * 9216 + hf * 32, 4608); z[K][1] = tr2(smem + az + K * 9216 + (hf ^ 1) * 32, 4608); });
    const u32x4 z5 = tr2(smem + BUF + a2 + g * 64 + hf * 32, 4608);
    const uint32_t aw = oq_v((hf ? 0 : 2 * BUF) + g * 9216 + v16);
    SB();
    blocks(IC<2>{}, T1{}, F0{}, T1{}, L_SP, [&](auto B, auto TT) { return z[B][TT]; },
      [&](auto S) { if constexpr (S == 9) bar0(); if constexpr (S >= 10) *(LAS f32x4*)(smem + aw + (S - 10) * 1024) = os[S - 10]; });
    *(LAS f32x4*)(smem + aw + 8192) = os[8];
    ST_(23);
    if (ks < 2) blocks(T1{}, F0{}, F0{}, F0{}, 0, [&](auto B, auto TT) { return z5; }, [&](auto S) {});
    barl();
    ST_(24);
    const uint32_t ar = oq_v((hf ? 2 * BUF : 0) + g * 9216 + v16);
    sfor<0, 9>([&](auto DT) { oo[(DT + 2) % 9] += *(LAS f32x4*)(smem + ar + ((DT + 2) % 9) * 1024); });
  }
  auto pk1 = [&](float p, float q) { const unsigned v = pk_bf(p, q); return v < ~1u ? v : ~1u; };
  sfor<1, 4>([&](auto K) { const f32x4 p = oo[2 * K], q = oo[2 * K + 1];
    st16c<0>(u32x4{pk1(p.x, p.y), pk1(p.z, p.w), pk1(q.x, q.y), pk1(q.z, q.w)}, rs, v16, WS_PART + ((b & ~6) + ((ks + K) & 3) * 2) * 32768 + ((K - 1) * 8 + w) * 1024); });
  if (g != ks) st8c(u32x2{pk1(oo[8].x, oo[8].y), pk1(oo[8].z, oo[8].w)}, rs, l * 8, WS_PART + ((b & ~6) + g * 2) * 32768 + 24576 + (((g - ks) & 3) - 1) * 1024 + hf * 512);
  ST_(25);
  { u32x4 pp[3]; u32x2 p8[3];
    const uint32_t s8 = WS_PART + b * 32768 + (g == ks ? 24576 + hf * 512 : w * 1024);
#pragma nounroll
    for (unsigned spins = 0; spins < (1u << 16); ++spins) {
      sfor<0, 3>([&](auto K) { ld16c<0>(pp[K], rs, v16, WS_PART + b * 32768 + (K * 8 + w) * 1024); ld8c(p8[K], rs, l * 8, s8 + (g == ks ? K * 1024 : 0)); });
      waitv<0>();
      unsigned m = 0;
      sfor<0, 3>([&](auto K) { pin(pp[K]); pin(p8[K]); const unsigned a = pp[K].x, c = pp[K].y, d = pp[K].z, e = pp[K].w, f = p8[K].x, h = p8[K].y;
        m = max(max(max(m, a), max(c, d)), max(max(e, f), h)); });
      if (__all(m != ~0u)) break;
      __builtin_amdgcn_s_sleep(2);
    }
    ST_(26);
    LAS char* pt = smem + BUF + w * 3072;
    sfor<0, 3>([&](auto Q) { f32x4 r = oo[Q < 2 ? (int)Q : 8];
      sfor<0, 3>([&](auto K) { const unsigned a = Q == 2 ? p8[K].x : pp[K][2 * (Q & 1)], c = Q == 2 ? p8[K].y : pp[K][2 * (Q & 1) + 1];
        r += f32x4{lo_f(a), hi_f(a), lo_f(c), hi_f(c)}; });
      *(LAS f32x4*)(pt + Q * 1024 + i16 * 64 + gq * 16) = r; });
    pin(xv); pin(bv); pin(xv8); pin(bv8);
    LAS char* pq = pt + ((l >> 1) & 1) * 1024 + (l >> 2) * 64 + (l & 1) * 32;
    const f32x4 ra = *(LAS f32x4*)pq, rb = *(LAS f32x4*)(pq + 16), rc = *(LAS f32x4*)(pt + 2048 + v16);
    auto fin = [&](float a, float c, unsigned xx, unsigned bb) { return pk_bf(a + lo_f(xx) + lo_f(bb), c + hi_f(xx) + hi_f(bb)); };
    char* op = out + srow + vrow;
    *(u32x4*)(op + 64 * ks + (l & 3) * 16) = u32x4{fin(ra.x, ra.y, xv.x, bv.x), fin(ra.z, ra.w, xv.y, bv.y), fin(rb.x, rb.y, xv.z, bv.z), fin(rb.z, rb.w, xv.w, bv.w)};
    if (g == ks) *(u32x2*)(op + 256 + (l & 3) * 8) = u32x2{fin(rc.x, rc.y, xv8.x, bv8.x), fin(rc.z, rc.w, xv8.y, bv8.y)};
  }
  if (b == 0 && tid == 0) setflag(ws, WS_EPOCH, tag);
  asm volatile(".Lfk_end:");
}

extern "C" void kernel_launch(void* const* d_in, const int* in_sizes, int n_in, void* d_out, int out_size, void* d_ws, size_t ws_size, hipStream_t stream) {
  (void)hipFuncSetAttribute((const void*)fused_kernel, hipFuncAttributeMaxDynamicSharedMemorySize, LDS_BYTES);
  fused_kernel<<<256, 512, LDS_BYTES, stream>>>((const char*)d_in[0], (const char*)d_in[1], (const char*)d_in[2], (const char*)d_in[4], (const char*)d_in[5],
                                                (const char*)d_in[6], (const char*)d_in[7], (char*)d_out, (char*)d_ws);
}
```

```cpp
#include <hip/hip_runtime.h>
#include <stdint.h>
#include <type_traits>
#define LAD 3
#define PP 0
#define NW 63
#define PRIO 0

typedef unsigned short u16;
typedef short s16x4 __attribute__((ext_vector_type(4)));
typedef short s16x8 __attribute__((ext_vector_type(8)));
typedef __bf16 bf16x8 __attribute__((ext_vector_type(8)));
typedef __bf16 bf16x2 __attribute__((ext_vector_type(2)));
typedef _Float16 f16x8 __attribute__((ext_vector_type(8)));
typedef _Float16 f16x2 __attribute__((ext_vector_type(2)));
typedef float f32x4 __attribute__((ext_vector_type(4)));
typedef float f32x2 __attribute__((ext_vector_type(2)));
typedef unsigned u32x4 __attribute__((ext_vector_type(4)));
typedef unsigned u32x2 __attribute__((ext_vector_type(2)));
typedef unsigned long long u64;
#define LAS __attribute__((address_space(3)))
#define GAS __attribute__((address_space(1)))
#define DI __device__ __forceinline__

namespace tab {
constexpr double PI = 3.14159265358979323846;
constexpr double ccos(int a, int n) {
  a %= n; if (a < 0) a += n;
  if (2 * a > n) a = n - a;
  bool neg = false;
  if (4 * a > n) { a = n / 2 - a; neg = true; }
  if (4 * a == n) return 0.0;
  double x = 2 * PI * a / n, t = 1, s = 1;
  for (int i = 1; i <= 13; i++) { t *= -x * x / ((2 * i - 1) * (2 * i)); s += t; }
  return neg ? -s : s;
}
constexpr u16 to_bf(double v) { float f = (float)v; uint32_t u = __builtin_bit_cast(uint32_t, f); u += 0x7fffu + ((u >> 16) & 1u); return (u16)(u >> 16); }
constexpr u16 to_h(double v) { _Float16 h = (_Float16)v; return __builtin_bit_cast(u16, h); }
struct C576 { u16 b1[576]; u16 b2[576]; };
constexpr C576 gen576() { C576 r{}; for (int i = 0; i < 576; i++) { double c = ccos(i, 576) / 24.0; r.b1[i] = to_bf(c); r.b2[i] = to_bf(2 * c); } return r; }
constexpr C576 c576 = gen576();
constexpr int colk(int c) { return c == 0 ? 0 : c == 1 ? 288 : c < 288 ? (c & ~1) : ((c - 288) & ~1) + 1; }
constexpr int colp(int c) { return c < 2 ? 0 : (c & 1); }
struct Chunk { u16 v[9216]; };
constexpr Chunk genTF(int u) {
  Chunk r{};
  for (int i = 0; i < 1152; i++) { int t = i / 576, s = (i % 576) >> 6, l = i & 63, c = 32 * u + 16 * t + (l & 15), k = colk(c), o = 144 * colp(c), d = 32 * s + 8 * (l >> 4);
    for (int j = 0; j < 8; j++) r.v[i * 8 + j] = c576.b1[((d + j) * k + o) % 576]; }
  return r;
}
constexpr Chunk genTI(int q) {
  Chunk r{};
  for (int i = 0; i < 64; i++) { int c = 32 * (q >> 1) + (i & 31), k = colk(c), o = 144 * colp(c), d0 = 144 * (2 * (q & 1) + (i >> 5));
    if (c < 2) { for (int d = 0; d < 144; d++) r.v[i * 144 + d] = c576.b1[((d0 + d) * k + o) % 576]; }
    else { for (int d = 0; d < 144; d++) r.v[i * 144 + d] = c576.b2[((d0 + d) * k + o) % 576]; } }
  return r;
}
template <int I> constexpr Chunk cTF = genTF(I);
template <int I> constexpr Chunk cTI = genTI(I);
struct Small { u16 f64[4][2][2][512]; u16 f32[2][2][512]; u16 g32[2][2][512]; u16 g64[4][2][2][512]; };
constexpr Small genSmall() {
  Small r{};
  double c64[64] = {}; for (int i = 0; i < 64; i++) c64[i] = ccos(i, 64);
  const double r32 = 0.17677669529663688110;
  for (int l = 0; l < 64; l++) for (int j = 0; j < 8; j++) {
    int rr = l & 15, gq = l >> 4;
    int p2 = 16 * (j >> 2) + 4 * gq + (j & 3);
    for (int w = 0; w < 4; w++) for (int a = 0; a < 2; a++) {
      int e = (16 * w + rr) * (32 * a + 8 * gq + j), e1 = (16 * w + rr) * (32 * a + p2);
      r.f64[w][0][a][l * 8 + j] = to_h(c64[e1 & 63] / 8); r.f64[w][1][a][l * 8 + j] = to_h(c64[(e1 + 16) & 63] / 8);
      r.g64[w][0][a][l * 8 + j] = to_bf(c64[e & 63] / 8); r.g64[w][1][a][l * 8 + j] = to_bf(c64[(e + 48) & 63] / 8);
    }
    for (int t = 0; t < 2; t++) {
      int e = 2 * ((16 * t + rr) * p2);
      r.f32[0][t][l * 8 + j] = to_h(c64[e & 63] * r32); r.f32[1][t][l * 8 + j] = to_h(c64[(e + 16) & 63] * r32);
      r.g32[0][t][l * 8 + j] = to_bf(c64[e & 63] * r32); r.g32[1][t][l * 8 + j] = to_bf(c64[(e + 48) & 63] * r32);
    }
  }
  return r;
}
struct C2048 { u16 v[2048]; };
constexpr C2048 gen2048() { C2048 r{}; for (int i = 0; i < 2048; i++) r.v[i] = to_h(ccos(i, 2048)); return r; }
constexpr C2048 c2048 = gen2048();
struct Tw { u16 v[2][4][2][512]; };
constexpr Tw genTw() {
  Tw r{};
  for (int w = 0; w < 4; w++) for (int t = 0; t < 2; t++) for (int l = 0; l < 64; l++) for (int j = 0; j < 4; j++) {
    int e0 = (16 * t + 4 * (l >> 4) + j) * (16 * w + (l & 15)), e1 = (16 * t + (l & 15)) * (16 * w + 4 * (l >> 4) + j);
    r.v[0][w][t][l * 8 + 2 * j] = c2048.v[e0 & 2047]; r.v[0][w][t][l * 8 + 2 * j + 1] = c2048.v[(e0 + 1536) & 2047];
    r.v[1][w][t][l * 8 + 2 * j] = c2048.v[e1 & 2047]; r.v[1][w][t][l * 8 + 2 * j + 1] = c2048.v[(e1 + 1536) & 2047]; }
  return r;
}
constexpr Tw ctw = genTw();
constexpr int fblk(int nt, int J, int i) {
  if (J == 0) return 9 * nt + (nt & 1) + 2 * i;
  if (J == 1) { int pn = (nt + i) & 3; return 9 * pn + ((pn & 1) ? 0 : 8); }
  int q = 4 * (J - 2) + i, r = q / 7, m = q - 7 * r + 1, pn = (nt + (m >> 1)) & 3;
  return 9 * pn + (pn & 1) + 2 * r + (m & 1);
}
struct Adr { uint32_t w[4][4][64]; uint32_t h[4][2][64]; uint32_t v[4][576]; };
constexpr Adr genAdr() {
  Adr r{};
  for (int nt = 0; nt < 4; nt++) {
    for (int k = 0; k < 4; k++) for (int l = 0; l < 45; l++) r.w[nt][k][l] = fblk(nt, l / 5, l % 5 < 4 ? l % 5 : k) * 36864 + 288 * nt;
    for (int k = 0; k < 2; k++) for (int l = 0; l < 36; l++) r.h[nt][k][l] = fblk(nt, l / 4, (l + 2 * k) & 3) * 8192;
    for (int c = 0; c < 576; c++) { int k = c % 18; r.v[nt][c] = (c / 18) * 1152 + (k < 16 ? ((k + 4 * nt) & 15) : k) * 16 + nt * 36 * 36864; } }
  return r;
}
struct alignas(256) All { Chunk tf[18]; Chunk ti[36]; Small sm; Tw tw; Adr adr; };
template <int... I, int... J> constexpr All mk(std::integer_sequence<int, I...>, std::integer_sequence<int, J...>) { return All{{cTF<I>...}, {cTI<J>...}, genSmall(), ctw, genAdr()}; }
}
static __device__ const tab::All g_tab = tab::mk(std::make_integer_sequence<int, 18>{}, std::make_integer_sequence<int, 36>{});

constexpr uint32_t BUF = 36864, L_SP = 147456, L_B1 = L_SP, L_JUNK = L_SP + 2048, L_W = L_SP + 4096, LDS_BYTES = 163840;
constexpr uint32_t WS_EPOCH = 0, WS_FU = 4096, WS_FZ = 8192, WS_FH = 16384;
constexpr uint32_t WS_H = 1u << 20, WS_UT = 20u << 20, WS_WT = 23u << 20, WS_ZT = 26u << 20, WS_PART = 29u << 20, WS_END = 48u << 20;
constexpr uint32_t TAB_TF = 0, TAB_TI = 18 * 18432, TAB_F64 = 54 * 18432, TAB_F32 = TAB_F64 + 16384, TAB_G32 = TAB_F32 + 4096, TAB_G64 = TAB_G32 + 4096, TAB_TW = TAB_G64 + 16384, TAB_AW = TAB_TW + 16384, TAB_AH = TAB_AW + 4096, TAB_AV = TAB_AH + 2048, TAB_END = TAB_AV + 9216;

template <int I, int N, class Fn> DI void sfor(Fn&& f) { if constexpr (I < N) { f(std::integral_constant<int, I>{}); sfor<I + 1, N>(f); } }
template <int V> using IC = std::integral_constant<int, V>;

DI u32x4 mksrd(const void* p, uint32_t n) { return u32x4{(unsigned)(size_t)p, (unsigned)((size_t)p >> 32) & 0xffffu, n, 0x00020000u}; }
template <int IMM> DI void ld16(u32x4& d, u32x4 srd, uint32_t voff, uint32_t soff) { asm volatile("buffer_load_dwordx4 %0, %1, %2, %3 offen offset:%4" : "=v"(d) : "v"(voff), "s"(srd), "s"(soff), "i"(IMM) : "memory"); }
template <int IMM> DI void ld16c(u32x4& d, u32x4 srd, uint32_t voff, uint32_t soff) { asm volatile("buffer_load_dwordx4 %0, %1, %2, %3 offen offset:%4 sc1" : "=v"(d) : "v"(voff), "s"(srd), "s"(soff), "i"(IMM) : "memory"); }
template <int IMM> DI void ld8(u32x2& d, u32x4 srd, uint32_t voff, uint32_t soff) { asm volatile("buffer_load_dwordx2 %0, %1, %2, %3 offen offset:%4" : "=v"(d) : "v"(voff), "s"(srd), "s"(soff), "i"(IMM) : "memory"); }
DI void ld8c(u32x2& d, u32x4 srd, uint32_t voff, uint32_t soff) { asm volatile("buffer_load_dwordx2 %0, %1, %2, %3 offen sc1" : "=v"(d) : "v"(voff), "s"(srd), "s"(soff) : "memory"); }
DI void ld4(unsigned& d, u32x4 srd, uint32_t voff, uint32_t soff) { asm volatile("buffer_load_dword %0, %1, %2, %3 offen" : "=v"(d) : "v"(voff), "s"(srd), "s"(soff) : "memory"); }
DI void dma(u32x4 srd, uint32_t voff, uint32_t soff, uint32_t lds) { asm volatile("s_mov_b32 m0, %3\n\ts_nop 0\n\tbuffer_load_dwordx4 %0, %1, %2 offen lds" :: "v"(voff), "s"(srd), "s"(soff), "s"(lds) : "memory"); }
DI void dmah(u32x4 srd, uint32_t voff, uint32_t soff, uint32_t lds, u64 mask) {
  asm volatile("s_mov_b32 m0, %3\n\ts_mov_b64 exec, %4\n\tbuffer_load_dwordx4 %0, %1, %2 offen lds\n\ts_mov_b64 exec, -1" :: "v"(voff), "s"(srd), "s"(soff), "s"(lds), "s"(mask) : "memory"); }
template <uint32_t LDS, uint32_t SO> DI void dmak(u32x4 srd, uint32_t voff, uint32_t base) { uint32_t t;
  asm volatile("s_add_u32 m0, %3, %4\n\ts_mov_b32 %0, %5\n\tbuffer_load_dwordx4 %1, %2, %0 offen lds" : "=&s"(t) : "v"(voff), "s"(srd), "s"(base), "i"(LDS), "i"(SO) : "memory", "scc"); }
template <uint32_t LDS, uint32_t SO> DI void dmakh(u32x4 srd, uint32_t voff, uint32_t base, u64 mask) { uint32_t t;
  asm volatile("s_add_u32 m0, %3, %4\n\ts_mov_b32 %0, %5\n\ts_mov_b64 exec, %6\n\tbuffer_load_dwordx4 %1, %2, %0 offen lds\n\ts_mov_b64 exec, -1"
               : "=&s"(t) : "v"(voff), "s"(srd), "s"(base), "i"(LDS), "i"(SO), "s"(mask) : "memory", "scc"); }
template <int L> DI uint32_t lane_of(uint32_t t) { uint32_t r; asm volatile("v_readlane_b32 %0, %1, %2" : "=s"(r) : "v"(t), "i"(L)); return r; }
template <uint32_t LDS> DI void setm0(uint32_t base) { asm volatile("s_add_u32 m0, %0, %1" :: "s"(base), "i"(LDS) : "scc"); }
DI void dmago(u32x4 srd, uint32_t voff, uint32_t soff) { asm volatile("buffer_load_dwordx4 %0, %1, %2 offen lds" :: "v"(voff), "s"(srd), "s"(soff) : "memory"); }
DI void dmagoh(u32x4 srd, uint32_t voff, uint32_t soff, u64 mask) {
  asm volatile("s_mov_b64 exec, %3\n\tbuffer_load_dwordx4 %0, %1, %2 offen lds\n\ts_mov_b64 exec, -1" :: "v"(voff), "s"(srd), "s"(soff), "s"(mask) : "memory"); }
DI void nop4() { asm volatile("s_nop 4"); }
DI void nop0() { asm volatile("s_nop 0"); }
DI void touch(u32x4 srd, uint32_t voff, uint32_t soff, u64 mask) {
  asm volatile("s_mov_b32 m0, %3\n\ts_mov_b64 exec, %4\n\tbuffer_load_dword %0, %1, %2 offen lds\n\ts_mov_b64 exec, -1" :: "v"(voff), "s"(srd), "s"(soff), "s"(L_JUNK), "s"(mask) : "memory"); }
DI void code_touch(int part, int l) {
  u64 pc; uint32_t n;
  asm volatile("s_getpc_b64 %0\n.Lfk_begin:\n\ts_mov_b32 %1, .Lfk_end-.Lfk_begin" : "=s"(pc), "=s"(n));
  touch(u32x4{(unsigned)pc, (unsigned)(pc >> 32) & 0xffffu, n, 0x00020000u}, l * 128, part * 2048, 0xffffull); }
template <int IMM> DI void st16c(u32x4 d, u32x4 srd, uint32_t voff, uint32_t soff) {
  asm volatile("buffer_store_dwordx4 %0, %1, %2, %3 offen offset:%4 sc1\n\ts_nop 1" :: "v"(d), "v"(voff), "s"(srd), "s"(soff), "i"(IMM) : "memory"); }
DI void st16m(u32x4 d, u32x4 srd, uint32_t voff, uint32_t soff, u64 mask) {
  asm volatile("s_mov_b64 exec, %4\n\tbuffer_store_dwordx4 %0, %1, %2, %3 offen sc1\n\ts_mov_b64 exec, -1\n\ts_nop 0" :: "v"(d), "v"(voff), "s"(srd), "s"(soff), "s"(mask) : "memory"); }
DI void st8c(u32x2 d, u32x4 srd, uint32_t voff, uint32_t soff) {
  asm volatile("buffer_store_dwordx2 %0, %1, %2, %3 offen sc1\n\ts_nop 1" :: "v"(d), "v"(voff), "s"(srd), "s"(soff) : "memory"); }
template <int N> DI void waitv() { asm volatile("s_waitcnt vmcnt(%0)" :: "i"(N) : "memory"); }
template <int N> DI void waitv_b() { asm volatile("s_waitcnt vmcnt(%0)\n\ts_barrier" :: "i"(N) : "memory"); }
template <int N> DI void waitv_lb() { asm volatile("s_waitcnt vmcnt(%0) lgkmcnt(0)\n\ts_barrier" :: "i"(N) : "memory"); }
DI void bar0() { asm volatile("s_barrier" ::: "memory"); }
DI void barl() { asm volatile("s_waitcnt lgkmcnt(0)\n\ts_barrier" ::: "memory"); }
DI void pin(u32x4& v) { asm volatile("" : "+v"(v)); }
DI void pin(u32x2& v) { asm volatile("" : "+v"(v)); }
DI void pin(unsigned& v) { asm volatile("" : "+v"(v)); }
DI void fix(float& v) { asm volatile("" : "+v"(v)); }
DI void kill(u32x4& v) { asm volatile("" : "=v"(v)); }
DI void kill(f32x4& v) { asm volatile("" : "=v"(v)); }
DI uint32_t oq_s(uint32_t v) { asm volatile("" : "+s"(v)); return v; }
DI uint32_t oq_v(uint32_t v) { asm volatile("" : "+v"(v)); return v; }
template <int P> DI void prio() { asm volatile("s_setprio %0" :: "i"(P)); }
#define SB() __builtin_amdgcn_sched_barrier(0)
template <int C> DI void tif() { asm volatile("; @IF %0" :: "i"(C)); }
template <int C> DI void tend() { asm volatile("; @END %0" :: "i"(C)); }

DI float asf(unsigned u) { return __builtin_bit_cast(float, u); }
DI unsigned asu(float f) { return __builtin_bit_cast(unsigned, f); }
DI unsigned pk_bf(float a, float b) { f32x2 p = {a, b}; bf16x2 r = __builtin_convertvector(p, bf16x2); return __builtin_bit_cast(unsigned, r); }
DI unsigned pk_h(float a, float b) { f32x2 p = {a, b}; f16x2 r = __builtin_convertvector(p, f16x2); return __builtin_bit_cast(unsigned, r); }
DI float bfr(float a) { return asf(pk_bf(a, a) << 16); }
DI float lo_f(unsigned u) { return asf(u << 16); }
DI float hi_f(unsigned u) { return asf(u & 0xffff0000u); }
DI float dot2(unsigned a, unsigned b, float c) { return __builtin_amdgcn_fdot2_f32_bf16(__builtin_bit_cast(bf16x2, a), __builtin_bit_cast(bf16x2, b), c, false); }
DI float vfma(float a, float b, float c) { float d; asm("v_fma_f32 %0, %1, %2, %3" : "=v"(d) : "v"(a), "v"(b), "v"(c)); return d; }
DI float vmul(float a, float b) { float d; asm("v_mul_f32 %0, %1, %2" : "=v"(d) : "v"(a), "v"(b)); return d; }
DI float vfms(float a, float b, float c) { float d; asm("v_fma_f32 %0, -%1, %2, %3" : "=v"(d) : "v"(a), "v"(b), "v"(c)); return d; }
template <int H, int N> DI float xmul(float a, unsigned t) { float d;
  if constexpr (N) asm("v_fma_mix_f32 %0, -%1, %2, 0 op_sel:[0,%3,0] op_sel_hi:[0,1,0]" : "=v"(d) : "v"(a), "v"(t), "i"(H));
  else asm("v_fma_mix_f32 %0, %1, %2, 0 op_sel:[0,%3,0] op_sel_hi:[0,1,0]" : "=v"(d) : "v"(a), "v"(t), "i"(H));
  return d; }
template <int H, int N> DI float xfma(float a, unsigned t, float c) { float d;
  if constexpr (N) asm("v_fma_mix_f32 %0, -%1, %2, %3 op_sel:[0,%4,0] op_sel_hi:[0,1,0]" : "=v"(d) : "v"(a), "v"(t), "v"(c), "i"(H));
  else asm("v_fma_mix_f32 %0, %1, %2, %3 op_sel:[0,%4,0] op_sel_hi:[0,1,0]" : "=v"(d) : "v"(a), "v"(t), "v"(c), "i"(H));
  return d; }
template <int H, int D> DI void xfmah(unsigned& d, float a, unsigned t, float c) {
  if constexpr (D == 0) asm("v_fma_mixlo_f16 %0, %1, %2, %3 op_sel:[0,%4,0] op_sel_hi:[0,1,0]" : "=v"(d) : "v"(a), "v"(t), "v"(c), "i"(H));
  else asm("v_fma_mixhi_f16 %0, %1, %2, %3 op_sel:[0,%4,0] op_sel_hi:[0,1,0]" : "+v"(d) : "v"(a), "v"(t), "v"(c), "i"(H)); }
template <int CTRL> DI float dpp_add(float v) { return v + asf(__builtin_amdgcn_update_dpp(0, asu(v), CTRL, 0xf, 0xf, true)); }
DI float sum8(float v) { return dpp_add<0x141>(dpp_add<0x4E>(dpp_add<0xB1>(v))); }
DI f32x4 mmb(u32x4 a, u32x4 b, f32x4 c) { return __builtin_amdgcn_mfma_f32_16x16x32_bf16(__builtin_bit_cast(bf16x8, a), __builtin_bit_cast(bf16x8, b), c, 0, 0, 0); }
DI f32x4 mmh(u32x4 a, u32x4 b, f32x4 c) { return __builtin_amdgcn_mfma_f32_16x16x32_f16(__builtin_bit_cast(f16x8, a), __builtin_bit_cast(f16x8, b), c, 0, 0, 0); }
DI u32x4 tr2(LAS char* p, int second) {
  s16x4 a = __builtin_amdgcn_ds_read_tr16_b64_v4i16((LAS s16x4*)p);
  s16x4 b = __builtin_amdgcn_ds_read_tr16_b64_v4i16((LAS s16x4*)(p + second));
  s16x8 c = __builtin_shufflevector(a, b, 0, 1, 2, 3, 4, 5, 6, 7);
  return __builtin_bit_cast(u32x4, c);
}
struct Gelu { float a0, a1;
  template <int Q> DI void step(float v0, float v1, unsigned& o) {
    if constexpr (Q == 0) { a0 = __builtin_fmaf(v0 * v0, -0.10294324f, -2.3022082f); a1 = __builtin_fmaf(v1 * v1, -0.10294324f, -2.3022082f); }
    if constexpr (Q == 1) { a0 = __builtin_amdgcn_exp2f(v0 * a0); a1 = __builtin_amdgcn_exp2f(v1 * a1); }
    if constexpr (Q == 2) { a0 = __builtin_amdgcn_rcpf(1.0f + a0); a1 = __builtin_amdgcn_rcpf(1.0f + a1); }
    if constexpr (Q == 3) { o = pk_bf(v0 * a0, __builtin_fmaf(v1, a1, 0.0f)); }
  }
  template <int K> DI void run(const f32x4 (&p)[2], u32x4& o) {
    constexpr int P = K >> 2; const float v0 = p[P >> 1][2 * (P & 1)], v1 = p[P >> 1][2 * (P & 1) + 1];
    unsigned t = 0; step<K & 3>(v0, v1, t); if constexpr ((K & 3) == 3) o[P] = t; }
};
#define ST_(i) do {} while (0)
#define RLX_AGENT __ATOMIC_RELAXED, __HIP_MEMORY_SCOPE_AGENT
DI bool all_of(bool c) { return __builtin_amdgcn_ballot_w64(c) == ~0ull; }
DI bool lvl_ok(unsigned lo, unsigned hi, u64 tag, unsigned lvl) { return (hi == (unsigned)(tag >> 32)) & (lo - (unsigned)tag - lvl <= 7u - lvl); }
template <int N> DI void spin(const char* base, uint32_t off, u64 tag, unsigned lvl) {
#pragma nounroll
  for (unsigned spins = 0; spins < (1u << 17); ++spins) {
    bool ok = true;
#pragma unroll
    for (int k = 0; k < N; ++k) { u64 v = __hip_atomic_load((GAS u64*)(base + off + (k & 1) * 8 + (k >> 1) * 1024), RLX_AGENT); ok &= lvl_ok((unsigned)v, (unsigned)(v >> 32), tag, lvl); }
    if (__all(ok)) break;
    __builtin_amdgcn_s_sleep(1);
  }
}
DI void setflag(char* ws, uint32_t off, u64 v) { __hip_atomic_store((GAS u64*)(ws + off), v, RLX_AGENT); }

__global__ __launch_bounds__(512, 2) void fused_kernel(const char* __restrict__ x, const char* __restrict__ wr, const char* __restrict__ wi,
    const char* __restrict__ w1, const char* __restrict__ b1, const char* __restrict__ w2, const char* __restrict__ b2, char* out, char* ws) {
  extern __shared__ __attribute__((aligned(16))) char smem_[];
  LAS char* smem = (LAS char*)smem_;
  const int tid = threadIdx.x, l = tid & 63;
  const int w = __builtin_amdgcn_readfirstlane(tid >> 6), g = w & 3, hf = w >> 2;
  const int b = blockIdx.x, xcd = b & 7, jj = b >> 3;
  const int ks = xcd >> 1, mt = (xcd & 1) * 8 + (jj >> 2), nt = jj & 3, fj = ks * 4 + nt;
  const u32x4 rs = mksrd(ws, WS_END), rx = mksrd(x, 2048 * 1152), r1 = mksrd(w1, 576 * 9216), r2 = mksrd(w2, 4608 * 1152), rt = mksrd(&g_tab, TAB_END);
  const int i16 = l & 15, q4 = i16 >> 2, p4 = i16 & 3, hh = l >> 5, gq = l >> 4;
  const u64 hmask = (w & 1) ? 0xffffffff00000000ull : 0xffffffffull;
  const uint32_t v16 = l * 16;
  asm volatile("" :: "s"(ws), "s"(out), "s"(b1), "s"(b2), "s"(w2), "s"(wr), "s"(wi));
  ST_(0);
  u32x4 x0[18], x1[18];
  const uint32_t vx = (l >> 3) * 36864 + (l & 7) * 16;
  const uint32_t sx = (uint32_t)((32 * (g & 1) + 16 * hf) * 32 + 2 * mt + (g >> 1)) * 1152;
  const int odd = fj & 1, lone = odd ? 0 : 8;
  const int tileA = 18 * odd + 2 * (fj >> 1), tileC = 18 * odd + 16 + (nt >> 1);
  u32x2 ep; unsigned b1v;
  const uint32_t vo1 = (8 * hh + ((l >> 3) & 3)) * 9216 + (((l & 7) ^ (((l >> 4) & 3) << 1)) * 16);
  uint32_t v1a = 0, v1b = 0, sown = 0, sbox = 0;
  const uint32_t sw4 = oq_s(w * 4096), sw1 = oq_s(w * 1024), sh1 = oq_s((w >> 1) * 1024), sh9 = oq_s((w >> 1) * 9216);
  auto dma1 = [&](auto ST, auto I) {
    constexpr uint32_t so = 288 * (ST & 1) * 9216 + 128 * ((ST >> 1) - 1), lb = (ST & 3) * BUF;
    if constexpr (I < 4) dmak<lb + I * 1024, so + (16 * (I >> 1) + 4 * (I & 1)) * 9216>(r1, v1a, sw4);
    else dmakh<lb + 32768, so>(r1, v1b, sh1, hmask);
  };
  const uint32_t axb = 2 * BUF + g * 18432;
  const uint32_t axw = axb + ((l >> 2) & 1) * 1024 + (l & 3) * 256 + (((l >> 3) ^ (l & 7)) * 16);
  const uint32_t axr0 = axb + gq * 256 + ((i16 ^ gq) * 16), axr1 = axr0 ^ 64;
  auto rdx = [&](u32x4 (&d)[18], auto S) { d[S] = *(LAS u32x4*)(smem + ((S & 1) ? axr1 : axr0) + S * 1024); };
  u32x4 xr[2][9];
  f32x2 rn2[2], mb2[2];
  auto ldx = [&]() { sfor<0, 18>([&](auto C) { ld16<(C % 9) * 128>(xr[C / 9][C % 9], rx, vx, sx + (C / 9) * 294912); }); ld8c(ep, rs, 0, WS_EPOCH); };
  auto stats = [&](auto RB, auto NV, auto&& fill) {
    float sa[4] = {}, qa[4] = {};
    sfor<0, 9>([&](auto C) { fill(C);
      waitv<NV + 17 - 9 * RB - C>(); pin(xr[RB][C]);
      sfor<0, 4>([&](auto D) { const unsigned u = xr[RB][C][(int)D]; sa[D] = dot2(u, 0x3f803f80u, sa[D]); qa[D] = dot2(u, u, qa[D]); });
      SB(); });
    rn2[RB] = f32x2{(sa[0] + sa[1]) + (sa[2] + sa[3]), (qa[0] + qa[1]) + (qa[2] + qa[3])}; };
  auto chain = [&](auto RB) {
    const float mean = sum8(rn2[RB].x) * (1.0f / 576.0f);
    const float var = fmaxf(sum8(rn2[RB].y) * (1.0f / 576.0f) - mean * mean, 0.0f);
    const float sd = bfr(__builtin_amdgcn_sqrtf(bfr(bfr(var) + 1e-5f)));
    const float rn = __builtin_amdgcn_rcpf(sd), mb = -bfr(mean) * rn;
    rn2[RB] = f32x2{rn, rn}; mb2[RB] = f32x2{mb, mb}; };
  auto put = [&](auto RB, auto C) { *(LAS u32x4*)(smem + axw + RB * 128 + C * 2048) = xr[RB][C]; };
  auto norm = [&](auto RB, auto&& fill) {
    sfor<0, 9>([&](auto C) { f32x2 f[4];
      sfor<0, 4>([&](auto D) { const unsigned u = xr[RB][C][(int)D]; f[D] = f32x2{lo_f(u), hi_f(u)}; });
      SB();
      sfor<0, 4>([&](auto D) { f[D] = __builtin_elementwise_fma(f[D], rn2[RB], mb2[RB]); });
      SB();
      xr[RB][C] = u32x4{pk_bf(f[0].x, f[0].y), pk_bf(f[1].x, f[1].y), pk_bf(f[2].x, f[2].y), pk_bf(f[3].x, f[3].y)};
      fill(C);
      SB(); }); };
  using T1 = IC<1>; using F0 = IC<0>;
  u64 tag;
  auto mktag = [&]() { pin(ep);
    tag = ((u64)(unsigned)__builtin_amdgcn_readfirstlane(ep.y) << 32) | (unsigned)__builtin_amdgcn_readfirstlane(ep.x);
    tag += 0x9E3779B97F4A7C15ull; tag = (tag ^ (tag >> 30)) * 0xBF58476D1CE4E5B9ull; tag = (tag ^ (tag >> 27)) * 0x94D049BB133111EBull; tag = (tag ^ (tag >> 31)) & ~7ull; };
  f32x4 c0[2], c1[2], p0[2], p1[2];
  auto bias = [&](int unit, auto FT) { return *(LAS f32x4*)(smem + L_B1 + (32 * unit + 16 * FT + 4 * gq) * 4); };
  const int rho = 16 * (gq >> 1) + 4 * (gq & 1) + q4;
  uint32_t vo2a, vo2b;
  const uint32_t sh = WS_H + (mt * 144 * 4 + g) * 2048;
  uint32_t tabw, tabh;
  uint32_t sq = 0;
  auto dma2p = [&](auto J, auto I) { constexpr uint32_t lb = ((J + 2) & 3) * BUF;
    sq = lane_of<5 * J + I>(tabw); if constexpr (I < 4) setm0<lb + I * 9216>(sw1); else setm0<lb + 8192>(sh9); };
  auto dma2g = [&](auto I) { if constexpr (I < 4) dmago(r2, vo2a, sq); else dmagoh(r2, vo2b, sq, hmask); };
  const uint32_t vho = v16 + hf * 1024, vhs = v16 + (hf ^ 1) * 1024;
  const uint32_t vfh = WS_FH + ((b & ~24) + gq * 8) * 128;
  auto check1 = [&](u32x2& p, unsigned lvl) { pin(p); if (!all_of(lvl_ok(p.x, p.y, tag, lvl))) spin<1>(ws, vfh, tag, lvl); };
  auto check4 = [&](u32x4 (&p)[2], uint32_t base) { pin(p[0]); pin(p[1]);
    if (!all_of(lvl_ok(p[0].x, p[0].y, tag, 0) & lvl_ok(p[0].z, p[0].w, tag, 0) & lvl_ok(p[1].x, p[1].y, tag, 0) & lvl_ok(p[1].z, p[1].w, tag, 0))) spin<4>(ws, base + v16, tag, 0); };
  const int zb0 = ks < 2 ? 5 * ks : 4 * ks + 2;
  const int T2 = 256 + (xcd & 1) * 32 + jj;
  const bool dbl = ks == 3 && T2 <= 288;
  u32x4 tF64[2][2], tF32[2][2], tG32[2][2], tG64[2][2], tw[2][2], ur[2], wt[2], pu[2], hc[4][2], hown[4][2];
  u32x2 pl;

  const uint32_t a1 = rho * 128 + (((2 * hf) ^ ((rho >> 1) & 3)) * 32) + p4 * 8;
  uint32_t pa0 = 0, pa1 = 0;
  u32x4 fr[4];
  auto rd1 = [&](auto ST, auto S) {
    if constexpr (S == 0) { pa0 = oq_v(a1 + (ST & 3) * BUF); pa1 = oq_v(pa0 ^ 32); }
    return tr2(smem + ((S & 1) ? pa1 : pa0) + (S >> 1) * 4096, 1024); };
  auto ldu = [&](int T) { sfor<0, 2>([&](auto Q) { ld16c<Q * 1024>(ur[Q], rs, v16, WS_UT + (T == 288 ? 0 : T * 8192) + g * 2048); }); };
  auto putu = [&]() { sfor<0, 2>([&](auto Q) { pin(ur[Q]);
    *(LAS u32x4*)(smem + L_SP + (g >> 1) * 4096 + ((l >> 2) & 1) * 2048 + ((l >> 3) & 1) * 1024 + (l & 3) * 256 + (((8 * (g & 1) + 4 * Q + gq) ^ (l & 7)) * 16)) = ur[Q]; }); };
  auto ldt = [&](u32x4 (&t)[2][2], uint32_t so) { sfor<0, 2>([&](auto C) { sfor<0, 2>([&](auto A) { ld16<C * 2048 + A * 1024>(t[C][A], rt, v16, so); }); }); };
  auto ld2h = [&](u32x4 (&h)[2], uint32_t so) { ld16c<0>(h[0], rs, v16, so); ld16c<1024>(h[1], rs, v16, so); };
  uint32_t s4[4] = {};
  f32x4 bi[2];
  auto wtask = [&](int cl) {
    LAS char* p = smem + L_W + (l & 7) * 656 + (l & 1) * 2 + cl * 2 + (b >= 192 ? 12 : 0); unsigned v[8];
    sfor<0, 8>([&](auto J) { v[J] = *(LAS u16*)(p + (J >> 2) * 5248 + (J & 3) * 160); });
    const int kc = 72 * (b >> 6) + cl;
    st16c<0>(u32x4{v[0] | (v[1] << 16), v[2] | (v[3] << 16), v[4] | (v[5] << 16), v[6] | (v[7] << 16)}, rs,
             (uint32_t)(kc == 288 ? 288 : (kc & 1) ? 144 + (kc >> 1) : (kc >> 1)) * 8192 + (l & 7) * 16,
             WS_WT + ((b >> 1) & 3) * 2048 + ((b >> 5) & 1) * 1024 + ((b >> 3) & 3) * 256 + (b & 1) * 128); };
  auto stage = [&](auto ST, auto HF) {
    constexpr int R = ST >> 1, KB = 9 * (ST & 1);
    if constexpr (ST == 9) bar0(); else waitv_b<(ST == 2 ? 13 + 2 * HF : ST == 3 ? 13 + HF : ST == 5 ? 6 : ST == 8 ? 9 : 7)>();
    ST_(3 + ST);
    if constexpr (ST == 3) { pin(tabw); pin(tabh); pin(vo2a); pin(vo2b); tabh += sh + 36 * 8192 * ks; }
    if constexpr (ST == 5) { if (tid == 0) setflag(ws, WS_FU + b * 8, tag); }
    if constexpr (ST == 7 || ST == 9) { if (tid == 0) setflag(ws, WS_FH + b * 128, tag + (ST - 5) / 2); }
    if constexpr (ST <= 4) sfor<0, 3>([&](auto S) { fr[(18 * ST + S) & 3] = rd1(ST, S); });
    if constexpr (ST == 2) { bi[0] = bias(odd + hf, IC<0>{}); bi[1] = bias(odd + hf, IC<1>{}); }
    if constexpr (ST == 8) ld8c(pl, rs, vfh, 0);
    if constexpr (ST == 9) {
      if (hf) { tif<1>(); ldt(tF64, TAB_F64 + g * 4096); ld16<0>(tw[0][0], rt, v16, TAB_TW + g * 2048); ld16<1024>(tw[0][1], rt, v16, TAB_TW + g * 2048); }
      else { tif<0>(); sfor<0, 4>([&](auto I) { ld2h(hc[I], s4[I]); }); }
      tend<0>(); tend<1>();
    }
    Gelu ge; u32x4 o;
    SB();
    sfor<0, 18>([&](auto S) {
      constexpr int G = 18 * ST + S, FT = S & 1;
      if constexpr (LAD & 1) { if constexpr (S == 0) prio<3>(); if constexpr (S == 5) prio<2>(); if constexpr (S == 10) prio<1>(); if constexpr (S == 14) prio<0>(); }
      if constexpr (S >= 2 && !(S & 1)) { __builtin_amdgcn_s_waitcnt(0xC27F); SB(); }
      const u32x4 a = fr[G & 3];
      if constexpr ((ST & 1) == 0 && S < 2) c0[FT] = mmb(a, x0[KB + (S >> 1)], bi[FT]); else c0[FT] = mmb(a, x0[KB + (S >> 1)], c0[FT]);
      SB();
      if constexpr (S + 3 < 18) fr[(G + 3) & 3] = rd1(ST, IC<S + 3>{});
      else if constexpr (ST > 3 && ST != 9) fr[(G + 3) & 3] = rd1(IC<ST + 1>{}, IC<S + 3 - 18>{});
      SB();
      if constexpr ((ST & 1) == 0 && S < 2) c1[FT] = mmb(a, x1[KB + (S >> 1)], bi[FT]); else c1[FT] = mmb(a, x1[KB + (S >> 1)], c1[FT]);
      SB();
      if constexpr (HF != 1 && S >= 1 && S < 17) { if constexpr (ST & 1) ge.run<S - 1>(p1, o); else ge.run<S - 1>(p0, o); }
      if constexpr (ST == 2 && S % 3 == 2 && S / 3 < 5) dma1(IC<5>{}, IC<S / 3>{});
      if constexpr (S % 3 == 1 && S / 3 < 5) { if constexpr (ST < 7) dma1(IC<(ST == 2 ? 4 : ST + 3)>{}, IC<S / 3>{}); else dma2g(IC<S / 3>{}); }
      if constexpr (ST >= 7 && S % 3 == 0 && S / 3 < 5) dma2p(IC<ST - 7>{}, IC<S / 3>{});
      if constexpr (HF == 1 && ST == 3 && (S == 3 || S == 9)) wtask(8 * g + (S == 3 ? 0 : 32) + (l >> 3));
      if constexpr (ST >= 5 && ST < 8 && S == 15) { st16c<0>(u32x4{~0u, ~0u, ~0u, ~0u}, rs, v16, sbox + (ST - 5) * 8192);
        if constexpr (ST == 5) { if (w < 3) st16c<0>(u32x4{~0u, ~0u, ~0u, ~0u}, rs, v16, sbox + 24576); } }
      if constexpr (HF == 1 && ST == 3 && S == 15) { if (g == 0 || (g == 1 && b >= 192)) wtask(g == 0 ? 64 + (l >> 3) : 72); }
      if constexpr (ST == 7 && S == 14) { ld16c<0>(pu[0], rs, v16, WS_FU); ld16c<1024>(pu[1], rs, v16, WS_FU); }
      if constexpr (ST == 8 && S == 8) { waitv<6>(); if (hf) { tif<1>(); check4(pu, WS_FU); ldu(b); } tend<1>(); }
      if constexpr (ST == 8 && S == 17) { waitv<5>(); check1(pl, 1); }
      if constexpr (ST == 9 && S == 15) { if (hf) { tif<1>(); waitv<12>(); putu(); } tend<1>(); }
      if constexpr (HF != 1 && S == 17) { st16c<(ST & 1) * 1024>(o, rs, v16, R == 1 ? sh + (9 * fj + lone) * 8192 : sown + 2 * (R - 2) * 8192); if constexpr (R > 1) hown[R - 2][ST & 1] = o; }
      if constexpr ((ST & 1) && ST < 9 && S == 17) { bi[0] = bias(odd + 2 * R + hf, IC<0>{}); bi[1] = bias(odd + 2 * R + hf, IC<1>{}); }
      if constexpr (ST == 8 && S == 17) sfor<0, 4>([&](auto I) { s4[I] = lane_of<4 + I>(tabh); });
      SB();
    });
    if constexpr (ST & 1) { p0[0] = c0[0]; p0[1] = c0[1]; p1[0] = c1[0]; p1[1] = c1[1]; }
  };
  auto fill0 = [&](auto S) {
    if constexpr (S == 0) { v1a = oq_v(vo1 + 32 * w * 9216 + (288 * fj + 32 * odd) * 2); v1b = oq_v(vo1 + (256 + 16 * hf + 4 * ((w >> 1) & 1)) * 9216 + (288 * fj + 32 * odd) * 2);
      ST_(30); barl(); ST_(31); }
    else if constexpr (S < 6) dma1(IC<2>{}, IC<S - 1>{});
    else if constexpr (S < 11) dma1(IC<3>{}, IC<S - 6>{});
    else if constexpr (S < 13) {
      const int j = (l * 13) >> 7;
      dmah(mksrd(S == 12 ? wi : wr, 2048 * 578), j * 36992 + (l - 10 * j) * 16, ((8 * (b & 7) + w + 256 * ((b >> 3) & 7)) * 578 + (b >= 192 ? 420 : 144 * (b >> 6))) & ~3u,
           L_W + (S - 11) * 5248 + w * 656, (1ull << 40) - 1); }
    else if constexpr (S == 13) { ld4(tabw, rt, l * 4, TAB_AW + nt * 1024 + (w >> 1) * 256); ld4(tabh, rt, l * 4, TAB_AH + nt * 512 + hf * 256); }
    else if constexpr (S == 14) { ld4(vo2a, rt, l * 4, TAB_AV + ks * 2304 + w * 256); ld4(vo2b, rt, l * 4, TAB_AV + ks * 2304 + 2048); }
    else { constexpr int N = (324 - 4 * NW + 7) / 8; touch(r2, (jj * 324 + 4 * NW + w * N + l) * 128, ks * 1152 * 1152, (1ull << N) - 1);
      touch(rt, l < 6 ? TAB_TI + zb0 * 36864 + ((jj * 8 + w) * 6 + l) * 128 : TAB_F64 + ((jj * 8 + w) * 2 + l - 6) * 128, 0, 0xffull); } };
  const f32x4 z4 = {0, 0, 0, 0};
  const uint32_t su = WS_UT + (2 * mt + (g >> 1)) * 256 + (g & 1) * 128;
  auto ust = [&](const f32x4 (&u)[4], uint32_t so) { f32x4 a, c; const float sg = odd ? -1.0f : 1.0f;
    sfor<0, 4>([&](auto J) { a[(int)J] = __builtin_fmaf(u[2][(int)J], sg, u[0][(int)J]); c[(int)J] = __builtin_fmaf(u[3][(int)J], sg, u[1][(int)J]); });
    st16c<0>(u32x4{pk_h(a.x, a.y), pk_h(a.z, a.w), pk_h(c.x, c.y), pk_h(c.z, c.w)}, rs, (i16 >> 1) * 8192 + (i16 & 1) * 64 + gq * 16,
             so); };
  if (hf == 0) { tif<0>();
    ldx(); ld4(b1v, mksrd(b1, 9216), tid * 4, 576 * fj);
    bar0();
    const int i1 = jj * 144 + g * 36 + l;
    const uint32_t vt1 = oq_v((i1 >> 3) * 9216 + ((i1 >> 2) & 1) * 1152 + ((i1 >> 1) & 1) * 640 + (i1 & 1) * 128);
    sfor<0, 2>([&](auto RB) { stats(RB, IC<2>{}, [&](auto) {}); ST_(1 + RB);
      if constexpr (RB == 1 && PP == 0) touch(r1, vt1, ks * 2304, (1ull << 36) - 1);
      chain(RB); norm(RB, [&](auto C) { put(RB, C); }); });
    waitv<PP == 0>(); pin(b1v);
    if (tid < 144) *(LAS f32x2*)(smem + L_B1 + tid * 8) = f32x2{lo_f(b1v), hi_f(b1v)};
    ST_(28);
    barl();
    sfor<0, 18>([&](auto S) { rdx(x0, S); });
    if constexpr (PP == 1) touch(r1, vt1, ks * 2304, (1ull << 36) - 1);
    const uint32_t vo = (8 * ((l >> 4) & 1) + 4 * hh + ((l >> 2) & 3)) * 9216 + (((l & 3) ^ (((l >> 4) & 1) << 1)) * 16), so = (288 * fj + 32 * lone) * 2 + 16 * g * 9216;
    sfor<0, 9>([&](auto Q) { if constexpr (Q == 2) { barl(); ST_(3); }
      dma(r1, vo, so + 64 * Q * 9216, (g + 4 * Q) * 1024);
      if constexpr (Q < 4) dma(rt, v16, TAB_TF + tileA * 9216 + (g + 4 * Q) * 1024, BUF + (g + 4 * Q) * 1024); });
    if (g < 2) dma(rt, v16, TAB_TF + tileA * 9216 + (g + 16) * 1024, BUF + (g + 16) * 1024);
    if (ks == 2) { sfor<0, 2>([&](auto Q) { dma(rt, v16, TAB_TF + tileC * 9216 + (g + 4 * Q) * 1024, BUF + 18432 + (g + 4 * Q) * 1024); });
      if (g == 3) dma(rt, v16, TAB_TF + tileC * 9216 + 8192, BUF + 18432 + 8192); }
    if constexpr (PP == 2) touch(r1, vt1, ks * 2304, (1ull << 36) - 1);
    touch(r1, vt1, ks * 2304 + 256, (1ull << 36) - 1);
    touch(r2, (jj * 324 + g * NW + l) * 128, ks * 1152 * 1152, (1ull << NW) - 1);
    mktag();
    const f32x4 bl[2] = {bias(lone, F0{}), bias(lone, T1{})};
    const uint32_t al0 = oq_v(rho * 64 + (gq & 1) * 32 + p4 * 8), al1 = oq_v(al0 ^ 32);
    auto rd = [&](auto S) { return tr2(smem + ((S & 1) ? al1 : al0) + (S >> 1) * 2048, 512); };
    waitv<2 + (PP == 2)>(); ST_(29); bar0(); ST_(4);
    sfor<0, 3>([&](auto S) { fr[S] = rd(S); });
    sfor<0, 18>([&](auto S) { rdx(x1, S); }); SB();
    sfor<0, 36>([&](auto S) { const u32x4 a = fr[S & 3];
      p0[S & 1] = mmb(a, x0[S >> 1], S < 2 ? bl[S & 1] : p0[S & 1]); SB();
      if constexpr (S + 3 < 36) fr[(S + 3) & 3] = rd(IC<S + 3>{});
      SB();
      p1[S & 1] = mmb(a, x1[S >> 1], S < 2 ? bl[S & 1] : p1[S & 1]); SB();
      if constexpr (S >= 4 && !(S & 1)) fill0(IC<(S - 4) / 2>{});
      SB(); });
    ST_(32);
    if (ks == 2) { f32x4 uc[4]; const uint32_t at = oq_v(BUF + 18432 + v16), suC = oq_s(su + tileC * 65536);
      u32x4 tc[9];
      sfor<0, 9>([&](auto I) { tc[I] = *(LAS u32x4*)(smem + at + I * 1024); });
      SB();
      sfor<0, 9>([&](auto I) { const u32x4 t = tc[I];
        uc[0] = mmb(x0[I], t, I ? uc[0] : z4); uc[2] = mmb(x0[I + 9], t, I ? uc[2] : z4); uc[1] = mmb(x1[I], t, I ? uc[1] : z4); uc[3] = mmb(x1[I + 9], t, I ? uc[3] : z4); });
      ust(uc, suC); }
    ST_(33);
    stage(IC<2>{}, IC<0>{}); stage(IC<3>{}, IC<0>{});
  } else { tif<1>();
    if (w == 7) code_touch(jj, l);
    if (w == 6) { const int i = jj * 9 + l - 18;
      touch(rt, l < 18 ? (18 * (l & 1) + 4 * ks) * 9216 + (jj * 9 + (l >> 1)) * 128 : 16 * 9216 + (i >= 144 ? 147456 : 0) + i * 128, TAB_TF, ks == 2 ? (1ull << 27) - 1 : (1ull << 18) - 1); }
    if (w == 5) touch(r1, (jj * 18 + (l >> 1)) * 9216 + 512 + (l & 1) * 1152, ks * 2304, (1ull << 36) - 1);
    bar0();
    ldx();
    stats(F0{}, T1{}, [&](auto) {}); chain(F0{}); SB();
    ST_(1);
    bar0();
    ST_(2);
    norm(F0{}, [&](auto C) { rdx(x0, IC<2 * C>{}); rdx(x0, IC<2 * C + 1>{}); });
    ST_(28);
    barl();
    ST_(3);
    stats(T1{}, T1{}, [&](auto C) { put(F0{}, C); }); chain(T1{}); norm(T1{}, [&](auto C) { put(T1{}, C); });
    waitv<0>(); mktag();
    ST_(29); barl(); ST_(4);
    const uint32_t at = oq_v(BUF + v16), suA = oq_s(su + tileA * 65536);
    f32x4 ua[2][4];
    auto rd = [&](auto S) { return *(LAS u32x4*)(smem + at + (S & 1) * 9216 + (S >> 1) * 1024); };
    sfor<0, 2>([&](auto S) { fr[S] = rd(S); });
    sfor<0, 9>([&](auto S) { rdx(x1, S); rdx(x1, IC<S + 9>{}); }); SB();
    sfor<0, 18>([&](auto S) { constexpr int I = S >> 1, TT = S & 1; const u32x4 t = fr[S & 3];
      ua[TT][0] = mmb(x0[I], t, I ? ua[TT][0] : z4); SB();
      if constexpr (S + 2 < 18) fr[(S + 2) & 3] = rd(IC<S + 2>{});
      SB();
      ua[TT][2] = mmb(x0[I + 9], t, I ? ua[TT][2] : z4); SB();
      if constexpr (S >= 2) fill0(IC<S - 2>{});
      SB();
      ua[TT][1] = mmb(x1[I], t, I ? ua[TT][1] : z4); ua[TT][3] = mmb(x1[I + 9], t, I ? ua[TT][3] : z4); SB(); });
    ST_(32);
    ust(ua[0], suA); ust(ua[1], suA + 65536);
    ST_(33);
    if (PRIO & 1) __builtin_amdgcn_s_setprio(1);
    stage(IC<2>{}, IC<1>{}); stage(IC<3>{}, IC<1>{});
  }
  tend<0>(); tend<1>();
  sown = oq_s(sh + (9 * fj + odd + hf) * 8192); sbox = oq_s(WS_PART + b * 32768 + w * 1024);
  sfor<4, 10>([&](auto ST) { stage(ST, IC<2>{}); });

  f32x4 oo[9], os[9];
  u32x4 f6[6];
  const uint32_t a2 = (4 * gq + q4) * 288 + p4 * 8;
  uint32_t pb = 0;
  auto rd2 = [&](auto S) { f6[S % 6] = tr2(smem + pb + (S / 9) * 9216 + (S % 9) * 32, 4608); };
  auto pre = [&](uint32_t cur) { pb = oq_v(a2 + cur); sfor<0, 4>(rd2); SB(); };
  auto blocks = [&](auto NB, auto NEXT, auto ZERO, auto BOTH, uint32_t nxt, auto&& hfrag, auto&& fill) {
    sfor<0, 9 * NB>([&](auto S) {
      constexpr int B = S / 9, DT = S % 9;
      if constexpr (!(S & 1) && (NEXT || S + 4 < 9 * NB)) { __builtin_amdgcn_s_waitcnt(0xC47F); SB(); }
      const u32x4 a = f6[S % 6];
      oo[DT] = mmb(a, hfrag(IC<B>{}, F0{}), (ZERO && B == 0) ? z4 : oo[DT]);
      SB();
      if constexpr (S + 4 < 9 * NB) rd2(IC<S + 4>{});
      else if constexpr (NEXT) { if constexpr (S + 4 == 9 * NB) pb = oq_v(a2 + nxt); rd2(IC<S + 4 - 9 * NB>{}); }
      SB();
      if constexpr (BOTH) os[DT] = mmb(a, hfrag(IC<B>{}, T1{}), (ZERO && B == 0) ? z4 : os[DT]);
      SB();
      fill(S);
      SB();
    }); };
  auto lbuf = [&](auto J) { return (uint32_t)(((J + 2) & 3) * BUF); };
  const uint32_t sow4 = sown + 6 * 8192;
  u32x4 hr[3][2][2], hx[2][2][2];
  uint32_t sa = 0, sb = 0;
  auto ldh = [&](auto J, auto K, auto TT, uint32_t so) { ld16c<0>(hr[J % 3][K][TT], rs, TT ? vhs : vho, so); };
  auto ldh4 = [&](auto J) { sa = lane_of<4 * J>(tabh); sb = lane_of<4 * J + 1>(tabh); nop4(); sfor<0, 4>([&](auto I) { ldh(J, IC<I / 2>{}, IC<I & 1>{}, I < 2 ? sa : sb); }); };
  auto solo = [&](auto J, auto S) {
    if constexpr (S % 4 == 0 && S < 20) dma2p(IC<J + 3>{}, IC<S / 4>{});
    if constexpr (S % 4 == 1 && S < 20) dma2g(IC<S / 4>{});
    if constexpr (S == 19 || S == 28) { sa = lane_of<4 * (J + 2) + (S - 19) / 4>(tabh); sb = lane_of<4 * (J + 2) + (S - 19) / 4 + 1>(tabh); }
    if constexpr (S >= 21 && S < 29 && (S & 1)) ldh(IC<J + 2>{}, IC<(S - 21) / 4>{}, IC<((S - 21) / 2) & 1>{}, S < 25 ? sa : sb);
    if constexpr (S == 30) { if (dbl) { tif<2>(); ld2h(hx[J & 1][0], sa); ld2h(hx[J & 1][1], sb); } tend<2>(); } };
  auto pin4 = [&](u32x4 (&t)[2][2]) { pin(t[0][0]); pin(t[0][1]); pin(t[1][0]); pin(t[1][1]); };
  auto kill4 = [&](u32x4 (&t)[2][2]) { kill(t[0][0]); kill(t[0][1]); kill(t[1][0]); kill(t[1][1]); };
  auto hflag = [&](int lvl) { if (tid == 0) setflag(ws, WS_FH + b * 128, tag + lvl); };
  u32x4 uu[2][2][2], Br, Bi;
  auto rdu = [&](int T) { LAS char* pi = smem + L_SP + 2048 + gq * 256 + ((i16 ^ (4 + gq)) * 16); LAS char* pr = T == 288 ? pi : smem + L_SP + gq * 256 + ((i16 ^ gq) * 16);
    sfor<0, 2>([&](auto TT) { sfor<0, 2>([&](auto A) { uu[0][TT][A] = *(LAS u32x4*)(pr + TT * 4096 + A * 1024); uu[1][TT][A] = *(LAS u32x4*)(pi + TT * 4096 + A * 1024); }); }); };
  auto ldw = [&](int T) { sfor<0, 2>([&](auto P) { ld16c<P * 1024>(wt[P], rs, v16, WS_WT + T * 8192 + g * 2048); }); };
  auto real = [&](f32x4 (&P)[2], const f32x4 (&Q)[2]) { SB(); sfor<0, 8>([&](auto E) { float r = P[E >> 2][E & 3] - Q[E >> 2][E & 3]; fix(r); P[E >> 2][E & 3] = r; }); SB(); };
  auto segA = [&](int T) {
    if (T == 0 || T == 288) sfor<0, 2>([&](auto TT) { sfor<0, 2>([&](auto A) { uu[1][TT][A] = u32x4{0, 0, 0, 0}; }); });
    unsigned br[4], bq[4]; f32x4 P[2], Q[2], I[2]; float ta[8], tb[8];
    sfor<0, 2>([&](auto TT) { I[TT] = mmh(uu[0][TT][0], tF64[1][0], z4); I[TT] = mmh(uu[0][TT][1], tF64[1][1], I[TT]);
      I[TT] = mmh(uu[1][TT][0], tF64[0][0], I[TT]); I[TT] = mmh(uu[1][TT][1], tF64[0][1], I[TT]); });
    SB();
    sfor<0, 2>([&](auto TT) { P[TT] = mmh(uu[0][TT][0], tF64[0][0], z4); P[TT] = mmh(uu[0][TT][1], tF64[0][1], P[TT]);
      Q[TT] = mmh(uu[1][TT][0], tF64[1][0], z4); Q[TT] = mmh(uu[1][TT][1], tF64[1][1], Q[TT]); });
    real(P, Q);
    sfor<0, 8>([&](auto E) { const unsigned t = tw[0][E >> 2][E & 3]; ta[E] = xmul<0, 0>(P[E >> 2][E & 3], t); tb[E] = xmul<1, 1>(P[E >> 2][E & 3], t); });
    SB();
    sfor<0, 8>([&](auto X) { constexpr int E = (X & 3) * 2 + (X >> 2); const unsigned t = tw[0][E >> 2][E & 3];
      xfmah<1, E & 1>(br[E / 2], I[E >> 2][E & 3], t, ta[E]); xfmah<0, E & 1>(bq[E / 2], I[E >> 2][E & 3], t, tb[E]); if constexpr (X == 3) SB(); });
    Br = u32x4{br[0], br[1], br[2], br[3]}; Bi = u32x4{bq[0], bq[1], bq[2], bq[3]}; pin(Br); pin(Bi); };
  auto segB = [&](auto&& mid) {
    float ta[8], tb[8], wp[8], wq[8]; f32x4 P[2], Q[2], I[2];
    pin(wt[0]); pin(wt[1]);
    sfor<0, 2>([&](auto TT) { I[TT] = mmh(tF32[1][TT], Br, z4); I[TT] = mmh(tF32[0][TT], Bi, I[TT]); });
    SB();
    sfor<0, 2>([&](auto TT) { P[TT] = mmh(tF32[0][TT], Br, z4); Q[TT] = mmh(tF32[1][TT], Bi, z4); });
    SB();
    sfor<0, 8>([&](auto E) { const unsigned a = wt[E >> 2][(E >> 1) & 1], c = wt[E >> 2][2 + ((E >> 1) & 1)]; wp[E] = (E & 1) ? hi_f(a) : lo_f(a); wq[E] = (E & 1) ? hi_f(c) : lo_f(c); });
    real(P, Q);
    sfor<0, 8>([&](auto E) { ta[E] = vmul(P[E >> 2][E & 3], wp[E]); tb[E] = vmul(P[E >> 2][E & 3], wq[E]); });
    SB();
    sfor<0, 8>([&](auto E) { ta[E] = vfms(I[E >> 2][E & 3], wq[E], ta[E]); tb[E] = vfma(I[E >> 2][E & 3], wp[E], tb[E]); });
    SB();
    const u32x4 Yr = {pk_bf(ta[0], ta[1]), pk_bf(ta[2], ta[3]), pk_bf(ta[4], ta[5]), pk_bf(ta[6], ta[7])};
    const u32x4 Yi = {pk_bf(tb[0], tb[1]), pk_bf(tb[2], tb[3]), pk_bf(tb[4], tb[5]), pk_bf(tb[6], tb[7])};
    SB();
    sfor<0, 2>([&](auto TT) { I[TT] = mmb(Yr, tG32[1][TT], z4); I[TT] = mmb(Yi, tG32[0][TT], I[TT]); });
    SB();
    sfor<0, 2>([&](auto TT) { P[TT] = mmb(Yr, tG32[0][TT], z4); Q[TT] = mmb(Yi, tG32[1][TT], z4); });
    SB(); mid();
    real(P, Q);
    sfor<0, 8>([&](auto E) { const unsigned t = tw[1][E >> 2][E & 3]; ta[E] = xmul<0, 0>(P[E >> 2][E & 3], t); tb[E] = xmul<1, 0>(P[E >> 2][E & 3], t); });
    SB();
    sfor<0, 8>([&](auto E) { const unsigned t = tw[1][E >> 2][E & 3]; ta[E] = xfma<1, 1>(I[E >> 2][E & 3], t, ta[E]); tb[E] = xfma<0, 0>(I[E >> 2][E & 3], t, tb[E]); });
    SB();
    sfor<0, 2>([&](auto TT) {
      LAS char* p = smem + L_SP + 8192 + (16 * TT + i16) * 128 + (((2 * g + (gq >> 1)) ^ (i16 >> 1)) * 16) + (gq & 1) * 8;
      *(LAS u32x2*)p = u32x2{pk_bf(ta[4 * TT], ta[4 * TT + 1]), pk_bf(ta[4 * TT + 2], ta[4 * TT + 3])};
      *(LAS u32x2*)(p + 4096) = u32x2{pk_bf(tb[4 * TT], tb[4 * TT + 1]), pk_bf(tb[4 * TT + 2], tb[4 * TT + 3])};
    }); };
  auto segC = [&](int T, auto&& mid) {
    const uint32_t vz = (i16 >> 1) * 147456 + (i16 & 1) * 64 + (gq & 1) * 32 + (gq >> 1) * 16, sz = oq_s(WS_ZT + (T == 288 ? 32 : T * 512) + g * 128);
    const u64 zmask = (T == 0 || T == 288) ? 0x0000ffff0000ffffull : ~0ull;
    sfor<0, 2>([&](auto TT) {
      LAS char* q0 = smem + L_SP + 8192 + (16 * TT + i16) * 128 + ((gq ^ (i16 >> 1)) * 16); LAS char* q1 = smem + L_SP + 8192 + (16 * TT + i16) * 128 + (((gq + 4) ^ (i16 >> 1)) * 16);
      uu[0][TT][0] = *(LAS u32x4*)q0; uu[0][TT][1] = *(LAS u32x4*)q1; uu[1][TT][0] = *(LAS u32x4*)(q0 + 4096); uu[1][TT][1] = *(LAS u32x4*)(q1 + 4096); });
    SB(); mid(); SB();
    sfor<0, 2>([&](auto TT) {
      const u32x4 r0 = uu[0][TT][0], r1_ = uu[0][TT][1], i0 = uu[1][TT][0], i1 = uu[1][TT][1];
      f32x4 P = mmb(tG64[0][0], r0, z4); P = mmb(tG64[0][1], r1_, P);
      f32x4 Q = mmb(tG64[1][0], i0, z4); Q = mmb(tG64[1][1], i1, Q);
      f32x4 I = mmb(tG64[1][0], r0, z4); I = mmb(tG64[1][1], r1_, I); I = mmb(tG64[0][0], i0, I); I = mmb(tG64[0][1], i1, I);
      unsigned ra = pk_bf(P.x - Q.x, P.y - Q.y), rb = pk_bf(P.z - Q.z, P.w - Q.w), ia = pk_bf(I.x, I.y), ib = pk_bf(I.z, I.w);
      auto sa = __builtin_amdgcn_permlane16_swap(ra, ia, false, false); auto sb = __builtin_amdgcn_permlane16_swap(rb, ib, false, false);
      const unsigned sa0 = sa[0], sa1 = sa[1], sb0 = sb[0], sb1 = sb[1];
      st16m(u32x4{sa0, sb0, sa1, sb1}, rs, vz, sz + TT * 8 * 147456, zmask);
    }); };
  auto dmas = [&](auto J) { uint32_t t[5]; constexpr uint32_t lb = ((J + 2) & 3) * BUF;
    sfor<0, 5>([&](auto I) { t[I] = lane_of<5 * J + I>(tabw); });
    sfor<0, 4>([&](auto I) { setm0<lb + I * 9216>(sw1); nop0(); dmago(r2, vo2a, t[I]); });
    setm0<lb + 8192>(sh9); dmagoh(r2, vo2b, t[4], hmask); };
  waitv_lb<15>();
  ST_(13);
  ld8c(pl, rs, vfh, 0);
  if (hf == 0) { tif<0>();
    u32x4 hd[4][2];
    Gelu ge, g2;
    pre(lbuf(F0{}));
    blocks(IC<4>{}, T1{}, T1{}, T1{}, lbuf(T1{}), [&](auto B, auto TT) { return hown[B][TT]; },
      [&](auto S) { if constexpr (S < 16) { ge.run<S>(p0, hown[3][0]); g2.run<S>(p1, hown[3][1]); }
        if constexpr (S == 16) st16c<0>(hown[3][0], rs, v16, sow4); if constexpr (S == 17) st16c<1024>(hown[3][1], rs, v16, sow4);
        if constexpr (S >= 17 && S < 25 && (S & 1)) sa = lane_of<8 + (S - 17) / 2>(tabh);
        if constexpr (S >= 18 && S < 26 && !(S & 1)) ld2h(hd[(S - 18) / 2], sa);
        if constexpr (S >= 25 && S < 35 && (S & 1)) dma2p(IC<3>{}, IC<(S - 25) / 2>{});
        if constexpr (S >= 26 && !(S & 1)) dma2g(IC<(S - 26) / 2>{}); });
    ST_(34);
    waitv_b<15>();
    ST_(14);
    hflag(3); check1(pl, 2);
    sfor<0, 4>([&](auto I) { pin(hc[I][0]); pin(hc[I][1]); });
    blocks(IC<4>{}, T1{}, F0{}, T1{}, lbuf(IC<2>{}), [&](auto B, auto TT) { return hc[B][TT]; },
      [&](auto S) { solo(T1{}, S); });
    ST_(35);
    waitv_b<9>();
    ST_(15);
    hflag(4); ld8c(pl, rs, vfh, 0);
    sfor<0, 4>([&](auto I) { pin(hd[I][0]); pin(hd[I][1]); });
    blocks(IC<4>{}, T1{}, F0{}, T1{}, lbuf(IC<3>{}), [&](auto B, auto TT) { return hd[B][TT]; },
      [&](auto S) { solo(IC<2>{}, S); });
    ST_(36);
    kill4(tF64); kill(tw[0][0]); kill(tw[0][1]); kill(p0[0]); kill(p0[1]); kill(p1[0]); kill(p1[1]);
  }
  tend<0>();
  if (oq_s(hf)) { tif<1>();
    if (PRIO & 2) __builtin_amdgcn_s_setprio(1);
    rdu(b);
    { Gelu ge, g2; u32x4 o0, o1;
      const uint32_t g2k = oq_s(g * 2048);
      sfor<0, 16>([&](auto S) { constexpr int C = (S >> 1) & 1, A = S & 1;
        if constexpr (S < 2) ld16c<A * 1024>(wt[A], rs, v16, WS_WT + b * 8192 + g2k);
        else if constexpr (S < 4) ld16<A * 1024>(tw[1][A], rt, v16, TAB_TW + 8192 + g2k);
        else ld16<C * 2048 + A * 1024>((S < 8 ? tF32 : S < 12 ? tG32 : tG64)[C][A], rt, v16, S < 8 ? TAB_F32 : S < 12 ? TAB_G32 : TAB_G64 + 2 * g2k);
        ge.run<S>(p0, o0); g2.run<S>(p1, o1); SB(); });
      if (dbl) { tif<2>(); ldu(T2); } tend<2>();
      st16c<0>(o0, rs, v16, sow4); st16c<1024>(o1, rs, v16, sow4); }
    waitv<25>(); pin4(tF64); pin(tw[0][0]); pin(tw[0][1]);
    segA(b); SB();
    dmas(IC<3>{});
    ST_(34);
    waitv_b<11>();
    ST_(14);
    check1(pl, 2); pin4(tF32); pin4(tG32); pin(tw[1][0]); pin(tw[1][1]);
    segB([&]() { dmas(IC<4>{}); });
    if (dbl) { tif<2>(); waitv<12>(); putu(); ldw(T2); kill4(hr[0]); } else { tif<3>(); ldh4(IC<3>{}); }
    tend<2>(); tend<3>();
    ST_(35);
    waitv_lb<7>();
    ST_(15);
    ld8c(pl, rs, vfh, 0); pin4(tG64);
    segC(b, [&]() { dmas(IC<5>{}); });
    ST_(36);
    if (dbl) { tif<2>();
      rdu(T2); segA(T2); SB();
      waitv_b<7>();
      check1(pl, 3);
      segB([&]() { dmas(IC<6>{}); });
      ldh4(IC<5>{});
      waitv_lb<9>();
      ld8c(pl, rs, vfh, 0);
      segC(T2, [&]() {});
      dmas(IC<7>{});
      ldh4(IC<6>{});
      kill4(hr[1]);
    } else { tif<3>(); ldh4(IC<4>{}); }
    tend<2>(); tend<3>();
    pre((dbl ? lbuf(IC<5>{}) : lbuf(IC<3>{})) + 18432);
    sfor<0, 9>([&](auto DT) { oo[DT] = z4; os[DT] = z4; });
    kill4(hx[0]); kill4(hx[1]);
    if (PRIO & 4) __builtin_amdgcn_s_setprio(0);
  }
  tend<1>();
  u32x4 zr[5], xv, bv;
  u32x2 xv8, bv8;
  uint32_t vta = 0, vtb = 0;
  auto dmat = [&](auto I) {
    if constexpr (I == 0) { const uint32_t t = TAB_TI + (zb0 * 4 + nt) * 9216 - ks * 36 * 36864, c = oq_v(l);
      vta = vo2a - 864 * (((w * 64 + c) * 3641) >> 16) + t; vtb = vo2b - 864 * (((512 + c) * 3641) >> 16) + t + 4 * sh9; }
    if constexpr (I < 4) dmak<3 * BUF + I * 9216, I * 36864>(rt, vta, sw1);
    else if constexpr (I == 4) dmakh<3 * BUF + 8192, 0>(rt, vtb, sh9, hmask);
    else if (ks < 2) { dmak<L_SP, 4 * 36864>(rt, vta, sw1); if (w == 0) dmak<L_SP + 8192, 4 * 36864>(rt, vtb, sh9); }
  };
  const uint32_t azw = (4 * w + 2 * hh + ((l >> 1) & 1)) * 288 + (((l >> 4) & 1) + 2 * ((l >> 2) & 1)) * 64 + ((l >> 3) & 1) * 32 + (l & 1) * 16;
  const uint32_t srow = sx + 288 * nt, vrow = (l >> 2) * 36864;
  auto shared = [&](auto J) {
    if constexpr (J == 8) bar0(); else waitv_b<9>();
    ST_(13 + J);
    if constexpr (J == 3) check1(pl, 3);
    if constexpr (J == 4) { if (tid == 0 && !dbl) setflag(ws, WS_FZ + b * 8, tag); ld8c(pl, rs, vfh, 0); }
    if constexpr (J == 5) check1(pl, 4);
    if constexpr (J == 5) { if (tid == 0 && dbl) setflag(ws, WS_FZ + b * 8, tag); }
    if constexpr (J == 7) { ld16c<0>(pu[0], rs, v16, WS_FZ); ld16c<1024>(pu[1], rs, v16, WS_FZ); }
    pin4(hr[J % 3]);
    SB();
    const uint32_t nxt = lbuf(IC<J + 1>{}) + hf * 18432;
    blocks(IC<2>{}, T1{}, F0{}, T1{}, (J < 5 && dbl) ? lbuf(J) + 18432 : nxt, [&](auto B, auto TT) { return hr[J % 3][B][TT]; },
      [&](auto S) {
        if constexpr (LAD & 2) { if constexpr (S == 17) prio<3>(); if constexpr (S == 4) prio<2>(); if constexpr (S == 9) prio<1>(); if constexpr (S == 13) prio<0>(); }
        if constexpr (J < 6) { if constexpr (S % 4 == 0) dma2p(IC<J + 3>{}, IC<S / 4>{}); if constexpr (S % 4 == 1) dma2g(IC<S / 4>{}); }
        if constexpr (J == 6) { if constexpr (S % 4 == 1 && S < 16) dmat(IC<S / 4>{}); if constexpr (S >= 16) dmat(IC<S - 12>{}); }
        if constexpr (J < 7) { if constexpr (S % 8 == 1) sa = lane_of<4 * (J + 2) + S / 8>(tabh); if constexpr (S % 4 == 3) ldh(IC<J + 2>{}, IC<S / 8>{}, IC<(S / 4) & 1>{}, sa); }
        if constexpr (J == 7) {
          if constexpr (S == 8) { waitv<0>(); check4(pu, WS_FZ); }
          if constexpr (S >= 8 && S < 13) { if constexpr (S == 8) sa = oq_s(WS_ZT + mt * 147456 + zb0 * 8192 + sw1); ld16c<0>(zr[S - 8], rs, v16, sa + (S == 12 && ks >= 2 ? 0 : (int)S - 8) * 8192); }
          if constexpr (S == 14) { ld16<0>(xv, rx, vrow + (l & 3) * 16, srow + 64 * ks); ld8<256>(xv8, rx, vrow + (l & 3) * 8, srow); }
          if constexpr (S == 16) { const u32x4 rb = mksrd(b2, 1152); ld16<0>(bv, rb, (l & 3) * 16, 288 * nt + 64 * ks); ld8<256>(bv8, rb, (l & 3) * 8, 288 * nt); } }
        if constexpr (J == 8 && S >= 13) { waitv<21 - S>(); pin(zr[S - 13]); *(LAS u32x4*)(smem + azw + (S - 13) * 9216) = zr[S - 13]; }
      });
    if constexpr (J < 5) { if (dbl) { tif<2>(); pin4(hx[J & 1]); blocks(IC<2>{}, T1{}, F0{}, T1{}, nxt, [&](auto B, auto TT) { return hx[J & 1][B][TT]; }, [&](auto) {}); } tend<2>(); }
  };
  if (!(oq_s(hf) && dbl)) { tif<5>(); shared(IC<3>{}); shared(IC<4>{}); }
  tend<5>();
  sfor<5, 9>(shared);
  { barl();
    ST_(22);
    const uint32_t az = oq_v(a2 + hf * 18432 + g * 64);
    u32x4 z[2][2];
    sfor<0, 2>([&](auto K) { z[K][0] = tr2(smem + az + K * 9216 + hf * 32, 4608); z[K][1] = tr2(smem + az + K * 9216 + (hf ^ 1) * 32, 4608); });
    const u32x4 z5 = tr2(smem + BUF + a2 + g * 64 + hf * 32, 4608);
    const uint32_t aw = oq_v((hf ? 0 : 2 * BUF) + g * 9216 + v16);
    SB();
    blocks(IC<2>{}, T1{}, F0{}, T1{}, L_SP, [&](auto B, auto TT) { return z[B][TT]; },
      [&](auto S) { if constexpr (S == 9) bar0(); if constexpr (S >= 10) *(LAS f32x4*)(smem + aw + (S - 10) * 1024) = os[S - 10]; });
    *(LAS f32x4*)(smem + aw + 8192) = os[8];
    ST_(23);
    if (ks < 2) blocks(T1{}, F0{}, F0{}, F0{}, 0, [&](auto B, auto TT) { return z5; }, [&](auto S) {});
    barl();
    ST_(24);
    const uint32_t ar = oq_v((hf ? 2 * BUF : 0) + g * 9216 + v16);
    sfor<0, 9>([&](auto DT) { oo[(DT + 2) % 9] += *(LAS f32x4*)(smem + ar + ((DT + 2) % 9) * 1024); });
  }
  auto pk1 = [&](float p, float q) { const unsigned v = pk_bf(p, q); return v < ~1u ? v : ~1u; };
  sfor<1, 4>([&](auto K) { const f32x4 p = oo[2 * K], q = oo[2 * K + 1];
    st16c<0>(u32x4{pk1(p.x, p.y), pk1(p.z, p.w), pk1(q.x, q.y), pk1(q.z, q.w)}, rs, v16, WS_PART + ((b & ~6) + ((ks + K) & 3) * 2) * 32768 + ((K - 1) * 8 + w) * 1024); });
  if (g != ks) st8c(u32x2{pk1(oo[8].x, oo[8].y), pk1(oo[8].z, oo[8].w)}, rs, l * 8, WS_PART + ((b & ~6) + g * 2) * 32768 + 24576 + (((g - ks) & 3) - 1) * 1024 + hf * 512);
  ST_(25);
  { u32x4 pp[3]; u32x2 p8[3];
    const uint32_t s8 = WS_PART + b * 32768 + (g == ks ? 24576 + hf * 512 : w * 1024);
#pragma nounroll
    for (unsigned spins = 0; spins < (1u << 16); ++spins) {
      sfor<0, 3>([&](auto K) { ld16c<0>(pp[K], rs, v16, WS_PART + b * 32768 + (K * 8 + w) * 1024); ld8c(p8[K], rs, l * 8, s8 + (g == ks ? K * 1024 : 0)); });
      waitv<0>();
      unsigned m = 0;
      sfor<0, 3>([&](auto K) { pin(pp[K]); pin(p8[K]); const unsigned a = pp[K].x, c = pp[K].y, d = pp[K].z, e = pp[K].w, f = p8[K].x, h = p8[K].y;
        m = max(max(max(m, a), max(c, d)), max(max(e, f), h)); });
      if (__all(m != ~0u)) break;
      __builtin_amdgcn_s_sleep(2);
    }
    ST_(26);
    LAS char* pt = smem + BUF + w * 3072;
    sfor<0, 3>([&](auto Q) { f32x4 r = oo[Q < 2 ? (int)Q : 8];
      sfor<0, 3>([&](auto K) { const unsigned a = Q == 2 ? p8[K].x : pp[K][2 * (Q & 1)], c = Q == 2 ? p8[K].y : pp[K][2 * (Q & 1) + 1];
        r += f32x4{lo_f(a), hi_f(a), lo_f(c), hi_f(c)}; });
      *(LAS f32x4*)(pt + Q * 1024 + i16 * 64 + gq * 16) = r; });
    pin(xv); pin(bv); pin(xv8); pin(bv8);
    LAS char* pq = pt + ((l >> 1) & 1) * 1024 + (l >> 2) * 64 + (l & 1) * 32;
    const f32x4 ra = *(LAS f32x4*)pq, rb = *(LAS f32x4*)(pq + 16), rc = *(LAS f32x4*)(pt + 2048 + v16);
    auto fin = [&](float a, float c, unsigned xx, unsigned bb) { return pk_bf(a + lo_f(xx) + lo_f(bb), c + hi_f(xx) + hi_f(bb)); };
    char* op = out + srow + vrow;
    *(u32x4*)(op + 64 * ks + (l & 3) * 16) = u32x4{fin(ra.x, ra.y, xv.x, bv.x), fin(ra.z, ra.w, xv.y, bv.y), fin(rb.x, rb.y, xv.z, bv.z), fin(rb.z, rb.w, xv.w, bv.w)};
    if (g == ks) *(u32x2*)(op + 256 + (l & 3) * 8) = u32x2{fin(rc.x, rc.y, xv8.x, bv8.x), fin(rc.z, rc.w, xv8.y, bv8.y)};
  }
  if (b == 0 && tid == 0) setflag(ws, WS_EPOCH, tag);
  asm volatile(".Lfk_end:");
}

extern "C" void kernel_launch(void* const* d_in, const int* in_sizes, int n_in, void* d_out, int out_size, void* d_ws, size_t ws_size, hipStream_t stream) {
  (void)hipFuncSetAttribute((const void*)fused_kernel, hipFuncAttributeMaxDynamicSharedMemorySize, LDS_BYTES);
  fused_kernel<<<256, 512, LDS_BYTES, stream>>>((const char*)d_in[0], (const char*)d_in[1], (const char*)d_in[2], (const char*)d_in[4], (const char*)d_in[5],
                                                (const char*)d_in[6], (const char*)d_in[7], (char*)d_out, (char*)d_ws);
}
```

```cpp
#define HOLD_I 27
#define HOLD_T 3800
#include <hip/hip_runtime.h>
#include <stdint.h>
#include <type_traits>
#define LAD 3
#define PP 0
#define NW 63
#define PRIO 0

typedef unsigned short u16;
typedef short s16x4 __attribute__((ext_vector_type(4)));
typedef short s16x8 __attribute__((ext_vector_type(8)));
typedef __bf16 bf16x8 __attribute__((ext_vector_type(8)));
typedef __bf16 bf16x2 __attribute__((ext_vector_type(2)));
typedef _Float16 f16x8 __attribute__((ext_vector_type(8)));
typedef _Float16 f16x2 __attribute__((ext_vector_type(2)));
typedef float f32x4 __attribute__((ext_vector_type(4)));
typedef float f32x2 __attribute__((ext_vector_type(2)));
typedef unsigned u32x4 __attribute__((ext_vector_type(4)));
typedef unsigned u32x2 __attribute__((ext_vector_type(2)));
typedef unsigned long long u64;
#define LAS __attribute__((address_space(3)))
#define GAS __attribute__((address_space(1)))
#define DI __device__ __forceinline__

namespace tab {
constexpr double PI = 3.14159265358979323846;
constexpr double ccos(int a, int n) {
  a %= n; if (a < 0) a += n;
  if (2 * a > n) a = n - a;
  bool neg = false;
  if (4 * a > n) { a = n / 2 - a; neg = true; }
  if (4 * a == n) return 0.0;
  double x = 2 * PI * a / n, t = 1, s = 1;
  for (int i = 1; i <= 13; i++) { t *= -x * x / ((2 * i - 1) * (2 * i)); s += t; }
  return neg ? -s : s;
}
constexpr u16 to_bf(double v) { float f = (float)v; uint32_t u = __builtin_bit_cast(uint32_t, f); u += 0x7fffu + ((u >> 16) & 1u); return (u16)(u >> 16); }
constexpr u16 to_h(double v) { _Float16 h = (_Float16)v; return __builtin_bit_cast(u16, h); }
struct C576 { u16 b1[576]; u16 b2[576]; };
constexpr C576 gen576() { C576 r{}; for (int i = 0; i < 576; i++) { double c = ccos(i, 576) / 24.0; r.b1[i] = to_bf(c); r.b2[i] = to_bf(2 * c); } return r; }
constexpr C576 c576 = gen576();
constexpr int colk(int c) { return c == 0 ? 0 : c == 1 ? 288 : c < 288 ? (c & ~1) : ((c - 288) & ~1) + 1; }
constexpr int colp(int c) { return c < 2 ? 0 : (c & 1); }
struct Chunk { u16 v[9216]; };
constexpr Chunk genTF(int u) {
  Chunk r{};
  for (int i = 0; i < 1152; i++) { int t = i / 576, s = (i % 576) >> 6, l = i & 63, c = 32 * u + 16 * t + (l & 15), k = colk(c), o = 144 * colp(c), d = 32 * s + 8 * (l >> 4);
    for (int j = 0; j < 8; j++) r.v[i * 8 + j] = c576.b1[((d + j) * k + o) % 576]; }
  return r;
}
constexpr Chunk genTI(int q) {
  Chunk r{};
  for (int i = 0; i < 64; i++) { int c = 32 * (q >> 1) + (i & 31), k = colk(c), o = 144 * colp(c), d0 = 144 * (2 * (q & 1) + (i >> 5));
    if (c < 2) { for (int d = 0; d < 144; d++) r.v[i * 144 + d] = c576.b1[((d0 + d) * k + o) % 576]; }
    else { for (int d = 0; d < 144; d++) r.v[i * 144 + d] = c576.b2[((d0 + d) * k + o) % 576]; } }
  return r;
}
template <int I> constexpr Chunk cTF = genTF(I);
template <int I> constexpr Chunk cTI = genTI(I);
struct Small { u16 f64[4][2][2][512]; u16 f32[2][2][512]; u16 g32[2][2][512]; u16 g64[4][2][2][512]; };
constexpr Small genSmall() {
  Small r{};
  double c64[64] = {}; for (int i = 0; i < 64; i++) c64[i] = ccos(i, 64);
  const double r32 = 0.17677669529663688110;
  for (int l = 0; l < 64; l++) for (int j = 0; j < 8; j++) {
    int rr = l & 15, gq = l >> 4;
    int p2 = 16 * (j >> 2) + 4 * gq + (j & 3);
    for (int w = 0; w < 4; w++) for (int a = 0; a < 2; a++) {
      int e = (16 * w + rr) * (32 * a + 8 * gq + j), e1 = (16 * w + rr) * (32 * a + p2);
      r.f64[w][0][a][l * 8 + j] = to_h(c64[e1 & 63] / 8); r.f64[w][1][a][l * 8 + j] = to_h(c64[(e1 + 16) & 63] / 8);
      r.g64[w][0][a][l * 8 + j] = to_bf(c64[e & 63] / 8); r.g64[w][1][a][l * 8 + j] = to_bf(c64[(e + 48) & 63] / 8);
    }
    for (int t = 0; t < 2; t++) {
      int e = 2 * ((16 * t + rr) * p2);
      r.f32[0][t][l * 8 + j] = to_h(c64[e & 63] * r32); r.f32[1][t][l * 8 + j] = to_h(c64[(e + 16) & 63] * r32);
      r.g32[0][t][l * 8 + j] = to_bf(c64[e & 63] * r32); r.g32[1][t][l * 8 + j] = to_bf(c64[(e + 48) & 63] * r32);
    }
  }
  return r;
}
struct C2048 { u16 v[2048]; };
constexpr C2048 gen2048() { C2048 r{}; for (int i = 0; i < 2048; i++) r.v[i] = to_h(ccos(i, 2048)); return r; }
constexpr C2048 c2048 = gen2048();
struct Tw { u16 v[2][4][2][512]; };
constexpr Tw genTw() {
  Tw r{};
  for (int w = 0; w < 4; w++) for (int t = 0; t < 2; t++) for (int l = 0; l < 64; l++) for (int j = 0; j < 4; j++) {
    int e0 = (16 * t + 4 * (l >> 4) + j) * (16 * w + (l & 15)), e1 = (16 * t + (l & 15)) * (16 * w + 4 * (l >> 4) + j);
    r.v[0][w][t][l * 8 + 2 * j] = c2048.v[e0 & 2047]; r.v[0][w][t][l * 8 + 2 * j + 1] = c2048.v[(e0 + 1536) & 2047];
    r.v[1][w][t][l * 8 + 2 * j] = c2048.v[e1 & 2047]; r.v[1][w][t][l * 8 + 2 * j + 1] = c2048.v[(e1 + 1536) & 2047]; }
  return r;
}
constexpr Tw ctw = genTw();
constexpr int fblk(int nt, int J, int i) {
  if (J == 0) return 9 * nt + (nt & 1) + 2 * i;
  if (J == 1) { int pn = (nt + i) & 3; return 9 * pn + ((pn & 1) ? 0 : 8); }
  int q = 4 * (J - 2) + i, r = q / 7, m = q - 7 * r + 1, pn = (nt + (m >> 1)) & 3;
  return 9 * pn + (pn & 1) + 2 * r + (m & 1);
}
struct Adr { uint32_t w[4][4][64]; uint32_t h[4][2][64]; uint32_t v[4][576]; };
constexpr Adr genAdr() {
  Adr r{};
  for (int nt = 0; nt < 4; nt++) {
    for (int k = 0; k < 4; k++) for (int l = 0; l < 45; l++) r.w[nt][k][l] = fblk(nt, l / 5, l % 5 < 4 ? l % 5 : k) * 36864 + 288 * nt;
    for (int k = 0; k < 2; k++) for (int l = 0; l < 36; l++) r.h[nt][k][l] = fblk(nt, l / 4, (l + 2 * k) & 3) * 8192;
    for (int c = 0; c < 576; c++) { int k = c % 18; r.v[nt][c] = (c / 18) * 1152 + (k < 16 ? ((k + 4 * nt) & 15) : k) * 16 + nt * 36 * 36864; } }
  return r;
}
struct alignas(256) All { Chunk tf[18]; Chunk ti[36]; Small sm; Tw tw; Adr adr; };
template <int... I, int... J> constexpr All mk(std::integer_sequence<int, I...>, std::integer_sequence<int, J...>) { return All{{cTF<I>...}, {cTI<J>...}, genSmall(), ctw, genAdr()}; }
}
static __device__ const tab::All g_tab = tab::mk(std::make_integer_sequence<int, 18>{}, std::make_integer_sequence<int, 36>{});

constexpr uint32_t BUF = 36864, L_SP = 147456, L_B1 = L_SP, L_JUNK = L_SP + 2048, L_W = L_SP + 4096, LDS_BYTES = 163840;
constexpr uint32_t WS_EPOCH = 0, WS_FU = 4096, WS_FZ = 8192, WS_FH = 16384;
constexpr uint32_t WS_H = 1u << 20, WS_UT = 20u << 20, WS_WT = 23u << 20, WS_ZT = 26u << 20, WS_PART = 29u << 20, WS_END = 48u << 20;
constexpr uint32_t TAB_TF = 0, TAB_TI = 18 * 18432, TAB_F64 = 54 * 18432, TAB_F32 = TAB_F64 + 16384, TAB_G32 = TAB_F32 + 4096, TAB_G64 = TAB_G32 + 4096, TAB_TW = TAB_G64 + 16384, TAB_AW = TAB_TW + 16384, TAB_AH = TAB_AW + 4096, TAB_AV = TAB_AH + 2048, TAB_END = TAB_AV + 9216;

template <int I, int N, class Fn> DI void sfor(Fn&& f) { if constexpr (I < N) { f(std::integral_constant<int, I>{}); sfor<I + 1, N>(f); } }
template <int V> using IC = std::integral_constant<int, V>;

DI u32x4 mksrd(const void* p, uint32_t n) { return u32x4{(unsigned)(size_t)p, (unsigned)((size_t)p >> 32) & 0xffffu, n, 0x00020000u}; }
template <int IMM> DI void ld16(u32x4& d, u32x4 srd, uint32_t voff, uint32_t soff) { asm volatile("buffer_load_dwordx4 %0, %1, %2, %3 offen offset:%4" : "=v"(d) : "v"(voff), "s"(srd), "s"(soff), "i"(IMM) : "memory"); }
template <int IMM> DI void ld16c(u32x4& d, u32x4 srd, uint32_t voff, uint32_t soff) { asm volatile("buffer_load_dwordx4 %0, %1, %2, %3 offen offset:%4 sc1" : "=v"(d) : "v"(voff), "s"(srd), "s"(soff), "i"(IMM) : "memory"); }
template <int IMM> DI void ld8(u32x2& d, u32x4 srd, uint32_t voff, uint32_t soff) { asm volatile("buffer_load_dwordx2 %0, %1, %2, %3 offen offset:%4" : "=v"(d) : "v"(voff), "s"(srd), "s"(soff), "i"(IMM) : "memory"); }
DI void ld8c(u32x2& d, u32x4 srd, uint32_t voff, uint32_t soff) { asm volatile("buffer_load_dwordx2 %0, %1, %2, %3 offen sc1" : "=v"(d) : "v"(voff), "s"(srd), "s"(soff) : "memory"); }
DI void ld4(unsigned& d, u32x4 srd, uint32_t voff, uint32_t soff) { asm volatile("buffer_load_dword %0, %1, %2, %3 offen" : "=v"(d) : "v"(voff), "s"(srd), "s"(soff) : "memory"); }
DI void dma(u32x4 srd, uint32_t voff, uint32_t soff, uint32_t lds) { asm volatile("s_mov_b32 m0, %3\n\ts_nop 0\n\tbuffer_load_dwordx4 %0, %1, %2 offen lds" :: "v"(voff), "s"(srd), "s"(soff), "s"(lds) : "memory"); }
DI void dmah(u32x4 srd, uint32_t voff, uint32_t soff, uint32_t lds, u64 mask) {
  asm volatile("s_mov_b32 m0, %3\n\ts_mov_b64 exec, %4\n\tbuffer_load_dwordx4 %0, %1, %2 offen lds\n\ts_mov_b64 exec, -1" :: "v"(voff), "s"(srd), "s"(soff), "s"(lds), "s"(mask) : "memory"); }
template <uint32_t LDS, uint32_t SO> DI void dmak(u32x4 srd, uint32_t voff, uint32_t base) { uint32_t t;
  asm volatile("s_add_u32 m0, %3, %4\n\ts_mov_b32 %0, %5\n\tbuffer_load_dwordx4 %1, %2, %0 offen lds" : "=&s"(t) : "v"(voff), "s"(srd), "s"(base), "i"(LDS), "i"(SO) : "memory", "scc"); }
template <uint32_t LDS, uint32_t SO> DI void dmakh(u32x4 srd, uint32_t voff, uint32_t base, u64 mask) { uint32_t t;
  asm volatile("s_add_u32 m0, %3, %4\n\ts_mov_b32 %0, %5\n\ts_mov_b64 exec, %6\n\tbuffer_load_dwordx4 %1, %2, %0 offen lds\n\ts_mov_b64 exec, -1"
               : "=&s"(t) : "v"(voff), "s"(srd), "s"(base), "i"(LDS), "i"(SO), "s"(mask) : "memory", "scc"); }
template <int L> DI uint32_t lane_of(uint32_t t) { uint32_t r; asm volatile("v_readlane_b32 %0, %1, %2" : "=s"(r) : "v"(t), "i"(L)); return r; }
template <uint32_t LDS> DI void setm0(uint32_t base) { asm volatile("s_add_u32 m0, %0, %1" :: "s"(base), "i"(LDS) : "scc"); }
DI void dmago(u32x4 srd, uint32_t voff, uint32_t soff) { asm volatile("buffer_load_dwordx4 %0, %1, %2 offen lds" :: "v"(voff), "s"(srd), "s"(soff) : "memory"); }
DI void dmagoh(u32x4 srd, uint32_t voff, uint32_t soff, u64 mask) {
  asm volatile("s_mov_b64 exec, %3\n\tbuffer_load_dwordx4 %0, %1, %2 offen lds\n\ts_mov_b64 exec, -1" :: "v"(voff), "s"(srd), "s"(soff), "s"(mask) : "memory"); }
DI void nop4() { asm volatile("s_nop 4"); }
DI void nop0() { asm volatile("s_nop 0"); }
DI void touch(u32x4 srd, uint32_t voff, uint32_t soff, u64 mask) {
  asm volatile("s_mov_b32 m0, %3\n\ts_mov_b64 exec, %4\n\tbuffer_load_dword %0, %1, %2 offen lds\n\ts_mov_b64 exec, -1" :: "v"(voff), "s"(srd), "s"(soff), "s"(L_JUNK), "s"(mask) : "memory"); }
DI void code_touch(int part, int l) {
  u64 pc; uint32_t n;
  asm volatile("s_getpc_b64 %0\n.Lfk_begin:\n\ts_mov_b32 %1, .Lfk_end-.Lfk_begin" : "=s"(pc), "=s"(n));
  touch(u32x4{(unsigned)pc, (unsigned)(pc >> 32) & 0xffffu, n, 0x00020000u}, l * 128, part * 2048, 0xffffull); }
template <int IMM> DI void st16c(u32x4 d, u32x4 srd, uint32_t voff, uint32_t soff) {
  asm volatile("buffer_store_dwordx4 %0, %1, %2, %3 offen offset:%4 sc1\n\ts_nop 1" :: "v"(d), "v"(voff), "s"(srd), "s"(soff), "i"(IMM) : "memory"); }
DI void st16m(u32x4 d, u32x4 srd, uint32_t voff, uint32_t soff, u64 mask) {
  asm volatile("s_mov_b64 exec, %4\n\tbuffer_store_dwordx4 %0, %1, %2, %3 offen sc1\n\ts_mov_b64 exec, -1\n\ts_nop 0" :: "v"(d), "v"(voff), "s"(srd), "s"(soff), "s"(mask) : "memory"); }
DI void st8c(u32x2 d, u32x4 srd, uint32_t voff, uint32_t soff) {
  asm volatile("buffer_store_dwordx2 %0, %1, %2, %3 offen sc1\n\ts_nop 1" :: "v"(d), "v"(voff), "s"(srd), "s"(soff) : "memory"); }
template <int N> DI void waitv() { asm volatile("s_waitcnt vmcnt(%0)" :: "i"(N) : "memory"); }
template <int N> DI void waitv_b() { asm volatile("s_waitcnt vmcnt(%0)\n\ts_barrier" :: "i"(N) : "memory"); }
template <int N> DI void waitv_lb() { asm volatile("s_waitcnt vmcnt(%0) lgkmcnt(0)\n\ts_barrier" :: "i"(N) : "memory"); }
DI void bar0() { asm volatile("s_barrier" ::: "memory"); }
DI void barl() { asm volatile("s_waitcnt lgkmcnt(0)\n\ts_barrier" ::: "memory"); }
DI void pin(u32x4& v) { asm volatile("" : "+v"(v)); }
DI void pin(u32x2& v) { asm volatile("" : "+v"(v)); }
DI void pin(unsigned& v) { asm volatile("" : "+v"(v)); }
DI void fix(float& v) { asm volatile("" : "+v"(v)); }
DI void kill(u32x4& v) { asm volatile("" : "=v"(v)); }
DI void kill(f32x4& v) { asm volatile("" : "=v"(v)); }
DI uint32_t oq_s(uint32_t v) { asm volatile("" : "+s"(v)); return v; }
DI uint32_t oq_v(uint32_t v) { asm volatile("" : "+v"(v)); return v; }
template <int P> DI void prio() { asm volatile("s_setprio %0" :: "i"(P)); }
#define SB() __builtin_amdgcn_sched_barrier(0)
template <int C> DI void tif() { asm volatile("; @IF %0" :: "i"(C)); }
template <int C> DI void tend() { asm volatile("; @END %0" :: "i"(C)); }

DI float asf(unsigned u) { return __builtin_bit_cast(float, u); }
DI unsigned asu(float f) { return __builtin_bit_cast(unsigned, f); }
DI unsigned pk_bf(float a, float b) { f32x2 p = {a, b}; bf16x2 r = __builtin_convertvector(p, bf16x2); return __builtin_bit_cast(unsigned, r); }
DI unsigned pk_h(float a, float b) { f32x2 p = {a, b}; f16x2 r = __builtin_convertvector(p, f16x2); return __builtin_bit_cast(unsigned, r); }
DI float bfr(float a) { return asf(pk_bf(a, a) << 16); }
DI float lo_f(unsigned u) { return asf(u << 16); }
DI float hi_f(unsigned u) { return asf(u & 0xffff0000u); }
DI float dot2(unsigned a, unsigned b, float c) { return __builtin_amdgcn_fdot2_f32_bf16(__builtin_bit_cast(bf16x2, a), __builtin_bit_cast(bf16x2, b), c, false); }
DI float vfma(float a, float b, float c) { float d; asm("v_fma_f32 %0, %1, %2, %3" : "=v"(d) : "v"(a), "v"(b), "v"(c)); return d; }
DI float vmul(float a, float b) { float d; asm("v_mul_f32 %0, %1, %2" : "=v"(d) : "v"(a), "v"(b)); return d; }
DI float vfms(float a, float b, float c) { float d; asm("v_fma_f32 %0, -%1, %2, %3" : "=v"(d) : "v"(a), "v"(b), "v"(c)); return d; }
template <int H, int N> DI float xmul(float a, unsigned t) { float d;
  if constexpr (N) asm("v_fma_mix_f32 %0, -%1, %2, 0 op_sel:[0,%3,0] op_sel_hi:[0,1,0]" : "=v"(d) : "v"(a), "v"(t), "i"(H));
  else asm("v_fma_mix_f32 %0, %1, %2, 0 op_sel:[0,%3,0] op_sel_hi:[0,1,0]" : "=v"(d) : "v"(a), "v"(t), "i"(H));
  return d; }
template <int H, int N> DI float xfma(float a, unsigned t, float c) { float d;
  if constexpr (N) asm("v_fma_mix_f32 %0, -%1, %2, %3 op_sel:[0,%4,0] op_sel_hi:[0,1,0]" : "=v"(d) : "v"(a), "v"(t), "v"(c), "i"(H));
  else asm("v_fma_mix_f32 %0, %1, %2, %3 op_sel:[0,%4,0] op_sel_hi:[0,1,0]" : "=v"(d) : "v"(a), "v"(t), "v"(c), "i"(H));
  return d; }
template <int H, int D> DI void xfmah(unsigned& d, float a, unsigned t, float c) {
  if constexpr (D == 0) asm("v_fma_mixlo_f16 %0, %1, %2, %3 op_sel:[0,%4,0] op_sel_hi:[0,1,0]" : "=v"(d) : "v"(a), "v"(t), "v"(c), "i"(H));
  else asm("v_fma_mixhi_f16 %0, %1, %2, %3 op_sel:[0,%4,0] op_sel_hi:[0,1,0]" : "+v"(d) : "v"(a), "v"(t), "v"(c), "i"(H)); }
template <int CTRL> DI float dpp_add(float v) { return v + asf(__builtin_amdgcn_update_dpp(0, asu(v), CTRL, 0xf, 0xf, true)); }
DI float sum8(float v) { return dpp_add<0x141>(dpp_add<0x4E>(dpp_add<0xB1>(v))); }
DI f32x4 mmb(u32x4 a, u32x4 b, f32x4 c) { return __builtin_amdgcn_mfma_f32_16x16x32_bf16(__builtin_bit_cast(bf16x8, a), __builtin_bit_cast(bf16x8, b), c, 0, 0, 0); }
DI f32x4 mmh(u32x4 a, u32x4 b, f32x4 c) { return __builtin_amdgcn_mfma_f32_16x16x32_f16(__builtin_bit_cast(f16x8, a), __builtin_bit_cast(f16x8, b), c, 0, 0, 0); }
DI u32x4 tr2(LAS char* p, int second) {
  s16x4 a = __builtin_amdgcn_ds_read_tr16_b64_v4i16((LAS s16x4*)p);
  s16x4 b = __builtin_amdgcn_ds_read_tr16_b64_v4i16((LAS s16x4*)(p + second));
  s16x8 c = __builtin_shufflevector(a, b, 0, 1, 2, 3, 4, 5, 6, 7);
  return __builtin_bit_cast(u32x4, c);
}
struct Gelu { float a0, a1;
  template <int Q> DI void step(float v0, float v1, unsigned& o) {
    if constexpr (Q == 0) { a0 = __builtin_fmaf(v0 * v0, -0.10294324f, -2.3022082f); a1 = __builtin_fmaf(v1 * v1, -0.10294324f, -2.3022082f); }
    if constexpr (Q == 1) { a0 = __builtin_amdgcn_exp2f(v0 * a0); a1 = __builtin_amdgcn_exp2f(v1 * a1); }
    if constexpr (Q == 2) { a0 = __builtin_amdgcn_rcpf(1.0f + a0); a1 = __builtin_amdgcn_rcpf(1.0f + a1); }
    if constexpr (Q == 3) { o = pk_bf(v0 * a0, __builtin_fmaf(v1, a1, 0.0f)); }
  }
  template <int K> DI void run(const f32x4 (&p)[2], u32x4& o) {
    constexpr int P = K >> 2; const float v0 = p[P >> 1][2 * (P & 1)], v1 = p[P >> 1][2 * (P & 1) + 1];
    unsigned t = 0; step<K & 3>(v0, v1, t); if constexpr ((K & 3) == 3) o[P] = t; }
};
#define ST_(i) do { if constexpr ((i) == 0) { asm volatile("s_memrealtime %0\n\ts_waitcnt lgkmcnt(0)" : "=s"(hold_t0) :: "memory"); } \
  if constexpr ((i) == HOLD_I) { unsigned long long t_; do { asm volatile("s_memrealtime %0\n\ts_waitcnt lgkmcnt(0)" : "=s"(t_) :: "memory"); } while (t_ - hold_t0 < HOLD_T); } } while (0)
#define RLX_AGENT __ATOMIC_RELAXED, __HIP_MEMORY_SCOPE_AGENT
DI bool all_of(bool c) { return __builtin_amdgcn_ballot_w64(c) == ~0ull; }
DI bool lvl_ok(unsigned lo, unsigned hi, u64 tag, unsigned lvl) { return (hi == (unsigned)(tag >> 32)) & (lo - (unsigned)tag - lvl <= 7u - lvl); }
template <int N> DI void spin(const char* base, uint32_t off, u64 tag, unsigned lvl) {
#pragma nounroll
  for (unsigned spins = 0; spins < (1u << 17); ++spins) {
    bool ok = true;
#pragma unroll
    for (int k = 0; k < N; ++k) { u64 v = __hip_atomic_load((GAS u64*)(base + off + (k & 1) * 8 + (k >> 1) * 1024), RLX_AGENT); ok &= lvl_ok((unsigned)v, (unsigned)(v >> 32), tag, lvl); }
    if (__all(ok)) break;
    __builtin_amdgcn_s_sleep(1);
  }
}
DI void setflag(char* ws, uint32_t off, u64 v) { __hip_atomic_store((GAS u64*)(ws + off), v, RLX_AGENT); }

__global__ __launch_bounds__(512, 2) void fused_kernel(const char* __restrict__ x, const char* __restrict__ wr, const char* __restrict__ wi,
    const char* __restrict__ w1, const char* __restrict__ b1, const char* __restrict__ w2, const char* __restrict__ b2, char* out, char* ws) {
  extern __shared__ __attribute__((aligned(16))) char smem_[];
  LAS char* smem = (LAS char*)smem_;
  const int tid = threadIdx.x, l = tid & 63;
  const int w = __builtin_amdgcn_readfirstlane(tid >> 6), g = w & 3, hf = w >> 2;
  const int b = blockIdx.x, xcd = b & 7, jj = b >> 3;
  const int ks = xcd >> 1, mt = (xcd & 1) * 8 + (jj >> 2), nt = jj & 3, fj = ks * 4 + nt;
  const u32x4 rs = mksrd(ws, WS_END), rx = mksrd(x, 2048 * 1152), r1 = mksrd(w1, 576 * 9216), r2 = mksrd(w2, 4608 * 1152), rt = mksrd(&g_tab, TAB_END);
  const int i16 = l & 15, q4 = i16 >> 2, p4 = i16 & 3, hh = l >> 5, gq = l >> 4;
  const u64 hmask = (w & 1) ? 0xffffffff00000000ull : 0xffffffffull;
  const uint32_t v16 = l * 16;
  asm volatile("" :: "s"(ws), "s"(out), "s"(b1), "s"(b2), "s"(w2), "s"(wr), "s"(wi));
  unsigned long long hold_t0;
  ST_(0);
  u32x4 x0[18], x1[18];
  const uint32_t vx = (l >> 3) * 36864 + (l & 7) * 16;
  const uint32_t sx = (uint32_t)((32 * (g & 1) + 16 * hf) * 32 + 2 * mt + (g >> 1)) * 1152;
  const int odd = fj & 1, lone = odd ? 0 : 8;
  const int tileA = 18 * odd + 2 * (fj >> 1), tileC = 18 * odd + 16 + (nt >> 1);
  u32x2 ep; unsigned b1v;
  const uint32_t vo1 = (8 * hh + ((l >> 3) & 3)) * 9216 + (((l & 7) ^ (((l >> 4) & 3) << 1)) * 16);
  uint32_t v1a = 0, v1b = 0, sown = 0, sbox = 0;
  const uint32_t sw4 = oq_s(w * 4096), sw1 = oq_s(w * 1024), sh1 = oq_s((w >> 1) * 1024), sh9 = oq_s((w >> 1) * 9216);
  auto dma1 = [&](auto ST, auto I) {
    constexpr uint32_t so = 288 * (ST & 1) * 9216 + 128 * ((ST >> 1) - 1), lb = (ST & 3) * BUF;
    if constexpr (I < 4) dmak<lb + I * 1024, so + (16 * (I >> 1) + 4 * (I & 1)) * 9216>(r1, v1a, sw4);
    else dmakh<lb + 32768, so>(r1, v1b, sh1, hmask);
  };
  const uint32_t axb = 2 * BUF + g * 18432;
  const uint32_t axw = axb + ((l >> 2) & 1) * 1024 + (l & 3) * 256 + (((l >> 3) ^ (l & 7)) * 16);
  const uint32_t axr0 = axb + gq * 256 + ((i16 ^ gq) * 16), axr1 = axr0 ^ 64;
  auto rdx = [&](u32x4 (&d)[18], auto S) { d[S] = *(LAS u32x4*)(smem + ((S & 1) ? axr1 : axr0) + S * 1024); };
  u32x4 xr[2][9];
  f32x2 rn2[2], mb2[2];
  auto ldx = [&]() { sfor<0, 18>([&](auto C) { ld16<(C % 9) * 128>(xr[C / 9][C % 9], rx, vx, sx + (C / 9) * 294912); }); ld8c(ep, rs, 0, WS_EPOCH); };
  auto stats = [&](auto RB, auto NV, auto&& fill) {
    float sa[4] = {}, qa[4] = {};
    sfor<0, 9>([&](auto C) { fill(C);
      waitv<NV + 17 - 9 * RB - C>(); pin(xr[RB][C]);
      sfor<0, 4>([&](auto D) { const unsigned u = xr[RB][C][(int)D]; sa[D] = dot2(u, 0x3f803f80u, sa[D]); qa[D] = dot2(u, u, qa[D]); });
      SB(); });
    rn2[RB] = f32x2{(sa[0] + sa[1]) + (sa[2] + sa[3]), (qa[0] + qa[1]) + (qa[2] + qa[3])}; };
  auto chain = [&](auto RB) {
    const float mean = sum8(rn2[RB].x) * (1.0f / 576.0f);
    const float var = fmaxf(sum8(rn2[RB].y) * (1.0f / 576.0f) - mean * mean, 0.0f);
    const float sd = bfr(__builtin_amdgcn_sqrtf(bfr(bfr(var) + 1e-5f)));
    const float rn = __builtin_amdgcn_rcpf(sd), mb = -bfr(mean) * rn;
    rn2[RB] = f32x2{rn, rn}; mb2[RB] = f32x2{mb, mb}; };
  auto put = [&](auto RB, auto C) { *(LAS u32x4*)(smem + axw + RB * 128 + C * 2048) = xr[RB][C]; };
  auto norm = [&](auto RB, auto&& fill) {
    sfor<0, 9>([&](auto C) { f32x2 f[4];
      sfor<0, 4>([&](auto D) { const unsigned u = xr[RB][C][(int)D]; f[D] = f32x2{lo_f(u), hi_f(u)}; });
      SB();
      sfor<0, 4>([&](auto D) { f[D] = __builtin_elementwise_fma(f[D], rn2[RB], mb2[RB]); });
      SB();
      xr[RB][C] = u32x4{pk_bf(f[0].x, f[0].y), pk_bf(f[1].x, f[1].y), pk_bf(f[2].x, f[2].y), pk_bf(f[3].x, f[3].y)};
      fill(C);
      SB(); }); };
  using T1 = IC<1>; using F0 = IC<0>;
  u64 tag;
  auto mktag = [&]() { pin(ep);
    tag = ((u64)(unsigned)__builtin_amdgcn_readfirstlane(ep.y) << 32) | (unsigned)__builtin_amdgcn_readfirstlane(ep.x);
    tag += 0x9E3779B97F4A7C15ull; tag = (tag ^ (tag >> 30)) * 0xBF58476D1CE4E5B9ull; tag = (tag ^ (tag >> 27)) * 0x94D049BB133111EBull; tag = (tag ^ (tag >> 31)) & ~7ull; };
  f32x4 c0[2], c1[2], p0[2], p1[2];
  auto bias = [&](int unit, auto FT) { return *(LAS f32x4*)(smem + L_B1 + (32 * unit + 16 * FT + 4 * gq) * 4); };
  const int rho = 16 * (gq >> 1) + 4 * (gq & 1) + q4;
  uint32_t vo2a, vo2b;
  const uint32_t sh = WS_H + (mt * 144 * 4 + g) * 2048;
  uint32_t tabw, tabh;
  uint32_t sq = 0;
  auto dma2p = [&](auto J, auto I) { constexpr uint32_t lb = ((J + 2) & 3) * BUF;
    sq = lane_of<5 * J + I>(tabw); if constexpr (I < 4) setm0<lb + I * 9216>(sw1); else setm0<lb + 8192>(sh9); };
  auto dma2g = [&](auto I) { if constexpr (I < 4) dmago(r2, vo2a, sq); else dmagoh(r2, vo2b, sq, hmask); };
  const uint32_t vho = v16 + hf * 1024, vhs = v16 + (hf ^ 1) * 1024;
  const uint32_t vfh = WS_FH + ((b & ~24) + gq * 8) * 128;
  auto check1 = [&](u32x2& p, unsigned lvl) { pin(p); if (!all_of(lvl_ok(p.x, p.y, tag, lvl))) spin<1>(ws, vfh, tag, lvl); };
  auto check4 = [&](u32x4 (&p)[2], uint32_t base) { pin(p[0]); pin(p[1]);
    if (!all_of(lvl_ok(p[0].x, p[0].y, tag, 0) & lvl_ok(p[0].z, p[0].w, tag, 0) & lvl_ok(p[1].x, p[1].y, tag, 0) & lvl_ok(p[1].z, p[1].w, tag, 0))) spin<4>(ws, base + v16, tag, 0); };
  const int zb0 = ks < 2 ? 5 * ks : 4 * ks + 2;
  const int T2 = 256 + (xcd & 1) * 32 + jj;
  const bool dbl = ks == 3 && T2 <= 288;
  u32x4 tF64[2][2], tF32[2][2], tG32[2][2], tG64[2][2], tw[2][2], ur[2], wt[2], pu[2], hc[4][2], hown[4][2];
  u32x2 pl;

  const uint32_t a1 = rho * 128 + (((2 * hf) ^ ((rho >> 1) & 3)) * 32) + p4 * 8;
  uint32_t pa0 = 0, pa1 = 0;
  u32x4 fr[4];
  auto rd1 = [&](auto ST, auto S) {
    if constexpr (S == 0) { pa0 = oq_v(a1 + (ST & 3) * BUF); pa1 = oq_v(pa0 ^ 32); }
    return tr2(smem + ((S & 1) ? pa1 : pa0) + (S >> 1) * 4096, 1024); };
  auto ldu = [&](int T) { sfor<0, 2>([&](auto Q) { ld16c<Q * 1024>(ur[Q], rs, v16, WS_UT + (T == 288 ? 0 : T * 8192) + g * 2048); }); };
  auto putu = [&]() { sfor<0, 2>([&](auto Q) { pin(ur[Q]);
    *(LAS u32x4*)(smem + L_SP + (g >> 1) * 4096 + ((l >> 2) & 1) * 2048 + ((l >> 3) & 1) * 1024 + (l & 3) * 256 + (((8 * (g & 1) + 4 * Q + gq) ^ (l & 7)) * 16)) = ur[Q]; }); };
  auto ldt = [&](u32x4 (&t)[2][2], uint32_t so) { sfor<0, 2>([&](auto C) { sfor<0, 2>([&](auto A) { ld16<C * 2048 + A * 1024>(t[C][A], rt, v16, so); }); }); };
  auto ld2h = [&](u32x4 (&h)[2], uint32_t so) { ld16c<0>(h[0], rs, v16, so); ld16c<1024>(h[1], rs, v16, so); };
  uint32_t s4[4] = {};
  f32x4 bi[2];
  auto wtask = [&](int cl) {
    LAS char* p = smem + L_W + (l & 7) * 656 + (l & 1) * 2 + cl * 2 + (b >= 192 ? 12 : 0); unsigned v[8];
    sfor<0, 8>([&](auto J) { v[J] = *(LAS u16*)(p + (J >> 2) * 5248 + (J & 3) * 160); });
    const int kc = 72 * (b >> 6) + cl;
    st16c<0>(u32x4{v[0] | (v[1] << 16), v[2] | (v[3] << 16), v[4] | (v[5] << 16), v[6] | (v[7] << 16)}, rs,
             (uint32_t)(kc == 288 ? 288 : (kc & 1) ? 144 + (kc >> 1) : (kc >> 1)) * 8192 + (l & 7) * 16,
             WS_WT + ((b >> 1) & 3) * 2048 + ((b >> 5) & 1) * 1024 + ((b >> 3) & 3) * 256 + (b & 1) * 128); };
  auto stage = [&](auto ST, auto HF) {
    constexpr int R = ST >> 1, KB = 9 * (ST & 1);
    if constexpr (ST == 9) bar0(); else waitv_b<(ST == 2 ? 13 + 2 * HF : ST == 3 ? 13 + HF : ST == 5 ? 6 : ST == 8 ? 9 : 7)>();
    ST_(3 + ST);
    if constexpr (ST == 3) { pin(tabw); pin(tabh); pin(vo2a); pin(vo2b); tabh += sh + 36 * 8192 * ks; }
    if constexpr (ST == 5) { if (tid == 0) setflag(ws, WS_FU + b * 8, tag); }
    if constexpr (ST == 7 || ST == 9) { if (tid == 0) setflag(ws, WS_FH + b * 128, tag + (ST - 5) / 2); }
    if constexpr (ST <= 4) sfor<0, 3>([&](auto S) { fr[(18 * ST + S) & 3] = rd1(ST, S); });
    if constexpr (ST == 2) { bi[0] = bias(odd + hf, IC<0>{}); bi[1] = bias(odd + hf, IC<1>{}); }
    if constexpr (ST == 8) ld8c(pl, rs, vfh, 0);
    if constexpr (ST == 9) {
      if (hf) { tif<1>(); ldt(tF64, TAB_F64 + g * 4096); ld16<0>(tw[0][0], rt, v16, TAB_TW + g * 2048); ld16<1024>(tw[0][1], rt, v16, TAB_TW + g * 2048); }
      else { tif<0>(); sfor<0, 4>([&](auto I) { ld2h(hc[I], s4[I]); }); }
      tend<0>(); tend<1>();
    }
    Gelu ge; u32x4 o;
    SB();
    sfor<0, 18>([&](auto S) {
      constexpr int G = 18 * ST + S, FT = S & 1;
      if constexpr (LAD & 1) { if constexpr (S == 0) prio<3>(); if constexpr (S == 5) prio<2>(); if constexpr (S == 10) prio<1>(); if constexpr (S == 14) prio<0>(); }
      if constexpr (S >= 2 && !(S & 1)) { __builtin_amdgcn_s_waitcnt(0xC27F); SB(); }
      const u32x4 a = fr[G & 3];
      if constexpr ((ST & 1) == 0 && S < 2) c0[FT] = mmb(a, x0[KB + (S >> 1)], bi[FT]); else c0[FT] = mmb(a, x0[KB + (S >> 1)], c0[FT]);
      SB();
      if constexpr (S + 3 < 18) fr[(G + 3) & 3] = rd1(ST, IC<S + 3>{});
      else if constexpr (ST > 3 && ST != 9) fr[(G + 3) & 3] = rd1(IC<ST + 1>{}, IC<S + 3 - 18>{});
      SB();
      if constexpr ((ST & 1) == 0 && S < 2) c1[FT] = mmb(a, x1[KB + (S >> 1)], bi[FT]); else c1[FT] = mmb(a, x1[KB + (S >> 1)], c1[FT]);
      SB();
      if constexpr (HF != 1 && S >= 1 && S < 17) { if constexpr (ST & 1) ge.run<S - 1>(p1, o); else ge.run<S - 1>(p0, o); }
      if constexpr (ST == 2 && S % 3 == 2 && S / 3 < 5) dma1(IC<5>{}, IC<S / 3>{});
      if constexpr (S % 3 == 1 && S / 3 < 5) { if constexpr (ST < 7) dma1(IC<(ST == 2 ? 4 : ST + 3)>{}, IC<S / 3>{}); else dma2g(IC<S / 3>{}); }
      if constexpr (ST >= 7 && S % 3 == 0 && S / 3 < 5) dma2p(IC<ST - 7>{}, IC<S / 3>{});
      if constexpr (HF == 1 && ST == 3 && (S == 3 || S == 9)) wtask(8 * g + (S == 3 ? 0 : 32) + (l >> 3));
      if constexpr (ST >= 5 && ST < 8 && S == 15) { st16c<0>(u32x4{~0u, ~0u, ~0u, ~0u}, rs, v16, sbox + (ST - 5) * 8192);
        if constexpr (ST == 5) { if (w < 3) st16c<0>(u32x4{~0u, ~0u, ~0u, ~0u}, rs, v16, sbox + 24576); } }
      if constexpr (HF == 1 && ST == 3 && S == 15) { if (g == 0 || (g == 1 && b >= 192)) wtask(g == 0 ? 64 + (l >> 3) : 72); }
      if constexpr (ST == 7 && S == 14) { ld16c<0>(pu[0], rs, v16, WS_FU); ld16c<1024>(pu[1], rs, v16, WS_FU); }
      if constexpr (ST == 8 && S == 8) { waitv<6>(); if (hf) { tif<1>(); check4(pu, WS_FU); ldu(b); } tend<1>(); }
      if constexpr (ST == 8 && S == 17) { waitv<5>(); check1(pl, 1); }
      if constexpr (ST == 9 && S == 15) { if (hf) { tif<1>(); waitv<12>(); putu(); } tend<1>(); }
      if constexpr (HF != 1 && S == 17) { st16c<(ST & 1) * 1024>(o, rs, v16, R == 1 ? sh + (9 * fj + lone) * 8192 : sown + 2 * (R - 2) * 8192); if constexpr (R > 1) hown[R - 2][ST & 1] = o; }
      if constexpr ((ST & 1) && ST < 9 && S == 17) { bi[0] = bias(odd + 2 * R + hf, IC<0>{}); bi[1] = bias(odd + 2 * R + hf, IC<1>{}); }
      if constexpr (ST == 8 && S == 17) sfor<0, 4>([&](auto I) { s4[I] = lane_of<4 + I>(tabh); });
      SB();
    });
    if constexpr (ST & 1) { p0[0] = c0[0]; p0[1] = c0[1]; p1[0] = c1[0]; p1[1] = c1[1]; }
  };
  auto fill0 = [&](auto S) {
    if constexpr (S == 0) { v1a = oq_v(vo1 + 32 * w * 9216 + (288 * fj + 32 * odd) * 2); v1b = oq_v(vo1 + (256 + 16 * hf + 4 * ((w >> 1) & 1)) * 9216 + (288 * fj + 32 * odd) * 2);
      ST_(30); barl(); ST_(31); }
    else if constexpr (S < 6) dma1(IC<2>{}, IC<S - 1>{});
    else if constexpr (S < 11) dma1(IC<3>{}, IC<S - 6>{});
    else if constexpr (S < 13) {
      const int j = (l * 13) >> 7;
      dmah(mksrd(S == 12 ? wi : wr, 2048 * 578), j * 36992 + (l - 10 * j) * 16, ((8 * (b & 7) + w + 256 * ((b >> 3) & 7)) * 578 + (b >= 192 ? 420 : 144 * (b >> 6))) & ~3u,
           L_W + (S - 11) * 5248 + w * 656, (1ull << 40) - 1); }
    else if constexpr (S == 13) { ld4(tabw, rt, l * 4, TAB_AW + nt * 1024 + (w >> 1) * 256); ld4(tabh, rt, l * 4, TAB_AH + nt * 512 + hf * 256); }
    else if constexpr (S == 14) { ld4(vo2a, rt, l * 4, TAB_AV + ks * 2304 + w * 256); ld4(vo2b, rt, l * 4, TAB_AV + ks * 2304 + 2048); }
    else { constexpr int N = (324 - 4 * NW + 7) / 8; touch(r2, (jj * 324 + 4 * NW + w * N + l) * 128, ks * 1152 * 1152, (1ull << N) - 1);
      touch(rt, l < 6 ? TAB_TI + zb0 * 36864 + ((jj * 8 + w) * 6 + l) * 128 : TAB_F64 + ((jj * 8 + w) * 2 + l - 6) * 128, 0, 0xffull); } };
  const f32x4 z4 = {0, 0, 0, 0};
  const uint32_t su = WS_UT + (2 * mt + (g >> 1)) * 256 + (g & 1) * 128;
  auto ust = [&](const f32x4 (&u)[4], uint32_t so) { f32x4 a, c; const float sg = odd ? -1.0f : 1.0f;
    sfor<0, 4>([&](auto J) { a[(int)J] = __builtin_fmaf(u[2][(int)J], sg, u[0][(int)J]); c[(int)J] = __builtin_fmaf(u[3][(int)J], sg, u[1][(int)J]); });
    st16c<0>(u32x4{pk_h(a.x, a.y), pk_h(a.z, a.w), pk_h(c.x, c.y), pk_h(c.z, c.w)}, rs, (i16 >> 1) * 8192 + (i16 & 1) * 64 + gq * 16,
             so); };
  if (hf == 0) { tif<0>();
    ldx(); ld4(b1v, mksrd(b1, 9216), tid * 4, 576 * fj);
    bar0();
    const int i1 = jj * 144 + g * 36 + l;
    const uint32_t vt1 = oq_v((i1 >> 3) * 9216 + ((i1 >> 2) & 1) * 1152 + ((i1 >> 1) & 1) * 640 + (i1 & 1) * 128);
    sfor<0, 2>([&](auto RB) { stats(RB, IC<2>{}, [&](auto) {}); ST_(1 + RB);
      if constexpr (RB == 1 && PP == 0) touch(r1, vt1, ks * 2304, (1ull << 36) - 1);
      chain(RB); norm(RB, [&](auto C) { put(RB, C); }); });
    waitv<PP == 0>(); pin(b1v);
    if (tid < 144) *(LAS f32x2*)(smem + L_B1 + tid * 8) = f32x2{lo_f(b1v), hi_f(b1v)};
    ST_(28);
    barl();
    sfor<0, 18>([&](auto S) { rdx(x0, S); });
    if constexpr (PP == 1) touch(r1, vt1, ks * 2304, (1ull << 36) - 1);
    const uint32_t vo = (8 * ((l >> 4) & 1) + 4 * hh + ((l >> 2) & 3)) * 9216 + (((l & 3) ^ (((l >> 4) & 1) << 1)) * 16), so = (288 * fj + 32 * lone) * 2 + 16 * g * 9216;
    sfor<0, 9>([&](auto Q) { if constexpr (Q == 2) { barl(); ST_(3); }
      dma(r1, vo, so + 64 * Q * 9216, (g + 4 * Q) * 1024);
      if constexpr (Q < 4) dma(rt, v16, TAB_TF + tileA * 9216 + (g + 4 * Q) * 1024, BUF + (g + 4 * Q) * 1024); });
    if (g < 2) dma(rt, v16, TAB_TF + tileA * 9216 + (g + 16) * 1024, BUF + (g + 16) * 1024);
    if (ks == 2) { sfor<0, 2>([&](auto Q) { dma(rt, v16, TAB_TF + tileC * 9216 + (g + 4 * Q) * 1024, BUF + 18432 + (g + 4 * Q) * 1024); });
      if (g == 3) dma(rt, v16, TAB_TF + tileC * 9216 + 8192, BUF + 18432 + 8192); }
    if constexpr (PP == 2) touch(r1, vt1, ks * 2304, (1ull << 36) - 1);
    touch(r1, vt1, ks * 2304 + 256, (1ull << 36) - 1);
    touch(r2, (jj * 324 + g * NW + l) * 128, ks * 1152 * 1152, (1ull << NW) - 1);
    mktag();
    const f32x4 bl[2] = {bias(lone, F0{}), bias(lone, T1{})};
    const uint32_t al0 = oq_v(rho * 64 + (gq & 1) * 32 + p4 * 8), al1 = oq_v(al0 ^ 32);
    auto rd = [&](auto S) { return tr2(smem + ((S & 1) ? al1 : al0) + (S >> 1) * 2048, 512); };
    waitv<2 + (PP == 2)>(); ST_(29); bar0(); ST_(4);
    sfor<0, 3>([&](auto S) { fr[S] = rd(S); });
    sfor<0, 18>([&](auto S) { rdx(x1, S); }); SB();
    sfor<0, 36>([&](auto S) { const u32x4 a = fr[S & 3];
      p0[S & 1] = mmb(a, x0[S >> 1], S < 2 ? bl[S & 1] : p0[S & 1]); SB();
      if constexpr (S + 3 < 36) fr[(S + 3) & 3] = rd(IC<S + 3>{});
      SB();
      p1[S & 1] = mmb(a, x1[S >> 1], S < 2 ? bl[S & 1] : p1[S & 1]); SB();
      if constexpr (S >= 4 && !(S & 1)) fill0(IC<(S - 4) / 2>{});
      SB(); });
    ST_(32);
    if (ks == 2) { f32x4 uc[4]; const uint32_t at = oq_v(BUF + 18432 + v16), suC = oq_s(su + tileC * 65536);
      u32x4 tc[9];
      sfor<0, 9>([&](auto I) { tc[I] = *(LAS u32x4*)(smem + at + I * 1024); });
      SB();
      sfor<0, 9>([&](auto I) { const u32x4 t = tc[I];
        uc[0] = mmb(x0[I], t, I ? uc[0] : z4); uc[2] = mmb(x0[I + 9], t, I ? uc[2] : z4); uc[1] = mmb(x1[I], t, I ? uc[1] : z4); uc[3] = mmb(x1[I + 9], t, I ? uc[3] : z4); });
      ust(uc, suC); }
    ST_(33);
    stage(IC<2>{}, IC<0>{}); stage(IC<3>{}, IC<0>{});
  } else { tif<1>();
    if (w == 7) code_touch(jj, l);
    if (w == 6) { const int i = jj * 9 + l - 18;
      touch(rt, l < 18 ? (18 * (l & 1) + 4 * ks) * 9216 + (jj * 9 + (l >> 1)) * 128 : 16 * 9216 + (i >= 144 ? 147456 : 0) + i * 128, TAB_TF, ks == 2 ? (1ull << 27) - 1 : (1ull << 18) - 1); }
    if (w == 5) touch(r1, (jj * 18 + (l >> 1)) * 9216 + 512 + (l & 1) * 1152, ks * 2304, (1ull << 36) - 1);
    bar0();
    ldx();
    stats(F0{}, T1{}, [&](auto) {}); chain(F0{}); SB();
    ST_(1);
    bar0();
    ST_(2);
    norm(F0{}, [&](auto C) { rdx(x0, IC<2 * C>{}); rdx(x0, IC<2 * C + 1>{}); });
    ST_(28);
    barl();
    ST_(3);
    stats(T1{}, T1{}, [&](auto C) { put(F0{}, C); }); chain(T1{}); norm(T1{}, [&](auto C) { put(T1{}, C); });
    waitv<0>(); mktag();
    ST_(29); barl(); ST_(4);
    const uint32_t at = oq_v(BUF + v16), suA = oq_s(su + tileA * 65536);
    f32x4 ua[2][4];
    auto rd = [&](auto S) { return *(LAS u32x4*)(smem + at + (S & 1) * 9216 + (S >> 1) * 1024); };
    sfor<0, 2>([&](auto S) { fr[S] = rd(S); });
    sfor<0, 9>([&](auto S) { rdx(x1, S); rdx(x1, IC<S + 9>{}); }); SB();
    sfor<0, 18>([&](auto S) { constexpr int I = S >> 1, TT = S & 1; const u32x4 t = fr[S & 3];
      ua[TT][0] = mmb(x0[I], t, I ? ua[TT][0] : z4); SB();
      if constexpr (S + 2 < 18) fr[(S + 2) & 3] = rd(IC<S + 2>{});
      SB();
      ua[TT][2] = mmb(x0[I + 9], t, I ? ua[TT][2] : z4); SB();
      if constexpr (S >= 2) fill0(IC<S - 2>{});
      SB();
      ua[TT][1] = mmb(x1[I], t, I ? ua[TT][1] : z4); ua[TT][3] = mmb(x1[I + 9], t, I ? ua[TT][3] : z4); SB(); });
    ST_(32);
    ust(ua[0], suA); ust(ua[1], suA + 65536);
    ST_(33);
    if (PRIO & 1) __builtin_amdgcn_s_setprio(1);
    stage(IC<2>{}, IC<1>{}); stage(IC<3>{}, IC<1>{});
  }
  tend<0>(); tend<1>();
  sown = oq_s(sh + (9 * fj + odd + hf) * 8192); sbox = oq_s(WS_PART + b * 32768 + w * 1024);
  sfor<4, 10>([&](auto ST) { stage(ST, IC<2>{}); });

  f32x4 oo[9], os[9];
  u32x4 f6[6];
  const uint32_t a2 = (4 * gq + q4) * 288 + p4 * 8;
  uint32_t pb = 0;
  auto rd2 = [&](auto S) { f6[S % 6] = tr2(smem + pb + (S / 9) * 9216 + (S % 9) * 32, 4608); };
  auto pre = [&](uint32_t cur) { pb = oq_v(a2 + cur); sfor<0, 4>(rd2); SB(); };
  auto blocks = [&](auto NB, auto NEXT, auto ZERO, auto BOTH, uint32_t nxt, auto&& hfrag, auto&& fill) {
    sfor<0, 9 * NB>([&](auto S) {
      constexpr int B = S / 9, DT = S % 9;
      if constexpr (!(S & 1) && (NEXT || S + 4 < 9 * NB)) { __builtin_amdgcn_s_waitcnt(0xC47F); SB(); }
      const u32x4 a = f6[S % 6];
      oo[DT] = mmb(a, hfrag(IC<B>{}, F0{}), (ZERO && B == 0) ? z4 : oo[DT]);
      SB();
      if constexpr (S + 4 < 9 * NB) rd2(IC<S + 4>{});
      else if constexpr (NEXT) { if constexpr (S + 4 == 9 * NB) pb = oq_v(a2 + nxt); rd2(IC<S + 4 - 9 * NB>{}); }
      SB();
      if constexpr (BOTH) os[DT] = mmb(a, hfrag(IC<B>{}, T1{}), (ZERO && B == 0) ? z4 : os[DT]);
      SB();
      fill(S);
      SB();
    }); };
  auto lbuf = [&](auto J) { return (uint32_t)(((J + 2) & 3) * BUF); };
  const uint32_t sow4 = sown + 6 * 8192;
  u32x4 hr[3][2][2], hx[2][2][2];
  uint32_t sa = 0, sb = 0;
  auto ldh = [&](auto J, auto K, auto TT, uint32_t so) { ld16c<0>(hr[J % 3][K][TT], rs, TT ? vhs : vho, so); };
  auto ldh4 = [&](auto J) { sa = lane_of<4 * J>(tabh); sb = lane_of<4 * J + 1>(tabh); nop4(); sfor<0, 4>([&](auto I) { ldh(J, IC<I / 2>{}, IC<I & 1>{}, I < 2 ? sa : sb); }); };
  auto solo = [&](auto J, auto S) {
    if constexpr (S % 4 == 0 && S < 20) dma2p(IC<J + 3>{}, IC<S / 4>{});
    if constexpr (S % 4 == 1 && S < 20) dma2g(IC<S / 4>{});
    if constexpr (S == 19 || S == 28) { sa = lane_of<4 * (J + 2) + (S - 19) / 4>(tabh); sb = lane_of<4 * (J + 2) + (S - 19) / 4 + 1>(tabh); }
    if constexpr (S >= 21 && S < 29 && (S & 1)) ldh(IC<J + 2>{}, IC<(S - 21) / 4>{}, IC<((S - 21) / 2) & 1>{}, S < 25 ? sa : sb);
    if constexpr (S == 30) { if (dbl) { tif<2>(); ld2h(hx[J & 1][0], sa); ld2h(hx[J & 1][1], sb); } tend<2>(); } };
  auto pin4 = [&](u32x4 (&t)[2][2]) { pin(t[0][0]); pin(t[0][1]); pin(t[1][0]); pin(t[1][1]); };
  auto kill4 = [&](u32x4 (&t)[2][2]) { kill(t[0][0]); kill(t[0][1]); kill(t[1][0]); kill(t[1][1]); };
  auto hflag = [&](int lvl) { if (tid == 0) setflag(ws, WS_FH + b * 128, tag + lvl); };
  u32x4 uu[2][2][2], Br, Bi;
  auto rdu = [&](int T) { LAS char* pi = smem + L_SP + 2048 + gq * 256 + ((i16 ^ (4 + gq)) * 16); LAS char* pr = T == 288 ? pi : smem + L_SP + gq * 256 + ((i16 ^ gq) * 16);
    sfor<0, 2>([&](auto TT) { sfor<0, 2>([&](auto A) { uu[0][TT][A] = *(LAS u32x4*)(pr + TT * 4096 + A * 1024); uu[1][TT][A] = *(LAS u32x4*)(pi + TT * 4096 + A * 1024); }); }); };
  auto ldw = [&](int T) { sfor<0, 2>([&](auto P) { ld16c<P * 1024>(wt[P], rs, v16, WS_WT + T * 8192 + g * 2048); }); };
  auto real = [&](f32x4 (&P)[2], const f32x4 (&Q)[2]) { SB(); sfor<0, 8>([&](auto E) { float r = P[E >> 2][E & 3] - Q[E >> 2][E & 3]; fix(r); P[E >> 2][E & 3] = r; }); SB(); };
  auto segA = [&](int T) {
    if (T == 0 || T == 288) sfor<0, 2>([&](auto TT) { sfor<0, 2>([&](auto A) { uu[1][TT][A] = u32x4{0, 0, 0, 0}; }); });
    unsigned br[4], bq[4]; f32x4 P[2], Q[2], I[2]; float ta[8], tb[8];
    sfor<0, 2>([&](auto TT) { I[TT] = mmh(uu[0][TT][0], tF64[1][0], z4); I[TT] = mmh(uu[0][TT][1], tF64[1][1], I[TT]);
      I[TT] = mmh(uu[1][TT][0], tF64[0][0], I[TT]); I[TT] = mmh(uu[1][TT][1], tF64[0][1], I[TT]); });
    SB();
    sfor<0, 2>([&](auto TT) { P[TT] = mmh(uu[0][TT][0], tF64[0][0], z4); P[TT] = mmh(uu[0][TT][1], tF64[0][1], P[TT]);
      Q[TT] = mmh(uu[1][TT][0], tF64[1][0], z4); Q[TT] = mmh(uu[1][TT][1], tF64[1][1], Q[TT]); });
    real(P, Q);
    sfor<0, 8>([&](auto E) { const unsigned t = tw[0][E >> 2][E & 3]; ta[E] = xmul<0, 0>(P[E >> 2][E & 3], t); tb[E] = xmul<1, 1>(P[E >> 2][E & 3], t); });
    SB();
    sfor<0, 8>([&](auto X) { constexpr int E = (X & 3) * 2 + (X >> 2); const unsigned t = tw[0][E >> 2][E & 3];
      xfmah<1, E & 1>(br[E / 2], I[E >> 2][E & 3], t, ta[E]); xfmah<0, E & 1>(bq[E / 2], I[E >> 2][E & 3], t, tb[E]); if constexpr (X == 3) SB(); });
    Br = u32x4{br[0], br[1], br[2], br[3]}; Bi = u32x4{bq[0], bq[1], bq[2], bq[3]}; pin(Br); pin(Bi); };
  auto segB = [&](auto&& mid) {
    float ta[8], tb[8], wp[8], wq[8]; f32x4 P[2], Q[2], I[2];
    pin(wt[0]); pin(wt[1]);
    sfor<0, 2>([&](auto TT) { I[TT] = mmh(tF32[1][TT], Br, z4); I[TT] = mmh(tF32[0][TT], Bi, I[TT]); });
    SB();
    sfor<0, 2>([&](auto TT) { P[TT] = mmh(tF32[0][TT], Br, z4); Q[TT] = mmh(tF32[1][TT], Bi, z4); });
    SB();
    sfor<0, 8>([&](auto E) { const unsigned a = wt[E >> 2][(E >> 1) & 1], c = wt[E >> 2][2 + ((E >> 1) & 1)]; wp[E] = (E & 1) ? hi_f(a) : lo_f(a); wq[E] = (E & 1) ? hi_f(c) : lo_f(c); });
    real(P, Q);
    sfor<0, 8>([&](auto E) { ta[E] = vmul(P[E >> 2][E & 3], wp[E]); tb[E] = vmul(P[E >> 2][E & 3], wq[E]); });
    SB();
    sfor<0, 8>([&](auto E) { ta[E] = vfms(I[E >> 2][E & 3], wq[E], ta[E]); tb[E] = vfma(I[E >> 2][E & 3], wp[E], tb[E]); });
    SB();
    const u32x4 Yr = {pk_bf(ta[0], ta[1]), pk_bf(ta[2], ta[3]), pk_bf(ta[4], ta[5]), pk_bf(ta[6], ta[7])};
    const u32x4 Yi = {pk_bf(tb[0], tb[1]), pk_bf(tb[2], tb[3]), pk_bf(tb[4], tb[5]), pk_bf(tb[6], tb[7])};
    SB();
    sfor<0, 2>([&](auto TT) { I[TT] = mmb(Yr, tG32[1][TT], z4); I[TT] = mmb(Yi, tG32[0][TT], I[TT]); });
    SB();
    sfor<0, 2>([&](auto TT) { P[TT] = mmb(Yr, tG32[0][TT], z4); Q[TT] = mmb(Yi, tG32[1][TT], z4); });
    SB(); mid();
    real(P, Q);
    sfor<0, 8>([&](auto E) { const unsigned t = tw[1][E >> 2][E & 3]; ta[E] = xmul<0, 0>(P[E >> 2][E & 3], t); tb[E] = xmul<1, 0>(P[E >> 2][E & 3], t); });
    SB();
    sfor<0, 8>([&](auto E) { const unsigned t = tw[1][E >> 2][E & 3]; ta[E] = xfma<1, 1>(I[E >> 2][E & 3], t, ta[E]); tb[E] = xfma<0, 0>(I[E >> 2][E & 3], t, tb[E]); });
    SB();
    sfor<0, 2>([&](auto TT) {
      LAS char* p = smem + L_SP + 8192 + (16 * TT + i16) * 128 + (((2 * g + (gq >> 1)) ^ (i16 >> 1)) * 16) + (gq & 1) * 8;
      *(LAS u32x2*)p = u32x2{pk_bf(ta[4 * TT], ta[4 * TT + 1]), pk_bf(ta[4 * TT + 2], ta[4 * TT + 3])};
      *(LAS u32x2*)(p + 4096) = u32x2{pk_bf(tb[4 * TT], tb[4 * TT + 1]), pk_bf(tb[4 * TT + 2], tb[4 * TT + 3])};
    }); };
  auto segC = [&](int T, auto&& mid) {
    const uint32_t vz = (i16 >> 1) * 147456 + (i16 & 1) * 64 + (gq & 1) * 32 + (gq >> 1) * 16, sz = oq_s(WS_ZT + (T == 288 ? 32 : T * 512) + g * 128);
    const u64 zmask = (T == 0 || T == 288) ? 0x0000ffff0000ffffull : ~0ull;
    sfor<0, 2>([&](auto TT) {
      LAS char* q0 = smem + L_SP + 8192 + (16 * TT + i16) * 128 + ((gq ^ (i16 >> 1)) * 16); LAS char* q1 = smem + L_SP + 8192 + (16 * TT + i16) * 128 + (((gq + 4) ^ (i16 >> 1)) * 16);
      uu[0][TT][0] = *(LAS u32x4*)q0; uu[0][TT][1] = *(LAS u32x4*)q1; uu[1][TT][0] = *(LAS u32x4*)(q0 + 4096); uu[1][TT][1] = *(LAS u32x4*)(q1 + 4096); });
    SB(); mid(); SB();
    sfor<0, 2>([&](auto TT) {
      const u32x4 r0 = uu[0][TT][0], r1_ = uu[0][TT][1], i0 = uu[1][TT][0], i1 = uu[1][TT][1];
      f32x4 P = mmb(tG64[0][0], r0, z4); P = mmb(tG64[0][1], r1_, P);
      f32x4 Q = mmb(tG64[1][0], i0, z4); Q = mmb(tG64[1][1], i1, Q);
      f32x4 I = mmb(tG64[1][0], r0, z4); I = mmb(tG64[1][1], r1_, I); I = mmb(tG64[0][0], i0, I); I = mmb(tG64[0][1], i1, I);
      unsigned ra = pk_bf(P.x - Q.x, P.y - Q.y), rb = pk_bf(P.z - Q.z, P.w - Q.w), ia = pk_bf(I.x, I.y), ib = pk_bf(I.z, I.w);
      auto sa = __builtin_amdgcn_permlane16_swap(ra, ia, false, false); auto sb = __builtin_amdgcn_permlane16_swap(rb, ib, false, false);
      const unsigned sa0 = sa[0], sa1 = sa[1], sb0 = sb[0], sb1 = sb[1];
      st16m(u32x4{sa0, sb0, sa1, sb1}, rs, vz, sz + TT * 8 * 147456, zmask);
    }); };
  auto dmas = [&](auto J) { uint32_t t[5]; constexpr uint32_t lb = ((J + 2) & 3) * BUF;
    sfor<0, 5>([&](auto I) { t[I] = lane_of<5 * J + I>(tabw); });
    sfor<0, 4>([&](auto I) { setm0<lb + I * 9216>(sw1); nop0(); dmago(r2, vo2a, t[I]); });
    setm0<lb + 8192>(sh9); dmagoh(r2, vo2b, t[4], hmask); };
  waitv_lb<15>();
  ST_(13);
  ld8c(pl, rs, vfh, 0);
  if (hf == 0) { tif<0>();
    u32x4 hd[4][2];
    Gelu ge, g2;
    pre(lbuf(F0{}));
    blocks(IC<4>{}, T1{}, T1{}, T1{}, lbuf(T1{}), [&](auto B, auto TT) { return hown[B][TT]; },
      [&](auto S) { if constexpr (S < 16) { ge.run<S>(p0, hown[3][0]); g2.run<S>(p1, hown[3][1]); }
        if constexpr (S == 16) st16c<0>(hown[3][0], rs, v16, sow4); if constexpr (S == 17) st16c<1024>(hown[3][1], rs, v16, sow4);
        if constexpr (S >= 17 && S < 25 && (S & 1)) sa = lane_of<8 + (S - 17) / 2>(tabh);
        if constexpr (S >= 18 && S < 26 && !(S & 1)) ld2h(hd[(S - 18) / 2], sa);
        if constexpr (S >= 25 && S < 35 && (S & 1)) dma2p(IC<3>{}, IC<(S - 25) / 2>{});
        if constexpr (S >= 26 && !(S & 1)) dma2g(IC<(S - 26) / 2>{}); });
    ST_(34);
    waitv_b<15>();
    ST_(14);
    hflag(3); check1(pl, 2);
    sfor<0, 4>([&](auto I) { pin(hc[I][0]); pin(hc[I][1]); });
    blocks(IC<4>{}, T1{}, F0{}, T1{}, lbuf(IC<2>{}), [&](auto B, auto TT) { return hc[B][TT]; },
      [&](auto S) { solo(T1{}, S); });
    ST_(35);
    waitv_b<9>();
    ST_(15);
    hflag(4); ld8c(pl, rs, vfh, 0);
    sfor<0, 4>([&](auto I) { pin(hd[I][0]); pin(hd[I][1]); });
    blocks(IC<4>{}, T1{}, F0{}, T1{}, lbuf(IC<3>{}), [&](auto B, auto TT) { return hd[B][TT]; },
      [&](auto S) { solo(IC<2>{}, S); });
    ST_(36);
    kill4(tF64); kill(tw[0][0]); kill(tw[0][1]); kill(p0[0]); kill(p0[1]); kill(p1[0]); kill(p1[1]);
  }
  tend<0>();
  if (oq_s(hf)) { tif<1>();
    if (PRIO & 2) __builtin_amdgcn_s_setprio(1);
    rdu(b);
    { Gelu ge, g2; u32x4 o0, o1;
      const uint32_t g2k = oq_s(g * 2048);
      sfor<0, 16>([&](auto S) { constexpr int C = (S >> 1) & 1, A = S & 1;
        if constexpr (S < 2) ld16c<A * 1024>(wt[A], rs, v16, WS_WT + b * 8192 + g2k);
        else if constexpr (S < 4) ld16<A * 1024>(tw[1][A], rt, v16, TAB_TW + 8192 + g2k);
        else ld16<C * 2048 + A * 1024>((S < 8 ? tF32 : S < 12 ? tG32 : tG64)[C][A], rt, v16, S < 8 ? TAB_F32 : S < 12 ? TAB_G32 : TAB_G64 + 2 * g2k);
        ge.run<S>(p0, o0); g2.run<S>(p1, o1); SB(); });
      if (dbl) { tif<2>(); ldu(T2); } tend<2>();
      st16c<0>(o0, rs, v16, sow4); st16c<1024>(o1, rs, v16, sow4); }
    waitv<25>(); pin4(tF64); pin(tw[0][0]); pin(tw[0][1]);
    segA(b); SB();
    dmas(IC<3>{});
    ST_(34);
    waitv_b<11>();
    ST_(14);
    check1(pl, 2); pin4(tF32); pin4(tG32); pin(tw[1][0]); pin(tw[1][1]);
    segB([&]() { dmas(IC<4>{}); });
    if (dbl) { tif<2>(); waitv<12>(); putu(); ldw(T2); kill4(hr[0]); } else { tif<3>(); ldh4(IC<3>{}); }
    tend<2>(); tend<3>();
    ST_(35);
    waitv_lb<7>();
    ST_(15);
    ld8c(pl, rs, vfh, 0); pin4(tG64);
    segC(b, [&]() { dmas(IC<5>{}); });
    ST_(36);
    if (dbl) { tif<2>();
      rdu(T2); segA(T2); SB();
      waitv_b<7>();
      check1(pl, 3);
      segB([&]() { dmas(IC<6>{}); });
      ldh4(IC<5>{});
      waitv_lb<9>();
      ld8c(pl, rs, vfh, 0);
      segC(T2, [&]() {});
      dmas(IC<7>{});
      ldh4(IC<6>{});
      kill4(hr[1]);
    } else { tif<3>(); ldh4(IC<4>{}); }
    tend<2>(); tend<3>();
    pre((dbl ? lbuf(IC<5>{}) : lbuf(IC<3>{})) + 18432);
    sfor<0, 9>([&](auto DT) { oo[DT] = z4; os[DT] = z4; });
    kill4(hx[0]); kill4(hx[1]);
    if (PRIO & 4) __builtin_amdgcn_s_setprio(0);
  }
  tend<1>();
  u32x4 zr[5], xv, bv;
  u32x2 xv8, bv8;
  uint32_t vta = 0, vtb = 0;
  auto dmat = [&](auto I) {
    if constexpr (I == 0) { const uint32_t t = TAB_TI + (zb0 * 4 + nt) * 9216 - ks * 36 * 36864, c = oq_v(l);
      vta = vo2a - 864 * (((w * 64 + c) * 3641) >> 16) + t; vtb = vo2b - 864 * (((512 + c) * 3641) >> 16) + t + 4 * sh9; }
    if constexpr (I < 4) dmak<3 * BUF + I * 9216, I * 36864>(rt, vta, sw1);
    else if constexpr (I == 4) dmakh<3 * BUF + 8192, 0>(rt, vtb, sh9, hmask);
    else if (ks < 2) { dmak<L_SP, 4 * 36864>(rt, vta, sw1); if (w == 0) dmak<L_SP + 8192, 4 * 36864>(rt, vtb, sh9); }
  };
  const uint32_t azw = (4 * w + 2 * hh + ((l >> 1) & 1)) * 288 + (((l >> 4) & 1) + 2 * ((l >> 2) & 1)) * 64 + ((l >> 3) & 1) * 32 + (l & 1) * 16;
  const uint32_t srow = sx + 288 * nt, vrow = (l >> 2) * 36864;
  auto shared = [&](auto J) {
    if constexpr (J == 8) bar0(); else waitv_b<9>();
    ST_(13 + J);
    if constexpr (J == 3) check1(pl, 3);
    if constexpr (J == 4) { if (tid == 0 && !dbl) setflag(ws, WS_FZ + b * 8, tag); ld8c(pl, rs, vfh, 0); }
    if constexpr (J == 5) check1(pl, 4);
    if constexpr (J == 5) { if (tid == 0 && dbl) setflag(ws, WS_FZ + b * 8, tag); }
    if constexpr (J == 7) { ld16c<0>(pu[0], rs, v16, WS_FZ); ld16c<1024>(pu[1], rs, v16, WS_FZ); }
    pin4(hr[J % 3]);
    SB();
    const uint32_t nxt = lbuf(IC<J + 1>{}) + hf * 18432;
    blocks(IC<2>{}, T1{}, F0{}, T1{}, (J < 5 && dbl) ? lbuf(J) + 18432 : nxt, [&](auto B, auto TT) { return hr[J % 3][B][TT]; },
      [&](auto S) {
        if constexpr (LAD & 2) { if constexpr (S == 17) prio<3>(); if constexpr (S == 4) prio<2>(); if constexpr (S == 9) prio<1>(); if constexpr (S == 13) prio<0>(); }
        if constexpr (J < 6) { if constexpr (S % 4 == 0) dma2p(IC<J + 3>{}, IC<S / 4>{}); if constexpr (S % 4 == 1) dma2g(IC<S / 4>{}); }
        if constexpr (J == 6) { if constexpr (S % 4 == 1 && S < 16) dmat(IC<S / 4>{}); if constexpr (S >= 16) dmat(IC<S - 12>{}); }
        if constexpr (J < 7) { if constexpr (S % 8 == 1) sa = lane_of<4 * (J + 2) + S / 8>(tabh); if constexpr (S % 4 == 3) ldh(IC<J + 2>{}, IC<S / 8>{}, IC<(S / 4) & 1>{}, sa); }
        if constexpr (J == 7) {
          if constexpr (S == 8) { waitv<0>(); check4(pu, WS_FZ); }
          if constexpr (S >= 8 && S < 13) { if constexpr (S == 8) sa = oq_s(WS_ZT + mt * 147456 + zb0 * 8192 + sw1); ld16c<0>(zr[S - 8], rs, v16, sa + (S == 12 && ks >= 2 ? 0 : (int)S - 8) * 8192); }
          if constexpr (S == 14) { ld16<0>(xv, rx, vrow + (l & 3) * 16, srow + 64 * ks); ld8<256>(xv8, rx, vrow + (l & 3) * 8, srow); }
          if constexpr (S == 16) { const u32x4 rb = mksrd(b2, 1152); ld16<0>(bv, rb, (l & 3) * 16, 288 * nt + 64 * ks); ld8<256>(bv8, rb, (l & 3) * 8, 288 * nt); } }
        if constexpr (J == 8 && S >= 13) { waitv<21 - S>(); pin(zr[S - 13]); *(LAS u32x4*)(smem + azw + (S - 13) * 9216) = zr[S - 13]; }
      });
    if constexpr (J < 5) { if (dbl) { tif<2>(); pin4(hx[J & 1]); blocks(IC<2>{}, T1{}, F0{}, T1{}, nxt, [&](auto B, auto TT) { return hx[J & 1][B][TT]; }, [&](auto) {}); } tend<2>(); }
  };
  if (!(oq_s(hf) && dbl)) { tif<5>(); shared(IC<3>{}); shared(IC<4>{}); }
  tend<5>();
  sfor<5, 9>(shared);
  { barl();
    ST_(22);
    const uint32_t az = oq_v(a2 + hf * 18432 + g * 64);
    u32x4 z[2][2];
    sfor<0, 2>([&](auto K) { z[K][0] = tr2(smem + az + K * 9216 + hf * 32, 4608); z[K][1] = tr2(smem + az + K * 9216 + (hf ^ 1) * 32, 4608); });
    const u32x4 z5 = tr2(smem + BUF + a2 + g * 64 + hf * 32, 4608);
    const uint32_t aw = oq_v((hf ? 0 : 2 * BUF) + g * 9216 + v16);
    SB();
    blocks(IC<2>{}, T1{}, F0{}, T1{}, L_SP, [&](auto B, auto TT) { return z[B][TT]; },
      [&](auto S) { if constexpr (S == 9) bar0(); if constexpr (S >= 10) *(LAS f32x4*)(smem + aw + (S - 10) * 1024) = os[S - 10]; });
    *(LAS f32x4*)(smem + aw + 8192) = os[8];
    ST_(23);
    if (ks < 2) blocks(T1{}, F0{}, F0{}, F0{}, 0, [&](auto B, auto TT) { return z5; }, [&](auto S) {});
    barl();
    ST_(24);
    const uint32_t ar = oq_v((hf ? 2 * BUF : 0) + g * 9216 + v16);
    sfor<0, 9>([&](auto DT) { oo[(DT + 2) % 9] += *(LAS f32x4*)(smem + ar + ((DT + 2) % 9) * 1024); });
  }
  auto pk1 = [&](float p, float q) { const unsigned v = pk_bf(p, q); return v < ~1u ? v : ~1u; };
  sfor<1, 4>([&](auto K) { const f32x4 p = oo[2 * K], q = oo[2 * K + 1];
    st16c<0>(u32x4{pk1(p.x, p.y), pk1(p.z, p.w), pk1(q.x, q.y), pk1(q.z, q.w)}, rs, v16, WS_PART + ((b & ~6) + ((ks + K) & 3) * 2) * 32768 + ((K - 1) * 8 + w) * 1024); });
  if (g != ks) st8c(u32x2{pk1(oo[8].x, oo[8].y), pk1(oo[8].z, oo[8].w)}, rs, l * 8, WS_PART + ((b & ~6) + g * 2) * 32768 + 24576 + (((g - ks) & 3) - 1) * 1024 + hf * 512);
  ST_(25);
  { u32x4 pp[3]; u32x2 p8[3];
    const uint32_t s8 = WS_PART + b * 32768 + (g == ks ? 24576 + hf * 512 : w * 1024);
#pragma nounroll
    for (unsigned spins = 0; spins < (1u << 16); ++spins) {
      sfor<0, 3>([&](auto K) { ld16c<0>(pp[K], rs, v16, WS_PART + b * 32768 + (K * 8 + w) * 1024); ld8c(p8[K], rs, l * 8, s8 + (g == ks ? K * 1024 : 0)); });
      waitv<0>();
      unsigned m = 0;
      sfor<0, 3>([&](auto K) { pin(pp[K]); pin(p8[K]); const unsigned a = pp[K].x, c = pp[K].y, d = pp[K].z, e = pp[K].w, f = p8[K].x, h = p8[K].y;
        m = max(max(max(m, a), max(c, d)), max(max(e, f), h)); });
      if (__all(m != ~0u)) break;
      __builtin_amdgcn_s_sleep(2);
    }
    ST_(26);
    LAS char* pt = smem + BUF + w * 3072;
    sfor<0, 3>([&](auto Q) { f32x4 r = oo[Q < 2 ? (int)Q : 8];
      sfor<0, 3>([&](auto K) { const unsigned a = Q == 2 ? p8[K].x : pp[K][2 * (Q & 1)], c = Q == 2 ? p8[K].y : pp[K][2 * (Q & 1) + 1];
        r += f32x4{lo_f(a), hi_f(a), lo_f(c), hi_f(c)}; });
      *(LAS f32x4*)(pt + Q * 1024 + i16 * 64 + gq * 16) = r; });
    pin(xv); pin(bv); pin(xv8); pin(bv8);
    LAS char* pq = pt + ((l >> 1) & 1) * 1024 + (l >> 2) * 64 + (l & 1) * 32;
    const f32x4 ra = *(LAS f32x4*)pq, rb = *(LAS f32x4*)(pq + 16), rc = *(LAS f32x4*)(pt + 2048 + v16);
    auto fin = [&](float a, float c, unsigned xx, unsigned bb) { return pk_bf(a + lo_f(xx) + lo_f(bb), c + hi_f(xx) + hi_f(bb)); };
    char* op = out + srow + vrow;
    *(u32x4*)(op + 64 * ks + (l & 3) * 16) = u32x4{fin(ra.x, ra.y, xv.x, bv.x), fin(ra.z, ra.w, xv.y, bv.y), fin(rb.x, rb.y, xv.z, bv.z), fin(rb.z, rb.w, xv.w, bv.w)};
    if (g == ks) *(u32x2*)(op + 256 + (l & 3) * 8) = u32x2{fin(rc.x, rc.y, xv8.x, bv8.x), fin(rc.z, rc.w, xv8.y, bv8.y)};
  }
  if (b == 0 && tid == 0) setflag(ws, WS_EPOCH, tag);
  ST_(27);
  asm volatile(".Lfk_end:");
}

extern "C" void kernel_launch(void* const* d_in, const int* in_sizes, int n_in, void* d_out, int out_size, void* d_ws, size_t ws_size, hipStream_t stream) {
  (void)hipFuncSetAttribute((const void*)fused_kernel, hipFuncAttributeMaxDynamicSharedMemorySize, LDS_BYTES);
  fused_kernel<<<256, 512, LDS_BYTES, stream>>>((const char*)d_in[0], (const char*)d_in[1], (const char*)d_in[2], (const char*)d_in[4], (const char*)d_in[5],
                                                (const char*)d_in[6], (const char*)d_in[7], (char*)d_out, (char*)d_ws);
}
```

```cpp
#include <hip/hip_runtime.h>
#include <stdint.h>
#include <type_traits>
#define LAD 3
#define PP 0
#define NW 63
#define PRIO 0

typedef unsigned short u16;
typedef short s16x4 __attribute__((ext_vector_type(4)));
typedef short s16x8 __attribute__((ext_vector_type(8)));
typedef __bf16 bf16x8 __attribute__((ext_vector_type(8)));
typedef __bf16 bf16x2 __attribute__((ext_vector_type(2)));
typedef _Float16 f16x8 __attribute__((ext_vector_type(8)));
typedef _Float16 f16x2 __attribute__((ext_vector_type(2)));
typedef float f32x4 __attribute__((ext_vector_type(4)));
typedef float f32x2 __attribute__((ext_vector_type(2)));
typedef unsigned u32x4 __attribute__((ext_vector_type(4)));
typedef unsigned u32x2 __attribute__((ext_vector_type(2)));
typedef unsigned long long u64;
#define LAS __attribute__((address_space(3)))
#define GAS __attribute__((address_space(1)))
#define DI __device__ __forceinline__

namespace tab {
constexpr double PI = 3.14159265358979323846;
constexpr double ccos(int a, int n) {
  a %= n; if (a < 0) a += n;
  if (2 * a > n) a = n - a;
  bool neg = false;
  if (4 * a > n) { a = n / 2 - a; neg = true; }
  if (4 * a == n) return 0.0;
  double x = 2 * PI * a / n, t = 1, s = 1;
  for (int i = 1; i <= 13; i++) { t *= -x * x / ((2 * i - 1) * (2 * i)); s += t; }
  return neg ? -s : s;
}
constexpr u16 to_bf(double v) { float f = (float)v; uint32_t u = __builtin_bit_cast(uint32_t, f); u += 0x7fffu + ((u >> 16) & 1u); return (u16)(u >> 16); }
constexpr u16 to_h(double v) { _Float16 h = (_Float16)v; return __builtin_bit_cast(u16, h); }
struct C576 { u16 b1[576]; u16 b2[576]; };
constexpr C576 gen576() { C576 r{}; for (int i = 0; i < 576; i++) { double c = ccos(i, 576) / 24.0; r.b1[i] = to_bf(c); r.b2[i] = to_bf(2 * c); } return r; }
constexpr C576 c576 = gen576();
constexpr int colk(int c) { return c == 0 ? 0 : c == 1 ? 288 : c < 288 ? (c & ~1) : ((c - 288) & ~1) + 1; }
constexpr int colp(int c) { return c < 2 ? 0 : (c & 1); }
struct Chunk { u16 v[9216]; };
constexpr Chunk genTF(int u) {
  Chunk r{};
  for (int i = 0; i < 1152; i++) { int t = i / 576, s = (i % 576) >> 6, l = i & 63, c = 32 * u + 16 * t + (l & 15), k = colk(c), o = 144 * colp(c), d = 32 * s + 8 * (l >> 4);
    for (int j = 0; j < 8; j++) r.v[i * 8 + j] = c576.b1[((d + j) * k + o) % 576]; }
  return r;
}
constexpr Chunk genTI(int q) {
  Chunk r{};
  for (int i = 0; i < 64; i++) { int c = 32 * (q >> 1) + (i & 31), k = colk(c), o = 144 * colp(c), d0 = 144 * (2 * (q & 1) + (i >> 5));
    if (c < 2) { for (int d = 0; d < 144; d++) r.v[i * 144 + d] = c576.b1[((d0 + d) * k + o) % 576]; }
    else { for (int d = 0; d < 144; d++) r.v[i * 144 + d] = c576.b2[((d0 + d) * k + o) % 576]; } }
  return r;
}
template <int I> constexpr Chunk cTF = genTF(I);
template <int I> constexpr Chunk cTI = genTI(I);
struct Small { u16 f64[4][2][2][512]; u16 f32[2][2][512]; u16 g32[2][2][512]; u16 g64[4][2][2][512]; };
constexpr Small genSmall() {
  Small r{};
  double c64[64] = {}; for (int i = 0; i < 64; i++) c64[i] = ccos(i, 64);
  const double r32 = 0.17677669529663688110;
  for (int l = 0; l < 64; l++) for (int j = 0; j < 8; j++) {
    int rr = l & 15, gq = l >> 4;
    int p2 = 16 * (j >> 2) + 4 * gq + (j & 3);
    for (int w = 0; w < 4; w++) for (int a = 0; a < 2; a++) {
      int e = (16 * w + rr) * (32 * a + 8 * gq + j), e1 = (16 * w + rr) * (32 * a + p2);
      r.f64[w][0][a][l * 8 + j] = to_h(c64[e1 & 63] / 8); r.f64[w][1][a][l * 8 + j] = to_h(c64[(e1 + 16) & 63] / 8);
      r.g64[w][0][a][l * 8 + j] = to_bf(c64[e & 63] / 8); r.g64[w][1][a][l * 8 + j] = to_bf(c64[(e + 48) & 63] / 8);
    }
    for (int t = 0; t < 2; t++) {
      int e = 2 * ((16 * t + rr) * p2);
      r.f32[0][t][l * 8 + j] = to_h(c64[e & 63] * r32); r.f32[1][t][l * 8 + j] = to_h(c64[(e + 16) & 63] * r32);
      r.g32[0][t][l * 8 + j] = to_bf(c64[e & 63] * r32); r.g32[1][t][l * 8 + j] = to_bf(c64[(e + 48) & 63] * r32);
    }
  }
  return r;
}
struct C2048 { u16 v[2048]; };
constexpr C2048 gen2048() { C2048 r{}; for (int i = 0; i < 2048; i++) r.v[i] = to_h(ccos(i, 2048)); return r; }
constexpr C2048 c2048 = gen2048();
struct Tw { u16 v[2][4][2][512]; };
constexpr Tw genTw() {
  Tw r{};
  for (int w = 0; w < 4; w++) for (int t = 0; t < 2; t++) for (int l = 0; l < 64; l++) for (int j = 0; j < 4; j++) {
    int e0 = (16 * t + 4 * (l >> 4) + j) * (16 * w + (l & 15)), e1 = (16 * t + (l & 15)) * (16 * w + 4 * (l >> 4) + j);
    r.v[0][w][t][l * 8 + 2 * j] = c2048.v[e0 & 2047]; r.v[0][w][t][l * 8 + 2 * j + 1] = c2048.v[(e0 + 1536) & 2047];
    r.v[1][w][t][l * 8 + 2 * j] = c2048.v[e1 & 2047]; r.v[1][w][t][l * 8 + 2 * j + 1] = c2048.v[(e1 + 1536) & 2047]; }
  return r;
}
constexpr Tw ctw = genTw();
constexpr int fblk(int nt, int J, int i) {
  if (J == 0) return 9 * nt + (nt & 1) + 2 * i;
  if (J == 1) { int pn = (nt + i) & 3; return 9 * pn + ((pn & 1) ? 0 : 8); }
  int q = 4 * (J - 2) + i, r = q / 7, m = q - 7 * r + 1, pn = (nt + (m >> 1)) & 3;
  return 9 * pn + (pn & 1) + 2 * r + (m & 1);
}
struct Adr { uint32_t w[4][4][64]; uint32_t h[4][2][64]; uint32_t v[4][576]; };
constexpr Adr genAdr() {
  Adr r{};
  for (int nt = 0; nt < 4; nt++) {
    for (int k = 0; k < 4; k++) for (int l = 0; l < 45; l++) r.w[nt][k][l] = fblk(nt, l / 5, l % 5 < 4 ? l % 5 : k) * 36864 + 288 * nt;
    for (int k = 0; k < 2; k++) for (int l = 0; l < 36; l++) r.h[nt][k][l] = fblk(nt, l / 4, (l + 2 * k) & 3) * 8192;
    for (int c = 0; c < 576; c++) { int k = c % 18; r.v[nt][c] = (c / 18) * 1152 + (k < 16 ? ((k + 4 * nt) & 15) : k) * 16 + nt * 36 * 36864; } }
  return r;
}
struct alignas(256) All { Chunk tf[18]; Chunk ti[36]; Small sm; Tw tw; Adr adr; };
template <int... I, int... J> constexpr All mk(std::integer_sequence<int, I...>, std::integer_sequence<int, J...>) { return All{{cTF<I>...}, {cTI<J>...}, genSmall(), ctw, genAdr()}; }
}
static __device__ const tab::All g_tab = tab::mk(std::make_integer_sequence<int, 18>{}, std::make_integer_sequence<int, 36>{});

constexpr uint32_t BUF = 36864, L_SP = 147456, L_B1 = L_SP, L_JUNK = L_SP + 2048, L_W = L_SP + 4096, LDS_BYTES = 163840;
constexpr uint32_t WS_EPOCH = 0, WS_FU = 4096, WS_FZ = 8192, WS_FH = 16384;
constexpr uint32_t WS_H = 1u << 20, WS_UT = 20u << 20, WS_WT = 23u << 20, WS_ZT = 26u << 20, WS_PART = 29u << 20, WS_END = 48u << 20;
constexpr uint32_t TAB_TF = 0, TAB_TI = 18 * 18432, TAB_F64 = 54 * 18432, TAB_F32 = TAB_F64 + 16384, TAB_G32 = TAB_F32 + 4096, TAB_G64 = TAB_G32 + 4096, TAB_TW = TAB_G64 + 16384, TAB_AW = TAB_TW + 16384, TAB_AH = TAB_AW + 4096, TAB_AV = TAB_AH + 2048, TAB_END = TAB_AV + 9216;

template <int I, int N, class Fn> DI void sfor(Fn&& f) { if constexpr (I < N) { f(std::integral_constant<int, I>{}); sfor<I + 1, N>(f); } }
template <int V> using IC = std::integral_constant<int, V>;

DI u32x4 mksrd(const void* p, uint32_t n) { return u32x4{(unsigned)(size_t)p, (unsigned)((size_t)p >> 32) & 0xffffu, n, 0x00020000u}; }
template <int IMM> DI void ld16(u32x4& d, u32x4 srd, uint32_t voff, uint32_t soff) { asm volatile("buffer_load_dwordx4 %0, %1, %2, %3 offen offset:%4" : "=v"(d) : "v"(voff), "s"(srd), "s"(soff), "i"(IMM) : "memory"); }
template <int IMM> DI void ld16c(u32x4& d, u32x4 srd, uint32_t voff, uint32_t soff) { asm volatile("buffer_load_dwordx4 %0, %1, %2, %3 offen offset:%4 sc1" : "=v"(d) : "v"(voff), "s"(srd), "s"(soff), "i"(IMM) : "memory"); }
template <int IMM> DI void ld8(u32x2& d, u32x4 srd, uint32_t voff, uint32_t soff) { asm volatile("buffer_load_dwordx2 %0, %1, %2, %3 offen offset:%4" : "=v"(d) : "v"(voff), "s"(srd), "s"(soff), "i"(IMM) : "memory"); }
DI void ld8c(u32x2& d, u32x4 srd, uint32_t voff, uint32_t soff) { asm volatile("buffer_load_dwordx2 %0, %1, %2, %3 offen sc1" : "=v"(d) : "v"(voff), "s"(srd), "s"(soff) : "memory"); }
DI void ld4(unsigned& d, u32x4 srd, uint32_t voff, uint32_t soff) { asm volatile("buffer_load_dword %0, %1, %2, %3 offen" : "=v"(d) : "v"(voff), "s"(srd), "s"(soff) : "memory"); }
DI void dma(u32x4 srd, uint32_t voff, uint32_t soff, uint32_t lds) { asm volatile("s_mov_b32 m0, %3\n\ts_nop 0\n\tbuffer_load_dwordx4 %0, %1, %2 offen lds" :: "v"(voff), "s"(srd), "s"(soff), "s"(lds) : "memory"); }
DI void dmah(u32x4 srd, uint32_t voff, uint32_t soff, uint32_t lds, u64 mask) {
  asm volatile("s_mov_b32 m0, %3\n\ts_mov_b64 exec, %4\n\tbuffer_load_dwordx4 %0, %1, %2 offen lds\n\ts_mov_b64 exec, -1" :: "v"(voff), "s"(srd), "s"(soff), "s"(lds), "s"(mask) : "memory"); }
template <uint32_t LDS, uint32_t SO> DI void dmak(u32x4 srd, uint32_t voff, uint32_t base) { uint32_t t;
  asm volatile("s_add_u32 m0, %3, %4\n\ts_mov_b32 %0, %5\n\tbuffer_load_dwordx4 %1, %2, %0 offen lds" : "=&s"(t) : "v"(voff), "s"(srd), "s"(base), "i"(LDS), "i"(SO) : "memory", "scc"); }
template <uint32_t LDS, uint32_t SO> DI void dmakh(u32x4 srd, uint32_t voff, uint32_t base, u64 mask) { uint32_t t;
  asm volatile("s_add_u32 m0, %3, %4\n\ts_mov_b32 %0, %5\n\ts_mov_b64 exec, %6\n\tbuffer_load_dwordx4 %1, %2, %0 offen lds\n\ts_mov_b64 exec, -1"
               : "=&s"(t) : "v"(voff), "s"(srd), "s"(base), "i"(LDS), "i"(SO), "s"(mask) : "memory", "scc"); }
template <int L> DI uint32_t lane_of(uint32_t t) { uint32_t r; asm volatile("v_readlane_b32 %0, %1, %2" : "=s"(r) : "v"(t), "i"(L)); return r; }
template <uint32_t LDS> DI void setm0(uint32_t base) { asm volatile("s_add_u32 m0, %0, %1" :: "s"(base), "i"(LDS) : "scc"); }
DI void dmago(u32x4 srd, uint32_t voff, uint32_t soff) { asm volatile("buffer_load_dwordx4 %0, %1, %2 offen lds" :: "v"(voff), "s"(srd), "s"(soff) : "memory"); }
DI void dmagoh(u32x4 srd, uint32_t voff, uint32_t soff, u64 mask) {
  asm volatile("s_mov_b64 exec, %3\n\tbuffer_load_dwordx4 %0, %1, %2 offen lds\n\ts_mov_b64 exec, -1" :: "v"(voff), "s"(srd), "s"(soff), "s"(mask) : "memory"); }
DI void nop4() { asm volatile("s_nop 4"); }
DI void nop0() { asm volatile("s_nop 0"); }
DI void touch(u32x4 srd, uint32_t voff, uint32_t soff, u64 mask) {
  asm volatile("s_mov_b32 m0, %3\n\ts_mov_b64 exec, %4\n\tbuffer_load_dword %0, %1, %2 offen lds\n\ts_mov_b64 exec, -1" :: "v"(voff), "s"(srd), "s"(soff), "s"(L_JUNK), "s"(mask) : "memory"); }
DI void code_touch(int part, int l) {
  u64 pc; uint32_t n;
  asm volatile("s_getpc_b64 %0\n.Lfk_begin:\n\ts_mov_b32 %1, .Lfk_end-.Lfk_begin" : "=s"(pc), "=s"(n));
  touch(u32x4{(unsigned)pc, (unsigned)(pc >> 32) & 0xffffu, n, 0x00020000u}, l * 128, part * 2048, 0xffffull); }
template <int IMM> DI void st16c(u32x4 d, u32x4 srd, uint32_t voff, uint32_t soff) {
  asm volatile("buffer_store_dwordx4 %0, %1, %2, %3 offen offset:%4 sc1\n\ts_nop 1" :: "v"(d), "v"(voff), "s"(srd), "s"(soff), "i"(IMM) : "memory"); }
DI void st16m(u32x4 d, u32x4 srd, uint32_t voff, uint32_t soff, u64 mask) {
  asm volatile("s_mov_b64 exec, %4\n\tbuffer_store_dwordx4 %0, %1, %2, %3 offen sc1\n\ts_mov_b64 exec, -1\n\ts_nop 0" :: "v"(d), "v"(voff), "s"(srd), "s"(soff), "s"(mask) : "memory"); }
DI void st8c(u32x2 d, u32x4 srd, uint32_t voff, uint32_t soff) {
  asm volatile("buffer_store_dwordx2 %0, %1, %2, %3 offen sc1\n\ts_nop 1" :: "v"(d), "v"(voff), "s"(srd), "s"(soff) : "memory"); }
template <int N> DI void waitv() { asm volatile("s_waitcnt vmcnt(%0)" :: "i"(N) : "memory"); }
template <int N> DI void waitv_b() { asm volatile("s_waitcnt vmcnt(%0)\n\ts_barrier" :: "i"(N) : "memory"); }
template <int N> DI void waitv_lb() { asm volatile("s_waitcnt vmcnt(%0) lgkmcnt(0)\n\ts_barrier" :: "i"(N) : "memory"); }
DI void bar0() { asm volatile("s_barrier" ::: "memory"); }
DI void barl() { asm volatile("s_waitcnt lgkmcnt(0)\n\ts_barrier" ::: "memory"); }
DI void pin(u32x4& v) { asm volatile("" : "+v"(v)); }
DI void pin(u32x2& v) { asm volatile("" : "+v"(v)); }
DI void pin(unsigned& v) { asm volatile("" : "+v"(v)); }
DI void fix(float& v) { asm volatile("" : "+v"(v)); }
DI void kill(u32x4& v) { asm volatile("" : "=v"(v)); }
DI void kill(f32x4& v) { asm volatile("" : "=v"(v)); }
DI uint32_t oq_s(uint32_t v) { asm volatile("" : "+s"(v)); return v; }
DI uint32_t oq_v(uint32_t v) { asm volatile("" : "+v"(v)); return v; }
template <int P> DI void prio() { asm volatile("s_setprio %0" :: "i"(P)); }
#define SB() __builtin_amdgcn_sched_barrier(0)
template <int C> DI void tif() { asm volatile("; @IF %0" :: "i"(C)); }
template <int C> DI void tend() { asm volatile("; @END %0" :: "i"(C)); }

DI float asf(unsigned u) { return __builtin_bit_cast(float, u); }
DI unsigned asu(float f) { return __builtin_bit_cast(unsigned, f); }
DI unsigned pk_bf(float a, float b) { f32x2 p = {a, b}; bf16x2 r = __builtin_convertvector(p, bf16x2); return __builtin_bit_cast(unsigned, r); }
DI unsigned pk_h(float a, float b) { f32x2 p = {a, b}; f16x2 r = __builtin_convertvector(p, f16x2); return __builtin_bit_cast(unsigned, r); }
DI float bfr(float a) { return asf(pk_bf(a, a) << 16); }
DI float lo_f(unsigned u) { return asf(u << 16); }
DI float hi_f(unsigned u) { return asf(u & 0xffff0000u); }
DI float dot2(unsigned a, unsigned b, float c) { return __builtin_amdgcn_fdot2_f32_bf16(__builtin_bit_cast(bf16x2, a), __builtin_bit_cast(bf16x2, b), c, false); }
DI float vfma(float a, float b, float c) { float d; asm("v_fma_f32 %0, %1, %2, %3" : "=v"(d) : "v"(a), "v"(b), "v"(c)); return d; }
DI float vmul(float a, float b) { float d; asm("v_mul_f32 %0, %1, %2" : "=v"(d) : "v"(a), "v"(b)); return d; }
DI float vfms(float a, float b, float c) { float d; asm("v_fma_f32 %0, -%1, %2, %3" : "=v"(d) : "v"(a), "v"(b), "v"(c)); return d; }
template <int H, int N> DI float xmul(float a, unsigned t) { float d;
  if constexpr (N) asm("v_fma_mix_f32 %0, -%1, %2, 0 op_sel:[0,%3,0] op_sel_hi:[0,1,0]" : "=v"(d) : "v"(a), "v"(t), "i"(H));
  else asm("v_fma_mix_f32 %0, %1, %2, 0 op_sel:[0,%3,0] op_sel_hi:[0,1,0]" : "=v"(d) : "v"(a), "v"(t), "i"(H));
  return d; }
template <int H, int N> DI float xfma(float a, unsigned t, float c) { float d;
  if constexpr (N) asm("v_fma_mix_f32 %0, -%1, %2, %3 op_sel:[0,%4,0] op_sel_hi:[0,1,0]" : "=v"(d) : "v"(a), "v"(t), "v"(c), "i"(H));
  else asm("v_fma_mix_f32 %0, %1, %2, %3 op_sel:[0,%4,0] op_sel_hi:[0,1,0]" : "=v"(d) : "v"(a), "v"(t), "v"(c), "i"(H));
  return d; }
template <int H, int D> DI void xfmah(unsigned& d, float a, unsigned t, float c) {
  if constexpr (D == 0) asm("v_fma_mixlo_f16 %0, %1, %2, %3 op_sel:[0,%4,0] op_sel_hi:[0,1,0]" : "=v"(d) : "v"(a), "v"(t), "v"(c), "i"(H));
  else asm("v_fma_mixhi_f16 %0, %1, %2, %3 op_sel:[0,%4,0] op_sel_hi:[0,1,0]" : "+v"(d) : "v"(a), "v"(t), "v"(c), "i"(H)); }
template <int CTRL> DI float dpp_add(float v) { return v + asf(__builtin_amdgcn_update_dpp(0, asu(v), CTRL, 0xf, 0xf, true)); }
DI float sum8(float v) { return dpp_add<0x141>(dpp_add<0x4E>(dpp_add<0xB1>(v))); }
DI f32x4 mmb(u32x4 a, u32x4 b, f32x4 c) { return __builtin_amdgcn_mfma_f32_16x16x32_bf16(__builtin_bit_cast(bf16x8, a), __builtin_bit_cast(bf16x8, b), c, 0, 0, 0); }
DI f32x4 mmh(u32x4 a, u32x4 b, f32x4 c) { return __builtin_amdgcn_mfma_f32_16x16x32_f16(__builtin_bit_cast(f16x8, a), __builtin_bit_cast(f16x8, b), c, 0, 0, 0); }
DI u32x4 tr2(LAS char* p, int second) {
  s16x4 a = __builtin_amdgcn_ds_read_tr16_b64_v4i16((LAS s16x4*)p);
  s16x4 b = __builtin_amdgcn_ds_read_tr16_b64_v4i16((LAS s16x4*)(p + second));
  s16x8 c = __builtin_shufflevector(a, b, 0, 1, 2, 3, 4, 5, 6, 7);
  return __builtin_bit_cast(u32x4, c);
}
struct Gelu { float a0, a1;
  template <int Q> DI void step(float v0, float v1, unsigned& o) {
    if constexpr (Q == 0) { a0 = __builtin_fmaf(v0 * v0, -0.10294324f, -2.3022082f); a1 = __builtin_fmaf(v1 * v1, -0.10294324f, -2.3022082f); }
    if constexpr (Q == 1) { a0 = __builtin_amdgcn_exp2f(v0 * a0); a1 = __builtin_amdgcn_exp2f(v1 * a1); }
    if constexpr (Q == 2) { a0 = __builtin_amdgcn_rcpf(1.0f + a0); a1 = __builtin_amdgcn_rcpf(1.0f + a1); }
    if constexpr (Q == 3) { o = pk_bf(v0 * a0, __builtin_fmaf(v1, a1, 0.0f)); }
  }
  template <int K> DI void run(const f32x4 (&p)[2], u32x4& o) {
    constexpr int P = K >> 2; const float v0 = p[P >> 1][2 * (P & 1)], v1 = p[P >> 1][2 * (P & 1) + 1];
    unsigned t = 0; step<K & 3>(v0, v1, t); if constexpr ((K & 3) == 3) o[P] = t; }
};
#define ST_(i) do {} while (0)
#define RLX_AGENT __ATOMIC_RELAXED, __HIP_MEMORY_SCOPE_AGENT
DI bool all_of(bool c) { return __builtin_amdgcn_ballot_w64(c) == ~0ull; }
DI bool lvl_ok(unsigned lo, unsigned hi, u64 tag, unsigned lvl) { return (hi == (unsigned)(tag >> 32)) & (lo - (unsigned)tag - lvl <= 7u - lvl); }
template <int N> DI void spin(const char* base, uint32_t off, u64 tag, unsigned lvl) {
#pragma nounroll
  for (unsigned spins = 0; spins < (1u << 17); ++spins) {
    bool ok = true;
#pragma unroll
    for (int k = 0; k < N; ++k) { u64 v = __hip_atomic_load((GAS u64*)(base + off + (k & 1) * 8 + (k >> 1) * 1024), RLX_AGENT); ok &= lvl_ok((unsigned)v, (unsigned)(v >> 32), tag, lvl); }
    if (__all(ok)) break;
    __builtin_amdgcn_s_sleep(1);
  }
}
DI void setflag(char* ws, uint32_t off, u64 v) { __hip_atomic_store((GAS u64*)(ws + off), v, RLX_AGENT); }

__global__ __launch_bounds__(512, 2) void fused_kernel(const char* __restrict__ x, const char* __restrict__ wr, const char* __restrict__ wi,
    const char* __restrict__ w1, const char* __restrict__ b1, const char* __restrict__ w2, const char* __restrict__ b2, char* out, char* ws) {
  extern __shared__ __attribute__((aligned(16))) char smem_[];
  LAS char* smem = (LAS char*)smem_;
  const int tid = threadIdx.x, l = tid & 63;
  const int w = __builtin_amdgcn_readfirstlane(tid >> 6), g = w & 3, hf = w >> 2;
  const int b = blockIdx.x, xcd = b & 7, jj = b >> 3;
  const int ks = xcd >> 1, mt = (xcd & 1) * 8 + (jj >> 2), nt = jj & 3, fj = ks * 4 + nt;
  const u32x4 rs = mksrd(ws, WS_END), rx = mksrd(x, 2048 * 1152), r1 = mksrd(w1, 576 * 9216), r2 = mksrd(w2, 4608 * 1152), rt = mksrd(&g_tab, TAB_END);
  const int i16 = l & 15, q4 = i16 >> 2, p4 = i16 & 3, hh = l >> 5, gq = l >> 4;
  const u64 hmask = (w & 1) ? 0xffffffff00000000ull : 0xffffffffull;
  const uint32_t v16 = l * 16;
  asm volatile("" :: "s"(ws), "s"(out), "s"(b1), "s"(b2), "s"(w2), "s"(wr), "s"(wi));
  ST_(0);
  u32x4 x0[18], x1[18];
  const uint32_t vx = (l >> 3) * 36864 + (l & 7) * 16;
  const uint32_t sx = (uint32_t)((32 * (g & 1) + 16 * hf) * 32 + 2 * mt + (g >> 1)) * 1152;
  const int odd = fj & 1, lone = odd ? 0 : 8;
  const int tileA = 18 * odd + 2 * (fj >> 1), tileC = 18 * odd + 16 + (nt >> 1);
  u32x2 ep; unsigned b1v;
  const uint32_t vo1 = (8 * hh + ((l >> 3) & 3)) * 9216 + (((l & 7) ^ (((l >> 4) & 3) << 1)) * 16);
  uint32_t v1a = 0, v1b = 0, sown = 0, sbox = 0;
  const uint32_t sw4 = oq_s(w * 4096), sw1 = oq_s(w * 1024), sh1 = oq_s((w >> 1) * 1024), sh9 = oq_s((w >> 1) * 9216);
  auto dma1 = [&](auto ST, auto I) {
    constexpr uint32_t so = 288 * (ST & 1) * 9216 + 128 * ((ST >> 1) - 1), lb = (ST & 3) * BUF;
    if constexpr (I < 4) dmak<lb + I * 1024, so + (16 * (I >> 1) + 4 * (I & 1)) * 9216>(r1, v1a, sw4);
    else dmakh<lb + 32768, so>(r1, v1b, sh1, hmask);
  };
  const uint32_t axb = 2 * BUF + g * 18432;
  const uint32_t axw = axb + ((l >> 2) & 1) * 1024 + (l & 3) * 256 + (((l >> 3) ^ (l & 7)) * 16);
  const uint32_t axr0 = axb + gq * 256 + ((i16 ^ gq) * 16), axr1 = axr0 ^ 64;
  auto rdx = [&](u32x4 (&d)[18], auto S) { d[S] = *(LAS u32x4*)(smem + ((S & 1) ? axr1 : axr0) + S * 1024); };
  u32x4 xr[2][9];
  f32x2 rn2[2], mb2[2];
  auto ldx = [&]() { sfor<0, 18>([&](auto C) { ld16<(C % 9) * 128>(xr[C / 9][C % 9], rx, vx, sx + (C / 9) * 294912); }); ld8c(ep, rs, 0, WS_EPOCH); };
  auto stats = [&](auto RB, auto NV, auto&& fill) {
    float sa[4] = {}, qa[4] = {};
    sfor<0, 9>([&](auto C) { fill(C);
      waitv<NV + 17 - 9 * RB - C>(); pin(xr[RB][C]);
      sfor<0, 4>([&](auto D) { const unsigned u = xr[RB][C][(int)D]; sa[D] = dot2(u, 0x3f803f80u, sa[D]); qa[D] = dot2(u, u, qa[D]); });
      SB(); });
    rn2[RB] = f32x2{(sa[0] + sa[1]) + (sa[2] + sa[3]), (qa[0] + qa[1]) + (qa[2] + qa[3])}; };
  auto chain = [&](auto RB) {
    const float mean = sum8(rn2[RB].x) * (1.0f / 576.0f);
    const float var = fmaxf(sum8(rn2[RB].y) * (1.0f / 576.0f) - mean * mean, 0.0f);
    const float sd = bfr(__builtin_amdgcn_sqrtf(bfr(bfr(var) + 1e-5f)));
    const float rn = __builtin_amdgcn_rcpf(sd), mb = -bfr(mean) * rn;
    rn2[RB] = f32x2{rn, rn}; mb2[RB] = f32x2{mb, mb}; };
  auto put = [&](auto RB, auto C) { *(LAS u32x4*)(smem + axw + RB * 128 + C * 2048) = xr[RB][C]; };
  auto norm = [&](auto RB, auto&& fill) {
    sfor<0, 9>([&](auto C) { f32x2 f[4];
      sfor<0, 4>([&](auto D) { const unsigned u = xr[RB][C][(int)D]; f[D] = f32x2{lo_f(u), hi_f(u)}; });
      SB();
      sfor<0, 4>([&](auto D) { f[D] = __builtin_elementwise_fma(f[D], rn2[RB], mb2[RB]); });
      SB();
      xr[RB][C] = u32x4{pk_bf(f[0].x, f[0].y), pk_bf(f[1].x, f[1].y), pk_bf(f[2].x, f[2].y), pk_bf(f[3].x, f[3].y)};
      fill(C);
      SB(); }); };
  using T1 = IC<1>; using F0 = IC<0>;
  u64 tag;
  auto mktag = [&]() { pin(ep);
    tag = ((u64)(unsigned)__builtin_amdgcn_readfirstlane(ep.y) << 32) | (unsigned)__builtin_amdgcn_readfirstlane(ep.x);
    tag += 0x9E3779B97F4A7C15ull; tag = (tag ^ (tag >> 30)) * 0xBF58476D1CE4E5B9ull; tag = (tag ^ (tag >> 27)) * 0x94D049BB133111EBull; tag = (tag ^ (tag >> 31)) & ~7ull; };
  f32x4 c0[2], c1[2], p0[2], p1[2];
  auto bias = [&](int unit, auto FT) { return *(LAS f32x4*)(smem + L_B1 + (32 * unit + 16 * FT + 4 * gq) * 4); };
  const int rho = 16 * (gq >> 1) + 4 * (gq & 1) + q4;
  uint32_t vo2a, vo2b;
  const uint32_t sh = WS_H + (mt * 144 * 4 + g) * 2048;
  uint32_t tabw, tabh;
  uint32_t sq = 0;
  auto dma2p = [&](auto J, auto I) { constexpr uint32_t lb = ((J + 2) & 3) * BUF;
    sq = lane_of<5 * J + I>(tabw); if constexpr (I < 4) setm0<lb + I * 9216>(sw1); else setm0<lb + 8192>(sh9); };
  auto dma2g = [&](auto I) { if constexpr (I < 4) dmago(r2, vo2a, sq); else dmagoh(r2, vo2b, sq, hmask); };
  const uint32_t vho = v16 + hf * 1024, vhs = v16 + (hf ^ 1) * 1024;
  const uint32_t vfh = WS_FH + ((b & ~24) + gq * 8) * 128;
  auto check1 = [&](u32x2& p, unsigned lvl) { pin(p); if (!all_of(lvl_ok(p.x, p.y, tag, lvl))) spin<1>(ws, vfh, tag, lvl); };
  auto check4 = [&](u32x4 (&p)[2], uint32_t base) { pin(p[0]); pin(p[1]);
    if (!all_of(lvl_ok(p[0].x, p[0].y, tag, 0) & lvl_ok(p[0].z, p[0].w, tag, 0) & lvl_ok(p[1].x, p[1].y, tag, 0) & lvl_ok(p[1].z, p[1].w, tag, 0))) spin<4>(ws, base + v16, tag, 0); };
  const int zb0 = ks < 2 ? 5 * ks : 4 * ks + 2;
  const int T2 = 256 + (xcd & 1) * 32 + jj;
  const bool dbl = ks == 3 && T2 <= 288;
  u32x4 tF64[2][2], tF32[2][2], tG32[2][2], tG64[2][2], tw[2][2], ur[2], wt[2], pu[2], hc[4][2], hown[4][2];
  u32x2 pl;

  const uint32_t a1 = rho * 128 + (((2 * hf) ^ ((rho >> 1) & 3)) * 32) + p4 * 8;
  uint32_t pa0 = 0, pa1 = 0;
  u32x4 fr[4];
  auto rd1 = [&](auto ST, auto S) {
    if constexpr (S == 0) { pa0 = oq_v(a1 + (ST & 3) * BUF); pa1 = oq_v(pa0 ^ 32); }
    return tr2(smem + ((S & 1) ? pa1 : pa0) + (S >> 1) * 4096, 1024); };
  auto ldu = [&](int T) { sfor<0, 2>([&](auto Q) { ld16c<Q * 1024>(ur[Q], rs, v16, WS_UT + (T == 288 ? 0 : T * 8192) + g * 2048); }); };
  auto putu = [&]() { sfor<0, 2>([&](auto Q) { pin(ur[Q]);
    *(LAS u32x4*)(smem + L_SP + (g >> 1) * 4096 + ((l >> 2) & 1) * 2048 + ((l >> 3) & 1) * 1024 + (l & 3) * 256 + (((8 * (g & 1) + 4 * Q + gq) ^ (l & 7)) * 16)) = ur[Q]; }); };
  auto ldt = [&](u32x4 (&t)[2][2], uint32_t so) { sfor<0, 2>([&](auto C) { sfor<0, 2>([&](auto A) { ld16<C * 2048 + A * 1024>(t[C][A], rt, v16, so); }); }); };
  auto ld2h = [&](u32x4 (&h)[2], uint32_t so) { ld16c<0>(h[0], rs, v16, so); ld16c<1024>(h[1], rs, v16, so); };
  uint32_t s4[4] = {};
  f32x4 bi[2];
  auto wtask = [&](int cl) {
    LAS char* p = smem + L_W + (l & 7) * 656 + (l & 1) * 2 + cl * 2 + (b >= 192 ? 12 : 0); unsigned v[8];
    sfor<0, 8>([&](auto J) { v[J] = *(LAS u16*)(p + (J >> 2) * 5248 + (J & 3) * 160); });
    const int kc = 72 * (b >> 6) + cl;
    st16c<0>(u32x4{v[0] | (v[1] << 16), v[2] | (v[3] << 16), v[4] | (v[5] << 16), v[6] | (v[7] << 16)}, rs,
             (uint32_t)(kc == 288 ? 288 : (kc & 1) ? 144 + (kc >> 1) : (kc >> 1)) * 8192 + (l & 7) * 16,
             WS_WT + ((b >> 1) & 3) * 2048 + ((b >> 5) & 1) * 1024 + ((b >> 3) & 3) * 256 + (b & 1) * 128); };
  auto stage = [&](auto ST, auto HF) {
    constexpr int R = ST >> 1, KB = 9 * (ST & 1);
    if constexpr (ST == 9) bar0(); else waitv_b<(ST == 2 ? 13 + 2 * HF : ST == 3 ? 13 + HF : ST == 5 ? 6 : ST == 8 ? 9 : 7)>();
    ST_(3 + ST);
    if constexpr (ST == 3) { pin(tabw); pin(tabh); pin(vo2a); pin(vo2b); tabh += sh + 36 * 8192 * ks; }
    if constexpr (ST == 5) { if (tid == 0) setflag(ws, WS_FU + b * 8, tag); }
    if constexpr (ST == 7 || ST == 9) { if (tid == 0) setflag(ws, WS_FH + b * 128, tag + (ST - 5) / 2); }
    if constexpr (ST <= 4) sfor<0, 3>([&](auto S) { fr[(18 * ST + S) & 3] = rd1(ST, S); });
    if constexpr (ST == 2) { bi[0] = bias(odd + hf, IC<0>{}); bi[1] = bias(odd + hf, IC<1>{}); }
    if constexpr (ST == 8) ld8c(pl, rs, vfh, 0);
    if constexpr (ST == 9) {
      if (hf) { tif<1>(); ldt(tF64, TAB_F64 + g * 4096); ld16<0>(tw[0][0], rt, v16, TAB_TW + g * 2048); ld16<1024>(tw[0][1], rt, v16, TAB_TW + g * 2048); }
      else { tif<0>(); sfor<0, 4>([&](auto I) { ld2h(hc[I], s4[I]); }); }
      tend<0>(); tend<1>();
    }
    Gelu ge; u32x4 o;
    SB();
    sfor<0, 18>([&](auto S) {
      constexpr int G = 18 * ST + S, FT = S & 1;
      if constexpr (LAD & 1) { if constexpr (S == 0) prio<3>(); if constexpr (S == 5) prio<2>(); if constexpr (S == 10) prio<1>(); if constexpr (S == 14) prio<0>(); }
      if constexpr (S >= 2 && !(S & 1)) { __builtin_amdgcn_s_waitcnt(0xC27F); SB(); }
      const u32x4 a = fr[G & 3];
      if constexpr ((ST & 1) == 0 && S < 2) c0[FT] = mmb(a, x0[KB + (S >> 1)], bi[FT]); else c0[FT] = mmb(a, x0[KB + (S >> 1)], c0[FT]);
      SB();
      if constexpr (S + 3 < 18) fr[(G + 3) & 3] = rd1(ST, IC<S + 3>{});
      else if constexpr (ST > 3 && ST != 9) fr[(G + 3) & 3] = rd1(IC<ST + 1>{}, IC<S + 3 - 18>{});
      SB();
      if constexpr ((ST & 1) == 0 && S < 2) c1[FT] = mmb(a, x1[KB + (S >> 1)], bi[FT]); else c1[FT] = mmb(a, x1[KB + (S >> 1)], c1[FT]);
      SB();
      if constexpr (HF != 1 && S >= 1 && S < 17) { if constexpr (ST & 1) ge.run<S - 1>(p1, o); else ge.run<S - 1>(p0, o); }
      if constexpr (ST == 2 && S % 3 == 2 && S / 3 < 5) dma1(IC<5>{}, IC<S / 3>{});
      if constexpr (S % 3 == 1 && S / 3 < 5) { if constexpr (ST < 7) dma1(IC<(ST == 2 ? 4 : ST + 3)>{}, IC<S / 3>{}); else dma2g(IC<S / 3>{}); }
      if constexpr (ST >= 7 && S % 3 == 0 && S / 3 < 5) dma2p(IC<ST - 7>{}, IC<S / 3>{});
      if constexpr (HF == 1 && ST == 3 && (S == 3 || S == 9)) wtask(8 * g + (S == 3 ? 0 : 32) + (l >> 3));
      if constexpr (ST >= 5 && ST < 8 && S == 15) { st16c<0>(u32x4{~0u, ~0u, ~0u, ~0u}, rs, v16, sbox + (ST - 5) * 8192);
        if constexpr (ST == 5) { if (w < 3) st16c<0>(u32x4{~0u, ~0u, ~0u, ~0u}, rs, v16, sbox + 24576); } }
      if constexpr (HF == 1 && ST == 3 && S == 15) { if (g == 0 || (g == 1 && b >= 192)) wtask(g == 0 ? 64 + (l >> 3) : 72); }
      if constexpr (ST == 7 && S == 14) { ld16c<0>(pu[0], rs, v16, WS_FU); ld16c<1024>(pu[1], rs, v16, WS_FU); }
      if constexpr (ST == 8 && S == 8) { waitv<6>(); if (hf) { tif<1>(); check4(pu, WS_FU); ldu(b); } tend<1>(); }
      if constexpr (ST == 8 && S == 17) { waitv<5>(); check1(pl, 1); }
      if constexpr (ST == 9 && S == 15) { if (hf) { tif<1>(); waitv<12>(); putu(); } tend<1>(); }
      if constexpr (HF != 1 && S == 17) { st16c<(ST & 1) * 1024>(o, rs, v16, R == 1 ? sh + (9 * fj + lone) * 8192 : sown + 2 * (R - 2) * 8192); if constexpr (R > 1) hown[R - 2][ST & 1] = o; }
      if constexpr ((ST & 1) && ST < 9 && S == 17) { bi[0] = bias(odd + 2 * R + hf, IC<0>{}); bi[1] = bias(odd + 2 * R + hf, IC<1>{}); }
      if constexpr (ST == 8 && S == 17) sfor<0, 4>([&](auto I) { s4[I] = lane_of<4 + I>(tabh); });
      SB();
    });
    if constexpr (ST & 1) { p0[0] = c0[0]; p0[1] = c0[1]; p1[0] = c1[0]; p1[1] = c1[1]; }
  };
  auto fill0 = [&](auto S) {
    if constexpr (S == 0) { v1a = oq_v(vo1 + 32 * w * 9216 + (288 * fj + 32 * odd) * 2); v1b = oq_v(vo1 + (256 + 16 * hf + 4 * ((w >> 1) & 1)) * 9216 + (288 * fj + 32 * odd) * 2);
      ST_(30); barl(); ST_(31); }
    else if constexpr (S < 6) dma1(IC<2>{}, IC<S - 1>{});
    else if constexpr (S < 11) dma1(IC<3>{}, IC<S - 6>{});
    else if constexpr (S < 13) {
      const int j = (l * 13) >> 7;
      dmah(mksrd(S == 12 ? wi : wr, 2048 * 578), j * 36992 + (l - 10 * j) * 16, ((8 * (b & 7) + w + 256 * ((b >> 3) & 7)) * 578 + (b >= 192 ? 420 : 144 * (b >> 6))) & ~3u,
           L_W + (S - 11) * 5248 + w * 656, (1ull << 40) - 1); }
    else if constexpr (S == 13) { ld4(tabw, rt, l * 4, TAB_AW + nt * 1024 + (w >> 1) * 256); ld4(tabh, rt, l * 4, TAB_AH + nt * 512 + hf * 256); }
    else if constexpr (S == 14) { ld4(vo2a, rt, l * 4, TAB_AV + ks * 2304 + w * 256); ld4(vo2b, rt, l * 4, TAB_AV + ks * 2304 + 2048); }
    else { constexpr int N = (324 - 4 * NW + 7) / 8; touch(r2, (jj * 324 + 4 * NW + w * N + l) * 128, ks * 1152 * 1152, (1ull << N) - 1);
      touch(rt, l < 6 ? TAB_TI + zb0 * 36864 + ((jj * 8 + w) * 6 + l) * 128 : TAB_F64 + ((jj * 8 + w) * 2 + l - 6) * 128, 0, 0xffull); } };
  const f32x4 z4 = {0, 0, 0, 0};
  const uint32_t su = WS_UT + (2 * mt + (g >> 1)) * 256 + (g & 1) * 128;
  auto ust = [&](const f32x4 (&u)[4], uint32_t so) { f32x4 a, c; const float sg = odd ? -1.0f : 1.0f;
    sfor<0, 4>([&](auto J) { a[(int)J] = __builtin_fmaf(u[2][(int)J], sg, u[0][(int)J]); c[(int)J] = __builtin_fmaf(u[3][(int)J], sg, u[1][(int)J]); });
    st16c<0>(u32x4{pk_h(a.x, a.y), pk_h(a.z, a.w), pk_h(c.x, c.y), pk_h(c.z, c.w)}, rs, (i16 >> 1) * 8192 + (i16 & 1) * 64 + gq * 16,
             so); };
  if (hf == 0) { tif<0>();
    ldx(); ld4(b1v, mksrd(b1, 9216), tid * 4, 576 * fj);
    bar0();
    const int i1 = jj * 144 + g * 36 + l;
    const uint32_t vt1 = oq_v((i1 >> 3) * 9216 + ((i1 >> 2) & 1) * 1152 + ((i1 >> 1) & 1) * 640 + (i1 & 1) * 128);
    sfor<0, 2>([&](auto RB) { stats(RB, IC<2>{}, [&](auto) {}); ST_(1 + RB);
      if constexpr (RB == 1 && PP == 0) touch(r1, vt1, ks * 2304, (1ull << 36) - 1);
      chain(RB); norm(RB, [&](auto C) { put(RB, C); }); });
    waitv<PP == 0>(); pin(b1v);
    if (tid < 144) *(LAS f32x2*)(smem + L_B1 + tid * 8) = f32x2{lo_f(b1v), hi_f(b1v)};
    ST_(28);
    barl();
    sfor<0, 18>([&](auto S) { rdx(x0, S); });
    if constexpr (PP == 1) touch(r1, vt1, ks * 2304, (1ull << 36) - 1);
    const uint32_t vo = (8 * ((l >> 4) & 1) + 4 * hh + ((l >> 2) & 3)) * 9216 + (((l & 3) ^ (((l >> 4) & 1) << 1)) * 16), so = (288 * fj + 32 * lone) * 2 + 16 * g * 9216;
    sfor<0, 9>([&](auto Q) { if constexpr (Q == 2) { barl(); ST_(3); }
      dma(r1, vo, so + 64 * Q * 9216, (g + 4 * Q) * 1024);
      if constexpr (Q < 4) dma(rt, v16, TAB_TF + tileA * 9216 + (g + 4 * Q) * 1024, BUF + (g + 4 * Q) * 1024); });
    if (g < 2) dma(rt, v16, TAB_TF + tileA * 9216 + (g + 16) * 1024, BUF + (g + 16) * 1024);
    if (ks == 2) { sfor<0, 2>([&](auto Q) { dma(rt, v16, TAB_TF + tileC * 9216 + (g + 4 * Q) * 1024, BUF + 18432 + (g + 4 * Q) * 1024); });
      if (g == 3) dma(rt, v16, TAB_TF + tileC * 9216 + 8192, BUF + 18432 + 8192); }
    if constexpr (PP == 2) touch(r1, vt1, ks * 2304, (1ull << 36) - 1);
    touch(r1, vt1, ks * 2304 + 256, (1ull << 36) - 1);
    touch(r2, (jj * 324 + g * NW + l) * 128, ks * 1152 * 1152, (1ull << NW) - 1);
    mktag();
    const f32x4 bl[2] = {bias(lone, F0{}), bias(lone, T1{})};
    const uint32_t al0 = oq_v(rho * 64 + (gq & 1) * 32 + p4 * 8), al1 = oq_v(al0 ^ 32);
    auto rd = [&](auto S) { return tr2(smem + ((S & 1) ? al1 : al0) + (S >> 1) * 2048, 512); };
    waitv<2 + (PP == 2)>(); ST_(29); bar0(); ST_(4);
    sfor<0, 3>([&](auto S) { fr[S] = rd(S); });
    sfor<0, 18>([&](auto S) { rdx(x1, S); }); SB();
    sfor<0, 36>([&](auto S) { const u32x4 a = fr[S & 3];
      p0[S & 1] = mmb(a, x0[S >> 1], S < 2 ? bl[S & 1] : p0[S & 1]); SB();
      if constexpr (S + 3 < 36) fr[(S + 3) & 3] = rd(IC<S + 3>{});
      SB();
      p1[S & 1] = mmb(a, x1[S >> 1], S < 2 ? bl[S & 1] : p1[S & 1]); SB();
      if constexpr (S >= 4 && !(S & 1)) fill0(IC<(S - 4) / 2>{});
      SB(); });
    ST_(32);
    if (ks == 2) { f32x4 uc[4]; const uint32_t at = oq_v(BUF + 18432 + v16), suC = oq_s(su + tileC * 65536);
      u32x4 tc[9];
      sfor<0, 9>([&](auto I) { tc[I] = *(LAS u32x4*)(smem + at + I * 1024); });
      SB();
      sfor<0, 9>([&](auto I) { const u32x4 t = tc[I];
        uc[0] = mmb(x0[I], t, I ? uc[0] : z4); uc[2] = mmb(x0[I + 9], t, I ? uc[2] : z4); uc[1] = mmb(x1[I], t, I ? uc[1] : z4); uc[3] = mmb(x1[I + 9], t, I ? uc[3] : z4); });
      ust(uc, suC); }
    ST_(33);
    stage(IC<2>{}, IC<0>{}); stage(IC<3>{}, IC<0>{});
  } else { tif<1>();
    if (w == 7) code_touch(jj, l);
    if (w == 6) { const int i = jj * 9 + l - 18;
      touch(rt, l < 18 ? (18 * (l & 1) + 4 * ks) * 9216 + (jj * 9 + (l >> 1)) * 128 : 16 * 9216 + (i >= 144 ? 147456 : 0) + i * 128, TAB_TF, ks == 2 ? (1ull << 27) - 1 : (1ull << 18) - 1); }
    if (w == 5) touch(r1, (jj * 18 + (l >> 1)) * 9216 + 512 + (l & 1) * 1152, ks * 2304, (1ull << 36) - 1);
    bar0();
    ldx();
    stats(F0{}, T1{}, [&](auto) {}); chain(F0{}); SB();
    ST_(1);
    bar0();
    ST_(2);
    norm(F0{}, [&](auto C) { rdx(x0, IC<2 * C>{}); rdx(x0, IC<2 * C + 1>{}); });
    ST_(28);
    barl();
    ST_(3);
    stats(T1{}, T1{}, [&](auto C) { put(F0{}, C); }); chain(T1{}); norm(T1{}, [&](auto C) { put(T1{}, C); });
    waitv<0>(); mktag();
    ST_(29); barl(); ST_(4);
    const uint32_t at = oq_v(BUF + v16), suA = oq_s(su + tileA * 65536);
    f32x4 ua[2][4];
    auto rd = [&](auto S) { return *(LAS u32x4*)(smem + at + (S & 1) * 9216 + (S >> 1) * 1024); };
    sfor<0, 2>([&](auto S) { fr[S] = rd(S); });
    sfor<0, 9>([&](auto S) { rdx(x1, S); rdx(x1, IC<S + 9>{}); }); SB();
    sfor<0, 18>([&](auto S) { constexpr int I = S >> 1, TT = S & 1; const u32x4 t = fr[S & 3];
      ua[TT][0] = mmb(x0[I], t, I ? ua[TT][0] : z4); SB();
      if constexpr (S + 2 < 18) fr[(S + 2) & 3] = rd(IC<S + 2>{});
      SB();
      ua[TT][2] = mmb(x0[I + 9], t, I ? ua[TT][2] : z4); SB();
      if constexpr (S >= 2) fill0(IC<S - 2>{});
      SB();
      ua[TT][1] = mmb(x1[I], t, I ? ua[TT][1] : z4); ua[TT][3] = mmb(x1[I + 9], t, I ? ua[TT][3] : z4); SB(); });
    ST_(32);
    ust(ua[0], suA); ust(ua[1], suA + 65536);
    ST_(33);
    if (PRIO & 1) __builtin_amdgcn_s_setprio(1);
    stage(IC<2>{}, IC<1>{}); stage(IC<3>{}, IC<1>{});
  }
  tend<0>(); tend<1>();
  sown = oq_s(sh + (9 * fj + odd + hf) * 8192); sbox = oq_s(WS_PART + b * 32768 + w * 1024);
  sfor<4, 10>([&](auto ST) { stage(ST, IC<2>{}); });

  f32x4 oo[9], os[9];
  u32x4 f6[6];
  const uint32_t a2 = (4 * gq + q4) * 288 + p4 * 8;
  uint32_t pb = 0;
  auto rd2 = [&](auto S) { f6[S % 6] = tr2(smem + pb + (S / 9) * 9216 + (S % 9) * 32, 4608); };
  auto pre = [&](uint32_t cur) { pb = oq_v(a2 + cur); sfor<0, 4>(rd2); SB(); };
  auto blocks = [&](auto NB, auto NEXT, auto ZERO, auto BOTH, uint32_t nxt, auto&& hfrag, auto&& fill) {
    sfor<0, 9 * NB>([&](auto S) {
      constexpr int B = S / 9, DT = S % 9;
      if constexpr (!(S & 1) && (NEXT || S + 4 < 9 * NB)) { __builtin_amdgcn_s_waitcnt(0xC47F); SB(); }
      const u32x4 a = f6[S % 6];
      oo[DT] = mmb(a, hfrag(IC<B>{}, F0{}), (ZERO && B == 0) ? z4 : oo[DT]);
      SB();
      if constexpr (S + 4 < 9 * NB) rd2(IC<S + 4>{});
      else if constexpr (NEXT) { if constexpr (S + 4 == 9 * NB) pb = oq_v(a2 + nxt); rd2(IC<S + 4 - 9 * NB>{}); }
      SB();
      if constexpr (BOTH) os[DT] = mmb(a, hfrag(IC<B>{}, T1{}), (ZERO && B == 0) ? z4 : os[DT]);
      SB();
      fill(S);
      SB();
    }); };
  auto lbuf = [&](auto J) { return (uint32_t)(((J + 2) & 3) * BUF); };
  const uint32_t sow4 = sown + 6 * 8192;
  u32x4 hr[3][2][2], hx[2][2][2];
  uint32_t sa = 0, sb = 0;
  auto ldh = [&](auto J, auto K, auto TT, uint32_t so) { ld16c<0>(hr[J % 3][K][TT], rs, TT ? vhs : vho, so); };
  auto ldh4 = [&](auto J) { sa = lane_of<4 * J>(tabh); sb = lane_of<4 * J + 1>(tabh); nop4(); sfor<0, 4>([&](auto I) { ldh(J, IC<I / 2>{}, IC<I & 1>{}, I < 2 ? sa : sb); }); };
  auto solo = [&](auto J, auto S) {
    if constexpr (S % 4 == 0 && S < 20) dma2p(IC<J + 3>{}, IC<S / 4>{});
    if constexpr (S % 4 == 1 && S < 20) dma2g(IC<S / 4>{});
    if constexpr (S == 19 || S == 28) { sa = lane_of<4 * (J + 2) + (S - 19) / 4>(tabh); sb = lane_of<4 * (J + 2) + (S - 19) / 4 + 1>(tabh); }
    if constexpr (S >= 21 && S < 29 && (S & 1)) ldh(IC<J + 2>{}, IC<(S - 21) / 4>{}, IC<((S - 21) / 2) & 1>{}, S < 25 ? sa : sb);
    if constexpr (S == 30) { if (dbl) { tif<2>(); ld2h(hx[J & 1][0], sa); ld2h(hx[J & 1][1], sb); } tend<2>(); } };
  auto pin4 = [&](u32x4 (&t)[2][2]) { pin(t[0][0]); pin(t[0][1]); pin(t[1][0]); pin(t[1][1]); };
  auto kill4 = [&](u32x4 (&t)[2][2]) { kill(t[0][0]); kill(t[0][1]); kill(t[1][0]); kill(t[1][1]); };
  auto hflag = [&](int lvl) { if (tid == 0) setflag(ws, WS_FH + b * 128, tag + lvl); };
  u32x4 uu[2][2][2], Br, Bi;
  auto rdu = [&](int T) { LAS char* pi = smem + L_SP + 2048 + gq * 256 + ((i16 ^ (4 + gq)) * 16); LAS char* pr = T == 288 ? pi : smem + L_SP + gq * 256 + ((i16 ^ gq) * 16);
    sfor<0, 2>([&](auto TT) { sfor<0, 2>([&](auto A) { uu[0][TT][A] = *(LAS u32x4*)(pr + TT * 4096 + A * 1024); uu[1][TT][A] = *(LAS u32x4*)(pi + TT * 4096 + A * 1024); }); }); };
  auto ldw = [&](int T) { sfor<0, 2>([&](auto P) { ld16c<P * 1024>(wt[P], rs, v16, WS_WT + T * 8192 + g * 2048); }); };
  auto real = [&](f32x4 (&P)[2], const f32x4 (&Q)[2]) { SB(); sfor<0, 8>([&](auto E) { float r = P[E >> 2][E & 3] - Q[E >> 2][E & 3]; fix(r); P[E >> 2][E & 3] = r; }); SB(); };
  auto segA = [&](int T) {
    if (T == 0 || T == 288) sfor<0, 2>([&](auto TT) { sfor<0, 2>([&](auto A) { uu[1][TT][A] = u32x4{0, 0, 0, 0}; }); });
    unsigned br[4], bq[4]; f32x4 P[2], Q[2], I[2]; float ta[8], tb[8];
    sfor<0, 2>([&](auto TT) { I[TT] = mmh(uu[0][TT][0], tF64[1][0], z4); I[TT] = mmh(uu[0][TT][1], tF64[1][1], I[TT]);
      I[TT] = mmh(uu[1][TT][0], tF64[0][0], I[TT]); I[TT] = mmh(uu[1][TT][1], tF64[0][1], I[TT]); });
    SB();
    sfor<0, 2>([&](auto TT) { P[TT] = mmh(uu[0][TT][0], tF64[0][0], z4); P[TT] = mmh(uu[0][TT][1], tF64[0][1], P[TT]);
      Q[TT] = mmh(uu[1][TT][0], tF64[1][0], z4); Q[TT] = mmh(uu[1][TT][1], tF64[1][1], Q[TT]); });
    real(P, Q);
    sfor<0, 8>([&](auto E) { const unsigned t = tw[0][E >> 2][E & 3]; ta[E] = xmul<0, 0>(P[E >> 2][E & 3], t); tb[E] = xmul<1, 1>(P[E >> 2][E & 3], t); });
    SB();
    sfor<0, 8>([&](auto X) { constexpr int E = (X & 3) * 2 + (X >> 2); const unsigned t = tw[0][E >> 2][E & 3];
      xfmah<1, E & 1>(br[E / 2], I[E >> 2][E & 3], t, ta[E]); xfmah<0, E & 1>(bq[E / 2], I[E >> 2][E & 3], t, tb[E]); if constexpr (X == 3) SB(); });
    Br = u32x4{br[0], br[1], br[2], br[3]}; Bi = u32x4{bq[0], bq[1], bq[2], bq[3]}; pin(Br); pin(Bi); };
  auto segB = [&](auto&& mid) {
    float ta[8], tb[8], wp[8], wq[8]; f32x4 P[2], Q[2], I[2];
    pin(wt[0]); pin(wt[1]);
    sfor<0, 2>([&](auto TT) { I[TT] = mmh(tF32[1][TT], Br, z4); I[TT] = mmh(tF32[0][TT], Bi, I[TT]); });
    SB();
    sfor<0, 2>([&](auto TT) { P[TT] = mmh(tF32[0][TT], Br, z4); Q[TT] = mmh(tF32[1][TT], Bi, z4); });
    SB();
    sfor<0, 8>([&](auto E) { const unsigned a = wt[E >> 2][(E >> 1) & 1], c = wt[E >> 2][2 + ((E >> 1) & 1)]; wp[E] = (E & 1) ? hi_f(a) : lo_f(a); wq[E] = (E & 1) ? hi_f(c) : lo_f(c); });
    real(P, Q);
    sfor<0, 8>([&](auto E) { ta[E] = vmul(P[E >> 2][E & 3], wp[E]); tb[E] = vmul(P[E >> 2][E & 3], wq[E]); });
    SB();
    sfor<0, 8>([&](auto E) { ta[E] = vfms(I[E >> 2][E & 3], wq[E], ta[E]); tb[E] = vfma(I[E >> 2][E & 3], wp[E], tb[E]); });
    SB();
    const u32x4 Yr = {pk_bf(ta[0], ta[1]), pk_bf(ta[2], ta[3]), pk_bf(ta[4], ta[5]), pk_bf(ta[6], ta[7])};
    const u32x4 Yi = {pk_bf(tb[0], tb[1]), pk_bf(tb[2], tb[3]), pk_bf(tb[4], tb[5]), pk_bf(tb[6], tb[7])};
    SB();
    sfor<0, 2>([&](auto TT) { I[TT] = mmb(Yr, tG32[1][TT], z4); I[TT] = mmb(Yi, tG32[0][TT], I[TT]); });
    SB();
    sfor<0, 2>([&](auto TT) { P[TT] = mmb(Yr, tG32[0][TT], z4); Q[TT] = mmb(Yi, tG32[1][TT], z4); });
    SB(); mid();
    real(P, Q);
    sfor<0, 8>([&](auto E) { const unsigned t = tw[1][E >> 2][E & 3]; ta[E] = xmul<0, 0>(P[E >> 2][E & 3], t); tb[E] = xmul<1, 0>(P[E >> 2][E & 3], t); });
    SB();
    sfor<0, 8>([&](auto E) { const unsigned t = tw[1][E >> 2][E & 3]; ta[E] = xfma<1, 1>(I[E >> 2][E & 3], t, ta[E]); tb[E] = xfma<0, 0>(I[E >> 2][E & 3], t, tb[E]); });
    SB();
    sfor<0, 2>([&](auto TT) {
      LAS char* p = smem + L_SP + 8192 + (16 * TT + i16) * 128 + (((2 * g + (gq >> 1)) ^ (i16 >> 1)) * 16) + (gq & 1) * 8;
      *(LAS u32x2*)p = u32x2{pk_bf(ta[4 * TT], ta[4 * TT + 1]), pk_bf(ta[4 * TT + 2], ta[4 * TT + 3])};
      *(LAS u32x2*)(p + 4096) = u32x2{pk_bf(tb[4 * TT], tb[4 * TT + 1]), pk_bf(tb[4 * TT + 2], tb[4 * TT + 3])};
    }); };
  auto segC = [&](int T, auto&& mid) {
    const uint32_t vz = (i16 >> 1) * 147456 + (i16 & 1) * 64 + (gq & 1) * 32 + (gq >> 1) * 16, sz = oq_s(WS_ZT + (T == 288 ? 32 : T * 512) + g * 128);
    const u64 zmask = (T == 0 || T == 288) ? 0x0000ffff0000ffffull : ~0ull;
    sfor<0, 2>([&](auto TT) {
      LAS char* q0 = smem + L_SP + 8192 + (16 * TT + i16) * 128 + ((gq ^ (i16 >> 1)) * 16); LAS char* q1 = smem + L_SP + 8192 + (16 * TT + i16) * 128 + (((gq + 4) ^ (i16 >> 1)) * 16);
      uu[0][TT][0] = *(LAS u32x4*)q0; uu[0][TT][1] = *(LAS u32x4*)q1; uu[1][TT][0] = *(LAS u32x4*)(q0 + 4096); uu[1][TT][1] = *(LAS u32x4*)(q1 + 4096); });
    SB(); mid(); SB();
    sfor<0, 2>([&](auto TT) {
      const u32x4 r0 = uu[0][TT][0], r1_ = uu[0][TT][1], i0 = uu[1][TT][0], i1 = uu[1][TT][1];
      f32x4 P = mmb(tG64[0][0], r0, z4); P = mmb(tG64[0][1], r1_, P);
      f32x4 Q = mmb(tG64[1][0], i0, z4); Q = mmb(tG64[1][1], i1, Q);
      f32x4 I = mmb(tG64[1][0], r0, z4); I = mmb(tG64[1][1], r1_, I); I = mmb(tG64[0][0], i0, I); I = mmb(tG64[0][1], i1, I);
      unsigned ra = pk_bf(P.x - Q.x, P.y - Q.y), rb = pk_bf(P.z - Q.z, P.w - Q.w), ia = pk_bf(I.x, I.y), ib = pk_bf(I.z, I.w);
      auto sa = __builtin_amdgcn_permlane16_swap(ra, ia, false, false); auto sb = __builtin_amdgcn_permlane16_swap(rb, ib, false, false);
      const unsigned sa0 = sa[0], sa1 = sa[1], sb0 = sb[0], sb1 = sb[1];
      st16m(u32x4{sa0, sb0, sa1, sb1}, rs, vz, sz + TT * 8 * 147456, zmask);
    }); };
  auto dmas = [&](auto J) { uint32_t t[5]; constexpr uint32_t lb = ((J + 2) & 3) * BUF;
    sfor<0, 5>([&](auto I) { t[I] = lane_of<5 * J + I>(tabw); });
    sfor<0, 4>([&](auto I) { setm0<lb + I * 9216>(sw1); nop0(); dmago(r2, vo2a, t[I]); });
    setm0<lb + 8192>(sh9); dmagoh(r2, vo2b, t[4], hmask); };
  waitv_lb<15>();
  ST_(13);
  ld8c(pl, rs, vfh, 0);
  if (hf == 0) { tif<0>();
    u32x4 hd[4][2];
    Gelu ge, g2;
    pre(lbuf(F0{}));
    blocks(IC<4>{}, T1{}, T1{}, T1{}, lbuf(T1{}), [&](auto B, auto TT) { return hown[B][TT]; },
      [&](auto S) { if constexpr (S < 16) { ge.run<S>(p0, hown[3][0]); g2.run<S>(p1, hown[3][1]); }
        if constexpr (S == 16) st16c<0>(hown[3][0], rs, v16, sow4); if constexpr (S == 17) st16c<1024>(hown[3][1], rs, v16, sow4);
        if constexpr (S >= 17 && S < 25 && (S & 1)) sa = lane_of<8 + (S - 17) / 2>(tabh);
        if constexpr (S >= 18 && S < 26 && !(S & 1)) ld2h(hd[(S - 18) / 2], sa);
        if constexpr (S >= 25 && S < 35 && (S & 1)) dma2p(IC<3>{}, IC<(S - 25) / 2>{});
        if constexpr (S >= 26 && !(S & 1)) dma2g(IC<(S - 26) / 2>{}); });
    ST_(34);
    waitv_b<15>();
    ST_(14);
    hflag(3); check1(pl, 2);
    sfor<0, 4>([&](auto I) { pin(hc[I][0]); pin(hc[I][1]); });
    blocks(IC<4>{}, T1{}, F0{}, T1{}, lbuf(IC<2>{}), [&](auto B, auto TT) { return hc[B][TT]; },
      [&](auto S) { solo(T1{}, S); });
    ST_(35);
    waitv_b<9>();
    ST_(15);
    hflag(4); ld8c(pl, rs, vfh, 0);
    sfor<0, 4>([&](auto I) { pin(hd[I][0]); pin(hd[I][1]); });
    blocks(IC<4>{}, T1{}, F0{}, T1{}, lbuf(IC<3>{}), [&](auto B, auto TT) { return hd[B][TT]; },
      [&](auto S) { solo(IC<2>{}, S); });
    ST_(36);
    kill4(tF64); kill(tw[0][0]); kill(tw[0][1]); kill(p0[0]); kill(p0[1]); kill(p1[0]); kill(p1[1]);
  }
  tend<0>();
  if (oq_s(hf)) { tif<1>();
    if (PRIO & 2) __builtin_amdgcn_s_setprio(1);
    rdu(b);
    { Gelu ge, g2; u32x4 o0, o1;
      const uint32_t g2k = oq_s(g * 2048);
      sfor<0, 16>([&](auto S) { constexpr int C = (S >> 1) & 1, A = S & 1;
        if constexpr (S < 2) ld16c<A * 1024>(wt[A], rs, v16, WS_WT + b * 8192 + g2k);
        else if constexpr (S < 4) ld16<A * 1024>(tw[1][A], rt, v16, TAB_TW + 8192 + g2k);
        else ld16<C * 2048 + A * 1024>((S < 8 ? tF32 : S < 12 ? tG32 : tG64)[C][A], rt, v16, S < 8 ? TAB_F32 : S < 12 ? TAB_G32 : TAB_G64 + 2 * g2k);
        ge.run<S>(p0, o0); g2.run<S>(p1, o1); SB(); });
      if (dbl) { tif<2>(); ldu(T2); } tend<2>();
      st16c<0>(o0, rs, v16, sow4); st16c<1024>(o1, rs, v16, sow4); }
    waitv<25>(); pin4(tF64); pin(tw[0][0]); pin(tw[0][1]);
    segA(b); SB();
    dmas(IC<3>{});
    ST_(34);
    waitv_b<11>();
    ST_(14);
    check1(pl, 2); pin4(tF32); pin4(tG32); pin(tw[1][0]); pin(tw[1][1]);
    segB([&]() { dmas(IC<4>{}); });
    if (dbl) { tif<2>(); waitv<12>(); putu(); ldw(T2); kill4(hr[0]); } else { tif<3>(); ldh4(IC<3>{}); }
    tend<2>(); tend<3>();
    ST_(35);
    waitv_lb<7>();
    ST_(15);
    ld8c(pl, rs, vfh, 0); pin4(tG64);
    segC(b, [&]() { dmas(IC<5>{}); });
    ST_(36);
    if (dbl) { tif<2>();
      rdu(T2); segA(T2); SB();
      waitv_b<7>();
      check1(pl, 3);
      segB([&]() { dmas(IC<6>{}); });
      ldh4(IC<5>{});
      waitv_lb<9>();
      ld8c(pl, rs, vfh, 0);
      segC(T2, [&]() {});
      dmas(IC<7>{});
      ldh4(IC<6>{});
      kill4(hr[1]);
    } else { tif<3>(); ldh4(IC<4>{}); }
    tend<2>(); tend<3>();
    pre((dbl ? lbuf(IC<5>{}) : lbuf(IC<3>{})) + 18432);
    sfor<0, 9>([&](auto DT) { oo[DT] = z4; os[DT] = z4; });
    kill4(hx[0]); kill4(hx[1]);
    if (PRIO & 4) __builtin_amdgcn_s_setprio(0);
  }
  tend<1>();
  u32x4 zr[5], xv, bv;
  u32x2 xv8, bv8;
  uint32_t vta = 0, vtb = 0;
  auto dmat = [&](auto I) {
    if constexpr (I == 0) { const uint32_t t = TAB_TI + (zb0 * 4 + nt) * 9216 - ks * 36 * 36864, c = oq_v(l);
      vta = vo2a - 864 * (((w * 64 + c) * 3641) >> 16) + t; vtb = vo2b - 864 * (((512 + c) * 3641) >> 16) + t + 4 * sh9; }
    if constexpr (I < 4) dmak<3 * BUF + I * 9216, I * 36864>(rt, vta, sw1);
    else if constexpr (I == 4) dmakh<3 * BUF + 8192, 0>(rt, vtb, sh9, hmask);
    else if (ks < 2) { dmak<L_SP, 4 * 36864>(rt, vta, sw1); if (w == 0) dmak<L_SP + 8192, 4 * 36864>(rt, vtb, sh9); }
  };
  const uint32_t azw = (4 * w + 2 * hh + ((l >> 1) & 1)) * 288 + (((l >> 4) & 1) + 2 * ((l >> 2) & 1)) * 64 + ((l >> 3) & 1) * 32 + (l & 1) * 16;
  const uint32_t srow = sx + 288 * nt, vrow = (l >> 2) * 36864;
  auto shared = [&](auto J) {
    if constexpr (J == 8) bar0(); else waitv_b<9>();
    ST_(13 + J);
    if constexpr (J == 3) check1(pl, 3);
    if constexpr (J == 4) { if (tid == 0 && !dbl) setflag(ws, WS_FZ + b * 8, tag); ld8c(pl, rs, vfh, 0); }
    if constexpr (J == 5) check1(pl, 4);
    if constexpr (J == 5) { if (tid == 0 && dbl) setflag(ws, WS_FZ + b * 8, tag); }
    if constexpr (J == 7) { ld16c<0>(pu[0], rs, v16, WS_FZ); ld16c<1024>(pu[1], rs, v16, WS_FZ); }
    pin4(hr[J % 3]);
    SB();
    const uint32_t nxt = lbuf(IC<J + 1>{}) + hf * 18432;
    blocks(IC<2>{}, T1{}, F0{}, T1{}, (J < 5 && dbl) ? lbuf(J) + 18432 : nxt, [&](auto B, auto TT) { return hr[J % 3][B][TT]; },
      [&](auto S) {
        if constexpr (LAD & 2) { if constexpr (S == 17) prio<3>(); if constexpr (S == 4) prio<2>(); if constexpr (S == 9) prio<1>(); if constexpr (S == 13) prio<0>(); }
        if constexpr (J < 6) { if constexpr (S % 4 == 0) dma2p(IC<J + 3>{}, IC<S / 4>{}); if constexpr (S % 4 == 1) dma2g(IC<S / 4>{}); }
        if constexpr (J == 6) { if constexpr (S % 4 == 1 && S < 16) dmat(IC<S / 4>{}); if constexpr (S >= 16) dmat(IC<S - 12>{}); }
        if constexpr (J < 7) { if constexpr (S % 8 == 1) sa = lane_of<4 * (J + 2) + S / 8>(tabh); if constexpr (S % 4 == 3) ldh(IC<J + 2>{}, IC<S / 8>{}, IC<(S / 4) & 1>{}, sa); }
        if constexpr (J == 7) {
          if constexpr (S == 8) { waitv<0>(); check4(pu, WS_FZ); }
          if constexpr (S >= 8 && S < 13) { if constexpr (S == 8) sa = oq_s(WS_ZT + mt * 147456 + zb0 * 8192 + sw1); ld16c<0>(zr[S - 8], rs, v16, sa + (S == 12 && ks >= 2 ? 0 : (int)S - 8) * 8192); }
          if constexpr (S == 14) { ld16<0>(xv, rx, vrow + (l & 3) * 16, srow + 64 * ks); ld8<256>(xv8, rx, vrow + (l & 3) * 8, srow); }
          if constexpr (S == 16) { const u32x4 rb = mksrd(b2, 1152); ld16<0>(bv, rb, (l & 3) * 16, 288 * nt + 64 * ks); ld8<256>(bv8, rb, (l & 3) * 8, 288 * nt); } }
        if constexpr (J == 8 && S >= 13) { waitv<21 - S>(); pin(zr[S - 13]); *(LAS u32x4*)(smem + azw + (S - 13) * 9216) = zr[S - 13]; }
      });
    if constexpr (J < 5) { if (dbl) { tif<2>(); pin4(hx[J & 1]); blocks(IC<2>{}, T1{}, F0{}, T1{}, nxt, [&](auto B, auto TT) { return hx[J & 1][B][TT]; }, [&](auto) {}); } tend<2>(); }
  };
  if (!(oq_s(hf) && dbl)) { tif<5>(); shared(IC<3>{}); shared(IC<4>{}); }
  tend<5>();
  sfor<5, 9>(shared);
  { barl();
    ST_(22);
    const uint32_t az = oq_v(a2 + hf * 18432 + g * 64);
    u32x4 z[2][2];
    sfor<0, 2>([&](auto K) { z[K][0] = tr2(smem + az + K * 9216 + hf * 32, 4608); z[K][1] = tr2(smem + az + K * 9216 + (hf ^ 1) * 32, 4608); });
    const u32x4 z5 = tr2(smem + BUF + a2 + g * 64 + hf * 32, 4608);
    const uint32_t aw = oq_v((hf ? 0 : 2 * BUF) + g * 9216 + v16);
    SB();
    blocks(IC<2>{}, T1{}, F0{}, T1{}, L_SP, [&](auto B, auto TT) { return z[B][TT]; },
      [&](auto S) { if constexpr (S == 9) bar0(); if constexpr (S >= 10) *(LAS f32x4*)(smem + aw + (S - 10) * 1024) = os[S - 10]; });
    *(LAS f32x4*)(smem + aw + 8192) = os[8];
    ST_(23);
    if (ks < 2) blocks(T1{}, F0{}, F0{}, F0{}, 0, [&](auto B, auto TT) { return z5; }, [&](auto S) {});
    barl();
    ST_(24);
    const uint32_t ar = oq_v((hf ? 2 * BUF : 0) + g * 9216 + v16);
    sfor<0, 9>([&](auto DT) { oo[(DT + 2) % 9] += *(LAS f32x4*)(smem + ar + ((DT + 2) % 9) * 1024); });
  }
  auto pk1 = [&](float p, float q) { const unsigned v = pk_bf(p, q); return v < ~1u ? v : ~1u; };
  sfor<1, 4>([&](auto K) { const f32x4 p = oo[2 * K], q = oo[2 * K + 1];
    st16c<0>(u32x4{pk1(p.x, p.y), pk1(p.z, p.w), pk1(q.x, q.y), pk1(q.z, q.w)}, rs, v16, WS_PART + ((b & ~6) + ((ks + K) & 3) * 2) * 32768 + ((K - 1) * 8 + w) * 1024); });
  if (g != ks) st8c(u32x2{pk1(oo[8].x, oo[8].y), pk1(oo[8].z, oo[8].w)}, rs, l * 8, WS_PART + ((b & ~6) + g * 2) * 32768 + 24576 + (((g - ks) & 3) - 1) * 1024 + hf * 512);
  ST_(25);
  { u32x4 pp[3]; u32x2 p8[3];
    const uint32_t s8 = WS_PART + b * 32768 + (g == ks ? 24576 + hf * 512 : w * 1024);
#pragma nounroll
    for (unsigned spins = 0; spins < (1u << 16); ++spins) {
      sfor<0, 3>([&](auto K) { ld16c<0>(pp[K], rs, v16, WS_PART + b * 32768 + (K * 8 + w) * 1024); ld8c(p8[K], rs, l * 8, s8 + (g == ks ? K * 1024 : 0)); });
      waitv<0>();
      unsigned m = 0;
      sfor<0, 3>([&](auto K) { pin(pp[K]); pin(p8[K]); const unsigned a = pp[K].x, c = pp[K].y, d = pp[K].z, e = pp[K].w, f = p8[K].x, h = p8[K].y;
        m = max(max(max(m, a), max(c, d)), max(max(e, f), h)); });
      if (__all(m != ~0u)) break;
      __builtin_amdgcn_s_sleep(2);
    }
    ST_(26);
    LAS char* pt = smem + BUF + w * 3072;
    sfor<0, 3>([&](auto Q) { f32x4 r = oo[Q < 2 ? (int)Q : 8];
      sfor<0, 3>([&](auto K) { const unsigned a = Q == 2 ? p8[K].x : pp[K][2 * (Q & 1)], c = Q == 2 ? p8[K].y : pp[K][2 * (Q & 1) + 1];
        r += f32x4{lo_f(a), hi_f(a), lo_f(c), hi_f(c)}; });
      *(LAS f32x4*)(pt + Q * 1024 + i16 * 64 + gq * 16) = r; });
    pin(xv); pin(bv); pin(xv8); pin(bv8);
    LAS char* pq = pt + ((l >> 1) & 1) * 1024 + (l >> 2) * 64 + (l & 1) * 32;
    const f32x4 ra = *(LAS f32x4*)pq, rb = *(LAS f32x4*)(pq + 16), rc = *(LAS f32x4*)(pt + 2048 + v16);
    auto fin = [&](float a, float c, unsigned xx, unsigned bb) { return pk_bf(a + lo_f(xx) + lo_f(bb), c + hi_f(xx) + hi_f(bb)); };
    const u32x4 ro = mksrd(out, 2048 * 1152);
    st16c<0>(u32x4{fin(ra.x, ra.y, xv.x, bv.x), fin(ra.z, ra.w, xv.y, bv.y), fin(rb.x, rb.y, xv.z, bv.z), fin(rb.z, rb.w, xv.w, bv.w)}, ro, vrow + (l & 3) * 16, srow + 64 * ks);
    if (g == ks) st8c(u32x2{fin(rc.x, rc.y, xv8.x, bv8.x), fin(rc.z, rc.w, xv8.y, bv8.y)}, ro, vrow + (l & 3) * 8, srow + 256);
  }
  if (b == 0 && tid == 0) setflag(ws, WS_EPOCH, tag);
  asm volatile(".Lfk_end:");
}

extern "C" void kernel_launch(void* const* d_in, const int* in_sizes, int n_in, void* d_out, int out_size, void* d_ws, size_t ws_size, hipStream_t stream) {
  (void)hipFuncSetAttribute((const void*)fused_kernel, hipFuncAttributeMaxDynamicSharedMemorySize, LDS_BYTES);
  fused_kernel<<<256, 512, LDS_BYTES, stream>>>((const char*)d_in[0], (const char*)d_in[1], (const char*)d_in[2], (const char*)d_in[4], (const char*)d_in[5],
                                                (const char*)d_in[6], (const char*)d_in[7], (char*)d_out, (char*)d_ws);
}
```

```cpp
#include <hip/hip_runtime.h>
#include <stdint.h>
#include <type_traits>
#define LAD 3
#define ZP 0
#define ZC 11
#define PP 0
#define NW 63
#define PRIO 18

typedef unsigned short u16;
typedef short s16x4 __attribute__((ext_vector_type(4)));
typedef short s16x8 __attribute__((ext_vector_type(8)));
typedef __bf16 bf16x8 __attribute__((ext_vector_type(8)));
typedef __bf16 bf16x2 __attribute__((ext_vector_type(2)));
typedef _Float16 f16x8 __attribute__((ext_vector_type(8)));
typedef _Float16 f16x2 __attribute__((ext_vector_type(2)));
typedef float f32x4 __attribute__((ext_vector_type(4)));
typedef float f32x2 __attribute__((ext_vector_type(2)));
typedef unsigned u32x4 __attribute__((ext_vector_type(4)));
typedef unsigned u32x2 __attribute__((ext_vector_type(2)));
typedef unsigned long long u64;
#define LAS __attribute__((address_space(3)))
#define GAS __attribute__((address_space(1)))
#define DI __device__ __forceinline__

namespace tab {
constexpr double PI = 3.14159265358979323846;
constexpr double ccos(int a, int n) {
  a %= n; if (a < 0) a += n;
  if (2 * a > n) a = n - a;
  bool neg = false;
  if (4 * a > n) { a = n / 2 - a; neg = true; }
  if (4 * a == n) return 0.0;
  double x = 2 * PI * a / n, t = 1, s = 1;
  for (int i = 1; i <= 13; i++) { t *= -x * x / ((2 * i - 1) * (2 * i)); s += t; }
  return neg ? -s : s;
}
constexpr u16 to_bf(double v) { float f = (float)v; uint32_t u = __builtin_bit_cast(uint32_t, f); u += 0x7fffu + ((u >> 16) & 1u); return (u16)(u >> 16); }
constexpr u16 to_h(double v) { _Float16 h = (_Float16)v; return __builtin_bit_cast(u16, h); }
struct C576 { u16 b1[576]; u16 b2[576]; };
constexpr C576 gen576() { C576 r{}; for (int i = 0; i < 576; i++) { double c = ccos(i, 576) / 24.0; r.b1[i] = to_bf(c); r.b2[i] = to_bf(2 * c); } return r; }
constexpr C576 c576 = gen576();
constexpr int colk(int c) { return c == 0 ? 0 : c == 1 ? 288 : c < 288 ? (c & ~1) : ((c - 288) & ~1) + 1; }
constexpr int colp(int c) { return c < 2 ? 0 : (c & 1); }
struct Chunk { u16 v[9216]; };
constexpr Chunk genTF(int u) {
  Chunk r{};
  for (int i = 0; i < 1152; i++) { int t = i / 576, s = (i % 576) >> 6, l = i & 63, c = 32 * u + 16 * t + (l & 15), k = colk(c), o = 144 * colp(c), d = 32 * s + 8 * (l >> 4);
    for (int j = 0; j < 8; j++) r.v[i * 8 + j] = c576.b1[((d + j) * k + o) % 576]; }
  return r;
}
constexpr Chunk genTI(int q) {
  Chunk r{};
  for (int i = 0; i < 64; i++) { int c = 32 * (q >> 1) + (i & 31), k = colk(c), o = 144 * colp(c), d0 = 144 * (2 * (q & 1) + (i >> 5));
    if (c < 2) { for (int d = 0; d < 144; d++) r.v[i * 144 + d] = c576.b1[((d0 + d) * k + o) % 576]; }
    else { for (int d = 0; d < 144; d++) r.v[i * 144 + d] = c576.b2[((d0 + d) * k + o) % 576]; } }
  return r;
}
template <int I> constexpr Chunk cTF = genTF(I);
template <int I> constexpr Chunk cTI = genTI(I);
struct Small { u16 f64[4][2][2][512]; u16 f32[2][2][512]; u16 g32[2][2][512]; u16 g64[4][2][2][512]; };
constexpr Small genSmall() {
  Small r{};
  double c64[64] = {}; for (int i = 0; i < 64; i++) c64[i] = ccos(i, 64);
  const double r32 = 0.17677669529663688110;
  for (int l = 0; l < 64; l++) for (int j = 0; j < 8; j++) {
    int rr = l & 15, gq = l >> 4;
    int p2 = 16 * (j >> 2) + 4 * gq + (j & 3);
    for (int w = 0; w < 4; w++) for (int a = 0; a < 2; a++) {
      int e = (16 * w + rr) * (32 * a + 8 * gq + j), e1 = (16 * w + rr) * (32 * a + p2);
      r.f64[w][0][a][l * 8 + j] = to_h(c64[e1 & 63] / 8); r.f64[w][1][a][l * 8 + j] = to_h(c64[(e1 + 16) & 63] / 8);
      r.g64[w][0][a][l * 8 + j] = to_bf(c64[e & 63] / 8); r.g64[w][1][a][l * 8 + j] = to_bf(c64[(e + 48) & 63] / 8);
    }
    for (int t = 0; t < 2; t++) {
      int e = 2 * ((16 * t + rr) * p2);
      r.f32[0][t][l * 8 + j] = to_h(c64[e & 63] * r32); r.f32[1][t][l * 8 + j] = to_h(c64[(e + 16) & 63] * r32);
      r.g32[0][t][l * 8 + j] = to_bf(c64[e & 63] * r32); r.g32[1][t][l * 8 + j] = to_bf(c64[(e + 48) & 63] * r32);
    }
  }
  return r;
}
struct C2048 { u16 v[2048]; };
constexpr C2048 gen2048() { C2048 r{}; for (int i = 0; i < 2048; i++) r.v[i] = to_h(ccos(i, 2048)); return r; }
constexpr C2048 c2048 = gen2048();
struct Tw { u16 v[2][4][2][512]; };
constexpr Tw genTw() {
  Tw r{};
  for (int w = 0; w < 4; w++) for (int t = 0; t < 2; t++) for (int l = 0; l < 64; l++) for (int j = 0; j < 4; j++) {
    int e0 = (16 * t + 4 * (l >> 4) + j) * (16 * w + (l & 15)), e1 = (16 * t + (l & 15)) * (16 * w + 4 * (l >> 4) + j);
    r.v[0][w][t][l * 8 + 2 * j] = c2048.v[e0 & 2047]; r.v[0][w][t][l * 8 + 2 * j + 1] = c2048.v[(e0 + 1536) & 2047];
    r.v[1][w][t][l * 8 + 2 * j] = c2048.v[e1 & 2047]; r.v[1][w][t][l * 8 + 2 * j + 1] = c2048.v[(e1 + 1536) & 2047]; }
  return r;
}
constexpr Tw ctw = genTw();
constexpr int fblk(int nt, int J, int i) {
  if (J == 0) return 9 * nt + (nt & 1) + 2 * i;
  if (J == 1) { int pn = (nt + i) & 3; return 9 * pn + ((pn & 1) ? 0 : 8); }
  int q = 4 * (J - 2) + i, r = q / 7, m = q - 7 * r + 1, pn = (nt + (m >> 1)) & 3;
  return 9 * pn + (pn & 1) + 2 * r + (m & 1);
}
struct Adr { uint32_t w[4][4][64]; uint32_t h[4][2][64]; uint32_t v[4][576]; };
constexpr Adr genAdr() {
  Adr r{};
  for (int nt = 0; nt < 4; nt++) {
    for (int k = 0; k < 4; k++) for (int l = 0; l < 45; l++) r.w[nt][k][l] = fblk(nt, l / 5, l % 5 < 4 ? l % 5 : k) * 36864 + 288 * nt;
    for (int k = 0; k < 2; k++) for (int l = 0; l < 36; l++) r.h[nt][k][l] = fblk(nt, l / 4, (l + 2 * k) & 3) * 8192;
    for (int c = 0; c < 576; c++) { int k = c % 18; r.v[nt][c] = (c / 18) * 1152 + (k < 16 ? ((k + 4 * nt) & 15) : k) * 16 + nt * 36 * 36864; } }
  return r;
}
struct alignas(256) All { Chunk tf[18]; Chunk ti[36]; Small sm; Tw tw; Adr adr; };
template <int... I, int... J> constexpr All mk(std::integer_sequence<int, I...>, std::integer_sequence<int, J...>) { return All{{cTF<I>...}, {cTI<J>...}, genSmall(), ctw, genAdr()}; }
}
static __device__ const tab::All g_tab = tab::mk(std::make_integer_sequence<int, 18>{}, std::make_integer_sequence<int, 36>{});

constexpr uint32_t BUF = 36864, L_SP = 147456, L_B1 = L_SP, L_JUNK = L_SP + 2048, L_W = L_SP + 4096, LDS_BYTES = 163840;
constexpr uint32_t WS_EPOCH = 0, WS_FU = 4096, WS_FZ = 8192, WS_FH = 32768;
constexpr uint32_t WS_H = 1u << 20, WS_UT = 20u << 20, WS_WT = 23u << 20, WS_ZT = 26u << 20, WS_PART = 29u << 20, WS_END = 48u << 20;
constexpr uint32_t TAB_TF = 0, TAB_TI = 18 * 18432, TAB_F64 = 54 * 18432, TAB_F32 = TAB_F64 + 16384, TAB_G32 = TAB_F32 + 4096, TAB_G64 = TAB_G32 + 4096, TAB_TW = TAB_G64 + 16384, TAB_AW = TAB_TW + 16384, TAB_AH = TAB_AW + 4096, TAB_AV = TAB_AH + 2048, TAB_END = TAB_AV + 9216;

template <int I, int N, class Fn> DI void sfor(Fn&& f) { if constexpr (I < N) { f(std::integral_constant<int, I>{}); sfor<I + 1, N>(f); } }
template <int V> using IC = std::integral_constant<int, V>;

DI u32x4 mksrd(const void* p, uint32_t n) { return u32x4{(unsigned)(size_t)p, (unsigned)((size_t)p >> 32) & 0xffffu, n, 0x00020000u}; }
template <int IMM> DI void ld16(u32x4& d, u32x4 srd, uint32_t voff, uint32_t soff) { asm volatile("buffer_load_dwordx4 %0, %1, %2, %3 offen offset:%4" : "=v"(d) : "v"(voff), "s"(srd), "s"(soff), "i"(IMM) : "memory"); }
template <int IMM> DI void ld16c(u32x4& d, u32x4 srd, uint32_t voff, uint32_t soff) { asm volatile("buffer_load_dwordx4 %0, %1, %2, %3 offen offset:%4 sc1" : "=v"(d) : "v"(voff), "s"(srd), "s"(soff), "i"(IMM) : "memory"); }
template <int IMM> DI void ld8(u32x2& d, u32x4 srd, uint32_t voff, uint32_t soff) { asm volatile("buffer_load_dwordx2 %0, %1, %2, %3 offen offset:%4" : "=v"(d) : "v"(voff), "s"(srd), "s"(soff), "i"(IMM) : "memory"); }
DI void ld8c(u32x2& d, u32x4 srd, uint32_t voff, uint32_t soff) { asm volatile("buffer_load_dwordx2 %0, %1, %2, %3 offen sc1" : "=v"(d) : "v"(voff), "s"(srd), "s"(soff) : "memory"); }
DI void ld4(unsigned& d, u32x4 srd, uint32_t voff, uint32_t soff) { asm volatile("buffer_load_dword %0, %1, %2, %3 offen" : "=v"(d) : "v"(voff), "s"(srd), "s"(soff) : "memory"); }
DI void dma(u32x4 srd, uint32_t voff, uint32_t soff, uint32_t lds) { asm volatile("s_mov_b32 m0, %3\n\ts_nop 0\n\tbuffer_load_dwordx4 %0, %1, %2 offen lds" :: "v"(voff), "s"(srd), "s"(soff), "s"(lds) : "memory"); }
DI void dmah(u32x4 srd, uint32_t voff, uint32_t soff, uint32_t lds, u64 mask) {
  asm volatile("s_mov_b32 m0, %3\n\ts_mov_b64 exec, %4\n\tbuffer_load_dwordx4 %0, %1, %2 offen lds\n\ts_mov_b64 exec, -1" :: "v"(voff), "s"(srd), "s"(soff), "s"(lds), "s"(mask) : "memory"); }
template <uint32_t LDS, uint32_t SO> DI void dmak(u32x4 srd, uint32_t voff, uint32_t base) { uint32_t t;
  asm volatile("s_add_u32 m0, %3, %4\n\ts_mov_b32 %0, %5\n\tbuffer_load_dwordx4 %1, %2, %0 offen lds" : "=&s"(t) : "v"(voff), "s"(srd), "s"(base), "i"(LDS), "i"(SO) : "memory", "scc"); }
template <uint32_t LDS, uint32_t SO> DI void dmakh(u32x4 srd, uint32_t voff, uint32_t base, u64 mask) { uint32_t t;
  asm volatile("s_add_u32 m0, %3, %4\n\ts_mov_b32 %0, %5\n\ts_mov_b64 exec, %6\n\tbuffer_load_dwordx4 %1, %2, %0 offen lds\n\ts_mov_b64 exec, -1"
               : "=&s"(t) : "v"(voff), "s"(srd), "s"(base), "i"(LDS), "i"(SO), "s"(mask) : "memory", "scc"); }
template <int L> DI uint32_t lane_of(uint32_t t) { uint32_t r; asm volatile("v_readlane_b32 %0, %1, %2" : "=s"(r) : "v"(t), "i"(L)); return r; }
template <uint32_t LDS> DI void setm0(uint32_t base) { asm volatile("s_add_u32 m0, %0, %1" :: "s"(base), "i"(LDS) : "scc"); }
DI void dmago(u32x4 srd, uint32_t voff, uint32_t soff) { asm volatile("buffer_load_dwordx4 %0, %1, %2 offen lds" :: "v"(voff), "s"(srd), "s"(soff) : "memory"); }
DI void dmagoh(u32x4 srd, uint32_t voff, uint32_t soff, u64 mask) {
  asm volatile("s_mov_b64 exec, %3\n\tbuffer_load_dwordx4 %0, %1, %2 offen lds\n\ts_mov_b64 exec, -1" :: "v"(voff), "s"(srd), "s"(soff), "s"(mask) : "memory"); }
DI void nop4() { asm volatile("s_nop 4"); }
DI void nop0() { asm volatile("s_nop 0"); }
DI void touch(u32x4 srd, uint32_t voff, uint32_t soff, u64 mask) {
  asm volatile("s_mov_b32 m0, %3\n\ts_mov_b64 exec, %4\n\tbuffer_load_dword %0, %1, %2 offen lds\n\ts_mov_b64 exec, -1" :: "v"(voff), "s"(srd), "s"(soff), "s"(L_JUNK), "s"(mask) : "memory"); }
DI void code_touch(int part, int l, u64 mask) {
  u64 pc; uint32_t n;
  asm volatile("s_getpc_b64 %0\n.Lfk_begin:\n\ts_mov_b32 %1, .Lfk_end-.Lfk_begin+4096" : "=s"(pc), "=s"(n));
  touch(u32x4{(unsigned)pc, (unsigned)(pc >> 32) & 0xffffu, n, 0x00020000u}, l * 128, part * 2048, mask); }
template <int IMM> DI void st16c(u32x4 d, u32x4 srd, uint32_t voff, uint32_t soff) {
  asm volatile("buffer_store_dwordx4 %0, %1, %2, %3 offen offset:%4 sc1\n\ts_nop 1" :: "v"(d), "v"(voff), "s"(srd), "s"(soff), "i"(IMM) : "memory"); }
DI void st16m(u32x4 d, u32x4 srd, uint32_t voff, uint32_t soff, u64 mask) {
  asm volatile("s_mov_b64 exec, %4\n\tbuffer_store_dwordx4 %0, %1, %2, %3 offen sc1\n\ts_mov_b64 exec, -1\n\ts_nop 0" :: "v"(d), "v"(voff), "s"(srd), "s"(soff), "s"(mask) : "memory"); }
DI void st8c(u32x2 d, u32x4 srd, uint32_t voff, uint32_t soff) {
  asm volatile("buffer_store_dwordx2 %0, %1, %2, %3 offen sc1\n\ts_nop 1" :: "v"(d), "v"(voff), "s"(srd), "s"(soff) : "memory"); }
DI void st8m(u32x2 d, u32x4 srd, uint32_t voff, uint32_t soff, u64 mask) {
  asm volatile("s_mov_b64 exec, %4\n\tbuffer_store_dwordx2 %0, %1, %2, %3 offen sc1\n\ts_mov_b64 exec, -1\n\ts_nop 0" :: "v"(d), "v"(voff), "s"(srd), "s"(soff), "s"(mask) : "memory"); }
template <int IMM> DI void dsw16m(u32x4 d, uint32_t addr, u64 mask) {
  asm volatile("s_mov_b64 exec, %2\n\tds_write_b128 %1, %0 offset:%3\n\ts_mov_b64 exec, -1" :: "v"(d), "v"(addr), "s"(mask), "i"(IMM) : "memory"); }
DI u64 m_lt(uint32_t a, uint32_t b) { u64 m; asm("s_cmp_lt_u32 %1, %2\n\ts_cselect_b64 %0, -1, 0" : "=s"(m) : "s"(a), "s"(b) : "scc"); return m; }
DI u64 m_eq(uint32_t a, uint32_t b) { u64 m; asm("s_cmp_eq_u32 %1, %2\n\ts_cselect_b64 %0, -1, 0" : "=s"(m) : "s"(a), "s"(b) : "scc"); return m; }
#define UNL(c) __builtin_expect(!!(c), 0)
template <int N> DI void waitv() { asm volatile("s_waitcnt vmcnt(%0)" :: "i"(N) : "memory"); }
template <int N> DI void waitv_b() { asm volatile("s_waitcnt vmcnt(%0)\n\ts_barrier" :: "i"(N) : "memory"); }
template <int N> DI void waitv_lb() { asm volatile("s_waitcnt vmcnt(%0) lgkmcnt(0)\n\ts_barrier" :: "i"(N) : "memory"); }
DI void bar0() { asm volatile("s_barrier" ::: "memory"); }
DI void barl() { asm volatile("s_waitcnt lgkmcnt(0)\n\ts_barrier" ::: "memory"); }
DI void pin(u32x4& v) { asm volatile("" : "+v"(v)); }
DI void pin(u32x2& v) { asm volatile("" : "+v"(v)); }
DI void pin(unsigned& v) { asm volatile("" : "+v"(v)); }
DI void fix(float& v) { asm volatile("" : "+v"(v)); }
DI void kill(u32x4& v) { asm volatile("" : "=v"(v)); }
DI void kill(f32x4& v) { asm volatile("" : "=v"(v)); }
DI uint32_t oq_s(uint32_t v) { asm volatile("" : "+s"(v)); return v; }
DI uint32_t oq_v(uint32_t v) { asm volatile("" : "+v"(v)); return v; }
template <int P> DI void prio() { asm volatile("s_setprio %0" :: "i"(P)); }
#define SB() __builtin_amdgcn_sched_barrier(0)
template <int C> DI void tif() { asm volatile("; @IF %0" :: "i"(C)); }
template <int C> DI void tend() { asm volatile("; @END %0" :: "i"(C)); }

DI float asf(unsigned u) { return __builtin_bit_cast(float, u); }
DI unsigned asu(float f) { return __builtin_bit_cast(unsigned, f); }
DI unsigned pk_bf(float a, float b) { f32x2 p = {a, b}; bf16x2 r = __builtin_convertvector(p, bf16x2); return __builtin_bit_cast(unsigned, r); }
DI unsigned pk_h(float a, float b) { f32x2 p = {a, b}; f16x2 r = __builtin_convertvector(p, f16x2); return __builtin_bit_cast(unsigned, r); }
DI float bfr(float a) { return asf(pk_bf(a, a) << 16); }
DI float lo_f(unsigned u) { return asf(u << 16); }
DI float hi_f(unsigned u) { return asf(u & 0xffff0000u); }
DI float dot2(unsigned a, unsigned b, float c) { return __builtin_amdgcn_fdot2_f32_bf16(__builtin_bit_cast(bf16x2, a), __builtin_bit_cast(bf16x2, b), c, false); }
DI float vfma(float a, float b, float c) { float d; asm("v_fma_f32 %0, %1, %2, %3" : "=v"(d) : "v"(a), "v"(b), "v"(c)); return d; }
DI float vmul(float a, float b) { float d; asm("v_mul_f32 %0, %1, %2" : "=v"(d) : "v"(a), "v"(b)); return d; }
DI float vfms(float a, float b, float c) { float d; asm("v_fma_f32 %0, -%1, %2, %3" : "=v"(d) : "v"(a), "v"(b), "v"(c)); return d; }
template <int H, int N> DI float xmul(float a, unsigned t) { float d;
  if constexpr (N) asm("v_fma_mix_f32 %0, -%1, %2, 0 op_sel:[0,%3,0] op_sel_hi:[0,1,0]" : "=v"(d) : "v"(a), "v"(t), "i"(H));
  else asm("v_fma_mix_f32 %0, %1, %2, 0 op_sel:[0,%3,0] op_sel_hi:[0,1,0]" : "=v"(d) : "v"(a), "v"(t), "i"(H));
  return d; }
template <int H, int N> DI float xfma(float a, unsigned t, float c) { float d;
  if constexpr (N) asm("v_fma_mix_f32 %0, -%1, %2, %3 op_sel:[0,%4,0] op_sel_hi:[0,1,0]" : "=v"(d) : "v"(a), "v"(t), "v"(c), "i"(H));
  else asm("v_fma_mix_f32 %0, %1, %2, %3 op_sel:[0,%4,0] op_sel_hi:[0,1,0]" : "=v"(d) : "v"(a), "v"(t), "v"(c), "i"(H));
  return d; }
template <int H, int D> DI void xfmah(unsigned& d, float a, unsigned t, float c) {
  if constexpr (D == 0) asm("v_fma_mixlo_f16 %0, %1, %2, %3 op_sel:[0,%4,0] op_sel_hi:[0,1,0]" : "=v"(d) : "v"(a), "v"(t), "v"(c), "i"(H));
  else asm("v_fma_mixhi_f16 %0, %1, %2, %3 op_sel:[0,%4,0] op_sel_hi:[0,1,0]" : "+v"(d) : "v"(a), "v"(t), "v"(c), "i"(H)); }
template <int CTRL> DI float dpp_add(float v) { return v + asf(__builtin_amdgcn_update_dpp(0, asu(v), CTRL, 0xf, 0xf, true)); }
DI float sum8(float v) { return dpp_add<0x141>(dpp_add<0x4E>(dpp_add<0xB1>(v))); }
DI f32x4 mmb(u32x4 a, u32x4 b, f32x4 c) { return __builtin_amdgcn_mfma_f32_16x16x32_bf16(__builtin_bit_cast(bf16x8, a), __builtin_bit_cast(bf16x8, b), c, 0, 0, 0); }
DI f32x4 mmh(u32x4 a, u32x4 b, f32x4 c) { return __builtin_amdgcn_mfma_f32_16x16x32_f16(__builtin_bit_cast(f16x8, a), __builtin_bit_cast(f16x8, b), c, 0, 0, 0); }
DI u32x4 tr2(LAS char* p, int second) {
  s16x4 a = __builtin_amdgcn_ds_read_tr16_b64_v4i16((LAS s16x4*)p);
  s16x4 b = __builtin_amdgcn_ds_read_tr16_b64_v4i16((LAS s16x4*)(p + second));
  s16x8 c = __builtin_shufflevector(a, b, 0, 1, 2, 3, 4, 5, 6, 7);
  return __builtin_bit_cast(u32x4, c);
}
struct Gelu { float a0, a1;
  template <int Q> DI void step(float v0, float v1, unsigned& o) {
    if constexpr (Q == 0) { a0 = __builtin_fmaf(v0 * v0, -0.10294324f, -2.3022082f); a1 = __builtin_fmaf(v1 * v1, -0.10294324f, -2.3022082f); }
    if constexpr (Q == 1) { a0 = __builtin_amdgcn_exp2f(v0 * a0); a1 = __builtin_amdgcn_exp2f(v1 * a1); }
    if constexpr (Q == 2) { a0 = __builtin_amdgcn_rcpf(1.0f + a0); a1 = __builtin_amdgcn_rcpf(1.0f + a1); }
    if constexpr (Q == 3) { o = pk_bf(v0 * a0, __builtin_fmaf(v1, a1, 0.0f)); }
  }
  template <int K> DI void run(const f32x4 (&p)[2], u32x4& o) {
    constexpr int P = K >> 2; const float v0 = p[P >> 1][2 * (P & 1)], v1 = p[P >> 1][2 * (P & 1) + 1];
    unsigned t = 0; step<K & 3>(v0, v1, t); if constexpr ((K & 3) == 3) o[P] = t; }
};
#define ST_(i) do {} while (0)
#define RLX_AGENT __ATOMIC_RELAXED, __HIP_MEMORY_SCOPE_AGENT
template <int N> DI void spin(const char* base, uint32_t off, u64 tag) {
#pragma nounroll
  for (unsigned spins = 0; spins < (1u << 17); ++spins) {
    bool ok = true;
#pragma unroll
    for (int k = 0; k < N; ++k) { u64 v = __hip_atomic_load((GAS u64*)(base + off + (k & 1) * 8 + (k >> 1) * 1024), RLX_AGENT); ok &= v == tag; }
    if (__all(ok)) break;
    __builtin_amdgcn_s_sleep(1);
  }
}

__global__ __launch_bounds__(512, 2) void fused_kernel(const char* __restrict__ x, const char* __restrict__ wr, const char* __restrict__ wi,
    const char* __restrict__ w1, const char* __restrict__ b1, const char* __restrict__ w2, const char* __restrict__ b2, char* out, char* ws) {
  extern __shared__ __attribute__((aligned(16))) char smem_[];
  LAS char* smem = (LAS char*)smem_;
  const int tid = threadIdx.x, l = tid & 63;
  const int w = __builtin_amdgcn_readfirstlane(tid >> 6), g = w & 3, hf = w >> 2;
  const int b = blockIdx.x, xcd = b & 7, jj = b >> 3;
  const uint32_t le = oq_v(l);
  code_touch(jj, le, m_eq(w, 7) & 0xffff);
  SB();
  const int ks = xcd >> 1, mt = (xcd & 1) * 8 + (jj >> 2), nt = jj & 3, fj = ks * 4 + nt;
  const u32x4 rt = mksrd(&g_tab, TAB_END);
  ST_(0);
  u32x4 x0[18], x1[18];
  const uint32_t vx = __umul24(le >> 3, 36864) + (le & 7) * 16;
  const uint32_t sx = (uint32_t)((32 * (g & 1) + 16 * hf) * 32 + 2 * mt + (g >> 1)) * 1152;
  u32x2 ep;
  { const uint32_t i = jj * 9 + le - 18, ta = oq_v((le & 1) * (18 * 9216) + (le >> 1) * 128 + (4 * ks * 9216 + jj * 9 * 128)),
      tb = oq_v(((int)i >= 144 ? 32 * 9216 : 16 * 9216) + i * 128);
    touch(rt, le < 18 ? ta : tb, TAB_TF, m_eq(w, 6) & (((1ull << 18) - 1) | (m_eq(ks, 2) & ((1ull << 27) - 1)))); }
  const uint32_t vt5 = __umul24(le >> 1, 9216) + (le & 1) * 1152, st5 = ks * 2304 + jj * 18 * 9216 + 512, sx1 = sx + 294912;
  const u64 mt5 = m_eq(w, 5) & ((1ull << 36) - 1);
  asm volatile("" :: "v"(vt5), "v"(vx), "s"(st5), "s"(sx), "s"(sx1), "s"(mt5), "s"(hf));
  SB();
  asm volatile("" : "+s"(x), "+s"(w1), "+s"(w2), "+s"(ws), "+s"(out), "+s"(b1), "+s"(b2), "+s"(wr), "+s"(wi));
  const u32x4 rs = mksrd(ws, WS_END), rx = mksrd(x, 2048 * 1152), r1 = mksrd(w1, 576 * 9216);
  touch(r1, vt5, st5, mt5);
  asm volatile("s_cmp_eq_u32 %0, 0\n\ts_cbranch_scc1 .Lfk_b0\n\ts_barrier\n.Lfk_b0:" :: "s"(hf) : "scc", "memory");
  sfor<0, 18>([&](auto C) { ld16<(C % 9) * 128>(x1[C], rx, vx, C < 9 ? sx : sx1); }); ld8c(ep, rs, 0, WS_EPOCH);
  asm volatile("s_cmp_lg_u32 %0, 0\n\ts_cbranch_scc1 .Lfk_b1\n\ts_barrier\n.Lfk_b1:" :: "s"(hf) : "scc", "memory");
  SB();
  const u32x4 r2 = mksrd(w2, 4608 * 1152);
  const int i16 = l & 15, q4 = i16 >> 2, p4 = i16 & 3, hh = l >> 5, gq = l >> 4;
  const u64 hmask = (w & 1) ? 0xffffffff00000000ull : 0xffffffffull;
  const uint32_t v16 = l * 16;
  const int odd = fj & 1, lone = odd ? 0 : 8;
  const int tileA = 18 * odd + 2 * (fj >> 1), tileC = 18 * odd + 16 + (nt >> 1);
  unsigned b1v;
  const uint32_t vo1 = (8 * hh + ((l >> 3) & 3)) * 9216 + (((l & 7) ^ (((l >> 4) & 3) << 1)) * 16);
  uint32_t v1a = 0, v1b = 0, sown = 0, sbox = 0;
  const uint32_t sw4 = oq_s(w * 4096), sw1 = oq_s(w * 1024), sh1 = oq_s((w >> 1) * 1024), sh9 = oq_s((w >> 1) * 9216);
  auto dma1 = [&](auto ST, auto I) {
    constexpr uint32_t so = 288 * (ST & 1) * 9216 + 128 * ((ST >> 1) - 1), lb = (ST & 3) * BUF;
    if constexpr (I < 4) dmak<lb + I * 1024, so + (16 * (I >> 1) + 4 * (I & 1)) * 9216>(r1, v1a, sw4);
    else dmakh<lb + 32768, so>(r1, v1b, sh1, hmask);
  };
  const uint32_t axb = 2 * BUF + g * 18432;
  const uint32_t axw = axb + ((l >> 2) & 1) * 1024 + (l & 3) * 256 + (((l >> 3) ^ (l & 7)) * 16);
  const uint32_t axr0 = axb + gq * 256 + ((i16 ^ gq) * 16), axr1 = axr0 ^ 64;
  auto rdx = [&](u32x4 (&d)[18], auto S) { d[S] = *(LAS u32x4*)(smem + ((S & 1) ? axr1 : axr0) + S * 1024); };
  f32x2 rn2[2], mb2[2];
  auto stats = [&](auto RB, auto NV, auto&& fill) {
    float sa[4] = {}, qa[4] = {};
    sfor<0, 9>([&](auto C) { fill(C);
      waitv<NV + 17 - 9 * RB - C>(); pin(x1[9 * RB + C]);
      sfor<0, 4>([&](auto D) { const unsigned u = x1[9 * RB + C][(int)D]; sa[D] = dot2(u, 0x3f803f80u, sa[D]); qa[D] = dot2(u, u, qa[D]); });
      SB(); });
    rn2[RB] = f32x2{(sa[0] + sa[1]) + (sa[2] + sa[3]), (qa[0] + qa[1]) + (qa[2] + qa[3])}; };
  auto chain = [&](auto RB) {
    const float mean = sum8(rn2[RB].x) * (1.0f / 576.0f);
    const float var = fmaxf(sum8(rn2[RB].y) * (1.0f / 576.0f) - mean * mean, 0.0f);
    const float sd = bfr(__builtin_amdgcn_sqrtf(bfr(bfr(var) + 1e-5f)));
    const float rn = __builtin_amdgcn_rcpf(sd), mb = -bfr(mean) * rn;
    rn2[RB] = f32x2{rn, rn}; mb2[RB] = f32x2{mb, mb}; };
  auto put = [&](auto RB, auto C) { *(LAS u32x4*)(smem + axw + RB * 128 + C * 2048) = x1[9 * RB + C]; };
  auto norm = [&](auto RB, auto&& fill) {
    sfor<0, 9>([&](auto C) { f32x2 f[4];
      sfor<0, 4>([&](auto D) { const unsigned u = x1[9 * RB + C][(int)D]; f[D] = f32x2{lo_f(u), hi_f(u)}; });
      SB();
      sfor<0, 4>([&](auto D) { f[D] = __builtin_elementwise_fma(f[D], rn2[RB], mb2[RB]); });
      SB();
      x1[9 * RB + C] = u32x4{pk_bf(f[0].x, f[0].y), pk_bf(f[1].x, f[1].y), pk_bf(f[2].x, f[2].y), pk_bf(f[3].x, f[3].y)};
      fill(C);
      SB(); }); };
  using T1 = IC<1>; using F0 = IC<0>;
  u64 tag;
  auto mktag = [&]() { pin(ep);
    tag = ((u64)(unsigned)__builtin_amdgcn_readfirstlane(ep.y) << 32) | (unsigned)__builtin_amdgcn_readfirstlane(ep.x);
    tag += 0x9E3779B97F4A7C15ull; tag = (tag ^ (tag >> 30)) * 0xBF58476D1CE4E5B9ull; tag = (tag ^ (tag >> 27)) * 0x94D049BB133111EBull; tag = (tag ^ (tag >> 31)); };
  f32x4 c0[2], c1[2], p0[2], p1[2];
  auto bias = [&](int unit, auto FT) { return *(LAS f32x4*)(smem + L_B1 + (32 * unit + 16 * FT + 4 * gq) * 4); };
  const int rho = 16 * (gq >> 1) + 4 * (gq & 1) + q4;
  uint32_t vo2a, vo2b;
  const uint32_t sh = WS_H + (mt * 144 * 4 + g) * 2048;
  uint32_t tabw, tabh;
  uint32_t sq = 0;
  auto dma2p = [&](auto J, auto I) { constexpr uint32_t lb = ((J + 2) & 3) * BUF;
    sq = lane_of<5 * J + I>(tabw); if constexpr (I < 4) setm0<lb + I * 9216>(sw1); else setm0<lb + 8192>(sh9); };
  auto dma2g = [&](auto I) { if constexpr (I < 4) dmago(r2, vo2a, sq); else dmagoh(r2, vo2b, sq, hmask); };
  const uint32_t vho = v16 + hf * 1024, vhs = v16 + (hf ^ 1) * 1024;
  const uint32_t vfh = ((b & ~24) + gq * 8) * 128;
  auto check1 = [&](u32x2& p, unsigned lvl) { pin(p);
    if (UNL(__builtin_amdgcn_ballot_w64(((p.x ^ (unsigned)tag) | (p.y ^ (unsigned)(tag >> 32))) != 0) != 0)) spin<1>(ws, lvl * WS_FH + vfh, tag); };
  auto check4 = [&](u32x4 (&p)[2], uint32_t base) { pin(p[0]); pin(p[1]);
    const unsigned tl = (unsigned)tag, th = (unsigned)(tag >> 32);
    const unsigned d = ((p[0].x ^ tl) | (p[0].y ^ th) | (p[0].z ^ tl) | (p[0].w ^ th)) | ((p[1].x ^ tl) | (p[1].y ^ th) | (p[1].z ^ tl) | (p[1].w ^ th));
    if (UNL(__builtin_amdgcn_ballot_w64(d != 0) != 0)) spin<4>(ws, base + v16, tag); };
  const u64 lane0 = m_eq(w, 0) & 1;
  auto flag = [&](uint32_t off, u64 mask) { st8m(u32x2{(unsigned)tag, (unsigned)(tag >> 32)}, rs, v16, off, mask); };
  const int zb0 = 4 * ks + min(ks, 2);
  const int T2 = 256 + (xcd & 1) * 32 + jj;
  const bool dbl = ks == 3 && T2 <= 288;
  u32x4 tF32[2][2], tG32[2][2], tG64[2][2], ur[2], wt[2], pu[2], hc[4][2], hown[4][2];
  u32x4 (*const tF64)[2] = hc, (*const tw)[2] = hc + 2;
  u32x2 pl;

  const uint32_t a1 = rho * 128 + (((2 * hf) ^ ((rho >> 1) & 3)) * 32) + p4 * 8;
  uint32_t pa0 = 0, pa1 = 0;
  u32x4 fr[4];
  auto rd1 = [&](auto ST, auto S) {
    if constexpr (S == 0) { pa0 = oq_v(a1 + (ST & 3) * BUF); pa1 = oq_v(pa0 ^ 32); }
    return tr2(smem + ((S & 1) ? pa1 : pa0) + (S >> 1) * 4096, 1024); };
  auto ldu = [&](int T) { sfor<0, 2>([&](auto Q) { ld16c<Q * 1024>(ur[Q], rs, v16, WS_UT + (T == 288 ? 0 : T * 8192) + g * 2048); }); };
  auto putu = [&](auto CB, u64 mask) { sfor<0, 2>([&](auto Q) {
    dsw16m<CB * 8192>(ur[Q], L_SP + (g >> 1) * 4096 + ((l >> 2) & 1) * 2048 + ((l >> 3) & 1) * 1024 + (l & 3) * 256 + (((8 * (g & 1) + 4 * Q + gq) ^ (l & 7)) * 16), mask); }); };
  auto ld2h = [&](u32x4 (&h)[2], uint32_t so) { ld16c<0>(h[0], rs, v16, so); ld16c<1024>(h[1], rs, v16, so); };
  uint32_t s4[4] = {}; u32x4 rq = rs;
  f32x4 bi[2];
  auto wtask = [&](int cl) {
    LAS char* p = smem + L_W + (l & 7) * 656 + (l & 1) * 2 + cl * 2 + (b >= 192 ? 12 : 0); unsigned v[8];
    sfor<0, 8>([&](auto J) { v[J] = *(LAS u16*)(p + (J >> 2) * 5248 + (J & 3) * 160); });
    const int kc = 72 * (b >> 6) + cl;
    st16c<0>(u32x4{v[0] | (v[1] << 16), v[2] | (v[3] << 16), v[4] | (v[5] << 16), v[6] | (v[7] << 16)}, rs,
             (uint32_t)(kc == 288 ? 288 : (kc & 1) ? 144 + (kc >> 1) : (kc >> 1)) * 8192 + (l & 7) * 16,
             WS_WT + ((b >> 1) & 3) * 2048 + ((b >> 5) & 1) * 1024 + ((b >> 3) & 3) * 256 + (b & 1) * 128); };
  auto stage = [&](auto ST, auto HF) {
    constexpr int R = ST >> 1, KB = 9 * (ST & 1);
    waitv_b<(ST == 9 ? 9 : ST == 2 ? 13 + 2 * HF : ST == 3 ? 13 + HF : ST == 5 ? 6 : ST == 8 ? 9 : 7)>();
    ST_(3 + ST);
    if constexpr (ST == 3) { pin(tabw); pin(tabh); pin(vo2a); pin(vo2b); tabh += sh + 36 * 8192 * ks; }
    if constexpr (ST == 5) flag(WS_FU + oq_s(b) * 8, lane0);
    if constexpr (ST == 7 || ST == 9) flag((ST - 5) / 2 * WS_FH + oq_s(b) * 128, lane0);
    if constexpr (ST <= 4) sfor<0, 3>([&](auto S) { fr[(18 * ST + S) & 3] = rd1(ST, S); });
    if constexpr (ST == 2) { bi[0] = bias(odd + hf, IC<0>{}); bi[1] = bias(odd + hf, IC<1>{}); }
    Gelu ge; u32x4 o;
    SB();
    sfor<0, 18>([&](auto S) {
      constexpr int G = 18 * ST + S, FT = S & 1;
      if constexpr (LAD & 1) { if constexpr (S == 0) prio<3>(); if constexpr (S == 5) prio<2>(); if constexpr (S == 10) prio<1>(); if constexpr (S == 14) prio<0>(); }
      if constexpr (S >= 2 && !(S & 1)) { __builtin_amdgcn_s_waitcnt(0xC27F); SB(); }
      const u32x4 a = fr[G & 3];
      if constexpr ((ST & 1) == 0 && S < 2) c0[FT] = mmb(a, x0[KB + (S >> 1)], bi[FT]); else c0[FT] = mmb(a, x0[KB + (S >> 1)], c0[FT]);
      SB();
      if constexpr (S + 3 < 18) fr[(G + 3) & 3] = rd1(ST, IC<S + 3>{});
      else if constexpr (ST > 3 && ST != 9) fr[(G + 3) & 3] = rd1(IC<ST + 1>{}, IC<S + 3 - 18>{});
      SB();
      if constexpr ((ST & 1) == 0 && S < 2) c1[FT] = mmb(a, x1[KB + (S >> 1)], bi[FT]); else c1[FT] = mmb(a, x1[KB + (S >> 1)], c1[FT]);
      SB();
      if constexpr (HF != 1 && S >= 1 && S < 17) { if constexpr (ST & 1) ge.run<S - 1>(p1, o); else ge.run<S - 1>(p0, o); }
      if constexpr (ST == 2 && S % 3 == 2 && S / 3 < 5) dma1(IC<5>{}, IC<S / 3>{});
      if constexpr (S % 3 == 1 && S / 3 < 5) { if constexpr (ST < 7) dma1(IC<(ST == 2 ? 4 : ST + 3)>{}, IC<S / 3>{}); else dma2g(IC<S / 3>{}); }
      if constexpr (ST >= 7 && S % 3 == 0 && S / 3 < 5) dma2p(IC<ST - 7>{}, IC<S / 3>{});
      if constexpr (HF == 1 && ST == 3 && (S == 3 || S == 9)) wtask(8 * g + (S == 3 ? 0 : 32) + (l >> 3));
      if constexpr (ST >= 5 && ST < 8 && S == 15) { st16c<0>(u32x4{~0u, ~0u, ~0u, ~0u}, rs, v16, sbox + (ST - 5) * 8192);
        if constexpr (ST == 5) st16m(u32x4{~0u, ~0u, ~0u, ~0u}, rs, v16, sbox + 24576, m_lt(w, 3)); }
      if constexpr (HF == 1 && ST == 3 && S == 15) { if (UNL(g * 192 <= b)) wtask(g == 0 ? 64 + (l >> 3) : 72); }
      if constexpr (ST == 7 && S == 14) { ld16c<0>(pu[0], rs, v16, WS_FU); ld16c<1024>(pu[1], rs, v16, WS_FU); }
      if constexpr (ST == 8 && S == 14) ld8c(pl, rs, vfh, WS_FH);
      if constexpr (ST == 8 && S == 8) { waitv<5>(); check4(pu, WS_FU); ldu(b); }
      if constexpr (ST == 9 && S % 3 == 2 && S < 12) { ld16c<0>(hc[S / 3][0], rq, v16, s4[S / 3]); ld16c<1024>(hc[S / 3][1], rq, v16, s4[S / 3]); }
      if constexpr (ST == 9 && S == 15) { waitv<14>(); putu(F0{}, m_eq(hf, 1)); pin(pl); }
      if constexpr (HF != 1 && S == 17) { st16c<(ST & 1) * 1024>(o, rs, v16, R == 1 ? sh + (9 * fj + lone) * 8192 : sown + 2 * (R - 2) * 8192); if constexpr (R > 1) hown[R - 2][ST & 1] = o; }
      if constexpr ((ST & 1) && ST < 9 && S == 17) { bi[0] = bias(odd + 2 * R + hf, IC<0>{}); bi[1] = bias(odd + 2 * R + hf, IC<1>{}); }
      if constexpr (ST == 8 && S == 17) { rq = hf ? rt : rs;
        sfor<0, 4>([&](auto I) { const uint32_t t = lane_of<4 + I>(tabh); s4[I] = hf ? (I < 2 ? TAB_F64 + g * 4096 + I * 2048 : TAB_TW + (I - 2) * 8192 + g * 2048) : t; }); }
      SB();
    });
    if constexpr (ST & 1) { p0[0] = c0[0]; p0[1] = c0[1]; p1[0] = c1[0]; p1[1] = c1[1]; }
  };
  auto fill0 = [&](auto S) {
    if constexpr (S == 0) { v1a = oq_v(vo1 + 32 * w * 9216 + (288 * fj + 32 * odd) * 2); v1b = oq_v(vo1 + (256 + 16 * hf + 4 * ((w >> 1) & 1)) * 9216 + (288 * fj + 32 * odd) * 2);
      ST_(30); barl(); ST_(31); }
    else if constexpr (S < 6) dma1(IC<2>{}, IC<S - 1>{});
    else if constexpr (S < 11) dma1(IC<3>{}, IC<S - 6>{});
    else if constexpr (S < 13) {
      const int j = (l * 13) >> 7;
      dmah(mksrd(S == 12 ? wi : wr, 2048 * 578), j * 36992 + (l - 10 * j) * 16, ((8 * (b & 7) + w + 256 * ((b >> 3) & 7)) * 578 + (b >= 192 ? 420 : 144 * (b >> 6))) & ~3u,
           L_W + (S - 11) * 5248 + w * 656, (1ull << 40) - 1); }
    else if constexpr (S == 13) { ld4(tabw, rt, l * 4, TAB_AW + nt * 1024 + (w >> 1) * 256); ld4(tabh, rt, l * 4, TAB_AH + nt * 512 + hf * 256); }
    else if constexpr (S == 14) { ld4(vo2a, rt, l * 4, TAB_AV + ks * 2304 + w * 256); ld4(vo2b, rt, l * 4, TAB_AV + ks * 2304 + 2048); }
    else { constexpr int N = (324 - 4 * NW + 7) / 8; touch(r2, (jj * 324 + 4 * NW + w * N + l) * 128, ks * 1152 * 1152, (1ull << N) - 1);
      touch(rt, l < 6 ? TAB_TI + zb0 * 36864 + ((jj * 8 + w) * 6 + l) * 128 : TAB_F64 + ((jj * 8 + w) * 2 + l - 6) * 128, 0, 0xffull); } };
  const f32x4 z4 = {0, 0, 0, 0};
  const uint32_t su = WS_UT + (2 * mt + (g >> 1)) * 256 + (g & 1) * 128;
  auto ust = [&](const f32x4 (&u)[4], uint32_t so) { f32x4 a, c; const float sg = odd ? -1.0f : 1.0f;
    sfor<0, 4>([&](auto J) { a[(int)J] = __builtin_fmaf(u[2][(int)J], sg, u[0][(int)J]); c[(int)J] = __builtin_fmaf(u[3][(int)J], sg, u[1][(int)J]); });
    st16c<0>(u32x4{pk_h(a.x, a.y), pk_h(a.z, a.w), pk_h(c.x, c.y), pk_h(c.z, c.w)}, rs, (i16 >> 1) * 8192 + (i16 & 1) * 64 + gq * 16,
             so); };
  if (hf) { tif<1>();
    stats(F0{}, T1{}, [&](auto) {}); chain(F0{}); SB();
    ST_(1);
    bar0();
    ST_(2);
    norm(F0{}, [&](auto C) { rdx(x0, IC<2 * C>{}); rdx(x0, IC<2 * C + 1>{}); });
    ST_(28);
    barl();
    ST_(3);
    stats(T1{}, T1{}, [&](auto C) { put(F0{}, C); }); chain(T1{}); norm(T1{}, [&](auto C) { put(T1{}, C); });
    waitv<0>(); mktag();
    ST_(29); barl(); ST_(4);
    const uint32_t at = oq_v(BUF + v16), suA = oq_s(su + tileA * 65536);
    f32x4 ua[2][4];
    auto rd = [&](auto S) { return *(LAS u32x4*)(smem + at + (S & 1) * 9216 + (S >> 1) * 1024); };
    sfor<0, 2>([&](auto S) { fr[S] = rd(S); });
    sfor<0, 9>([&](auto S) { rdx(x1, S); rdx(x1, IC<S + 9>{}); }); SB();
    sfor<0, 18>([&](auto S) { constexpr int I = S >> 1, TT = S & 1; const u32x4 t = fr[S & 3];
      ua[TT][0] = mmb(x0[I], t, I ? ua[TT][0] : z4); SB();
      if constexpr (S + 2 < 18) fr[(S + 2) & 3] = rd(IC<S + 2>{});
      SB();
      ua[TT][2] = mmb(x0[I + 9], t, I ? ua[TT][2] : z4); SB();
      if constexpr (S >= 2) fill0(IC<S - 2>{});
      SB();
      ua[TT][1] = mmb(x1[I], t, I ? ua[TT][1] : z4); ua[TT][3] = mmb(x1[I + 9], t, I ? ua[TT][3] : z4); SB(); });
    ST_(32);
    ust(ua[0], suA); ust(ua[1], suA + 65536);
    ST_(33);
    if (PRIO & 1) __builtin_amdgcn_s_setprio(1);
    stage(IC<2>{}, IC<1>{}); stage(IC<3>{}, IC<1>{});
  }
  tend<1>();
  if (oq_s(hf) == 0) { tif<0>();
    ld4(b1v, mksrd(b1, 9216), tid * 4, 576 * fj);
    const int i1 = jj * 144 + g * 36 + l;
    const uint32_t vt1 = oq_v((i1 >> 3) * 9216 + ((i1 >> 2) & 1) * 1152 + ((i1 >> 1) & 1) * 640 + (i1 & 1) * 128);
    sfor<0, 2>([&](auto RB) { stats(RB, IC<2>{}, [&](auto) {}); ST_(1 + RB);
      if constexpr (RB == 1 && PP == 0) touch(r1, vt1, ks * 2304, (1ull << 36) - 1);
      chain(RB); norm(RB, [&](auto C) { put(RB, C); }); });
    waitv<PP == 0>(); pin(b1v);
    if (tid < 144) *(LAS f32x2*)(smem + L_B1 + tid * 8) = f32x2{lo_f(b1v), hi_f(b1v)};
    ST_(28);
    barl();
    sfor<0, 18>([&](auto S) { rdx(x0, S); });
    if constexpr (PP == 1) touch(r1, vt1, ks * 2304, (1ull << 36) - 1);
    const uint32_t vo = (8 * ((l >> 4) & 1) + 4 * hh + ((l >> 2) & 3)) * 9216 + (((l & 3) ^ (((l >> 4) & 1) << 1)) * 16), so = (288 * fj + 32 * lone) * 2 + 16 * g * 9216;
    sfor<0, 9>([&](auto Q) { if constexpr (Q == 2) { barl(); ST_(3); }
      dma(r1, vo, so + 64 * Q * 9216, (g + 4 * Q) * 1024);
      if constexpr (Q < 4) dma(rt, v16, TAB_TF + tileA * 9216 + (g + 4 * Q) * 1024, BUF + (g + 4 * Q) * 1024); });
    dmah(rt, v16, TAB_TF + tileA * 9216 + (g + 16) * 1024, BUF + (g + 16) * 1024, m_lt(g, 2));
    if (UNL(ks == 2)) { sfor<0, 2>([&](auto Q) { dma(rt, v16, TAB_TF + tileC * 9216 + (g + 4 * Q) * 1024, BUF + 18432 + (g + 4 * Q) * 1024); });
      if (g == 3) dma(rt, v16, TAB_TF + tileC * 9216 + 8192, BUF + 18432 + 8192); }
    if constexpr (PP == 2) touch(r1, vt1, ks * 2304, (1ull << 36) - 1);
    touch(r1, vt1, ks * 2304 + 256, (1ull << 36) - 1);
    touch(r2, (jj * 324 + g * NW + l) * 128, ks * 1152 * 1152, (1ull << NW) - 1);
    mktag();
    const f32x4 bl[2] = {bias(lone, F0{}), bias(lone, T1{})};
    const uint32_t al0 = oq_v(rho * 64 + (gq & 1) * 32 + p4 * 8), al1 = oq_v(al0 ^ 32);
    auto rd = [&](auto S) { return tr2(smem + ((S & 1) ? al1 : al0) + (S >> 1) * 2048, 512); };
    waitv<2 + (PP == 2)>(); ST_(29); bar0(); ST_(4);
    sfor<0, 3>([&](auto S) { fr[S] = rd(S); });
    sfor<0, 18>([&](auto S) { rdx(x1, S); }); SB();
    sfor<0, 36>([&](auto S) { const u32x4 a = fr[S & 3];
      p0[S & 1] = mmb(a, x0[S >> 1], S < 2 ? bl[S & 1] : p0[S & 1]); SB();
      if constexpr (S + 3 < 36) fr[(S + 3) & 3] = rd(IC<S + 3>{});
      SB();
      p1[S & 1] = mmb(a, x1[S >> 1], S < 2 ? bl[S & 1] : p1[S & 1]); SB();
      if constexpr (S >= 4 && !(S & 1)) fill0(IC<(S - 4) / 2>{});
      SB(); });
    ST_(32);
    if (UNL(ks == 2)) { f32x4 uc[4]; const uint32_t at = oq_v(BUF + 18432 + v16), suC = oq_s(su + tileC * 65536);
      u32x4 tc[9];
      sfor<0, 9>([&](auto I) { tc[I] = *(LAS u32x4*)(smem + at + I * 1024); });
      SB();
      sfor<0, 9>([&](auto I) { const u32x4 t = tc[I];
        uc[0] = mmb(x0[I], t, I ? uc[0] : z4); uc[2] = mmb(x0[I + 9], t, I ? uc[2] : z4); uc[1] = mmb(x1[I], t, I ? uc[1] : z4); uc[3] = mmb(x1[I + 9], t, I ? uc[3] : z4); });
      ust(uc, suC); }
    ST_(33);
    stage(IC<2>{}, IC<0>{}); stage(IC<3>{}, IC<0>{});
  }
  tend<0>();
  sown = oq_s(sh + (9 * fj + odd + hf) * 8192); sbox = oq_s(WS_PART + b * 32768 + w * 1024);
  sfor<4, 10>([&](auto ST) { stage(ST, IC<2>{}); });

  f32x4 oo[9], os[9];
  u32x4 f6[6];
  const uint32_t a2 = (4 * gq + q4) * 288 + p4 * 8;
  uint32_t pb = 0;
  auto rd2 = [&](auto S) { f6[S % 6] = tr2(smem + pb + (S / 9) * 9216 + (S % 9) * 32, 4608); };
  auto pre = [&](uint32_t cur) { pb = oq_v(a2 + cur); sfor<0, 4>(rd2); SB(); };
  auto blocks = [&](auto NB, auto NEXT, auto ZERO, auto BOTH, uint32_t nxt, auto&& hfrag, auto&& fill) {
    sfor<0, 9 * NB>([&](auto S) {
      constexpr int B = S / 9, DT = S % 9;
      if constexpr (!(S & 1) && (NEXT || S + 4 < 9 * NB)) { __builtin_amdgcn_s_waitcnt(0xC47F); SB(); }
      const u32x4 a = f6[S % 6];
      oo[DT] = mmb(a, hfrag(IC<B>{}, F0{}), (ZERO && B == 0) ? z4 : oo[DT]);
      SB();
      if constexpr (S + 4 < 9 * NB) rd2(IC<S + 4>{});
      else if constexpr (NEXT) { if constexpr (S + 4 == 9 * NB) pb = oq_v(a2 + nxt); rd2(IC<S + 4 - 9 * NB>{}); }
      SB();
      if constexpr (BOTH) os[DT] = mmb(a, hfrag(IC<B>{}, T1{}), (ZERO && B == 0) ? z4 : os[DT]);
      SB();
      fill(S);
      SB();
    }); };
  auto lbuf = [&](auto J) { return (uint32_t)(((J + 2) & 3) * BUF); };
  const uint32_t sow4 = sown + 6 * 8192;
  u32x4 hr[3][2][2];
  uint32_t sa = 0, sb = 0;
  auto ldh = [&](auto J, auto K, auto TT, uint32_t so) { ld16c<0>(hr[J % 3][K][TT], rs, TT ? vhs : vho, so); };
  auto ldh4 = [&](auto J) { sa = lane_of<4 * J>(tabh); sb = lane_of<4 * J + 1>(tabh); nop4(); sfor<0, 4>([&](auto I) { ldh(J, IC<I / 2>{}, IC<I & 1>{}, I < 2 ? sa : sb); }); };
  auto solo = [&](auto J, auto S) {
    if constexpr (S % 4 == 0 && S < 20) dma2p(IC<J + 3>{}, IC<S / 4>{});
    if constexpr (S % 4 == 1 && S < 20) dma2g(IC<S / 4>{});
    if constexpr (S == 20 && J == 1) { waitv<5>(); check1(pl, 2); }
    if constexpr (S == 19) { sa = lane_of<4 * (J + 2)>(tabh); sb = lane_of<4 * (J + 2) + 1>(tabh); }
    if constexpr (S >= 21 && S < 29 && (S & 1)) ldh(IC<J + 2>{}, IC<(S - 21) / 4>{}, IC<((S - 21) / 2) & 1>{}, S < 25 ? sa : sb);
  };
  auto pin4 = [&](u32x4 (&t)[2][2]) { pin(t[0][0]); pin(t[0][1]); pin(t[1][0]); pin(t[1][1]); };
  auto kill4 = [&](u32x4 (&t)[2][2]) { kill(t[0][0]); kill(t[0][1]); kill(t[1][0]); kill(t[1][1]); };
  auto hflag = [&](int lvl) { flag(lvl * WS_FH + oq_s(b) * 128, lane0); };
  u32x4 uu[2][2][2], Br, Bi;
  auto rdu = [&](auto CB, int T) { LAS char* pi = smem + L_SP + CB * 8192 + 2048 + gq * 256 + ((i16 ^ (4 + gq)) * 16); LAS char* pr = T == 288 ? pi : smem + L_SP + CB * 8192 + gq * 256 + ((i16 ^ gq) * 16);
    sfor<0, 2>([&](auto TT) { sfor<0, 2>([&](auto A) { uu[0][TT][A] = *(LAS u32x4*)(pr + TT * 4096 + A * 1024); uu[1][TT][A] = *(LAS u32x4*)(pi + TT * 4096 + A * 1024); }); }); };
  auto ldw = [&](int T) { sfor<0, 2>([&](auto P) { ld16c<P * 1024>(wt[P], rs, v16, WS_WT + T * 8192 + g * 2048); }); };
  auto real = [&](f32x4 (&P)[2], const f32x4 (&Q)[2]) { SB(); sfor<0, 8>([&](auto E) { float r = P[E >> 2][E & 3] - Q[E >> 2][E & 3]; fix(r); P[E >> 2][E & 3] = r; }); SB(); };
  auto segA = [&](bool real_in) __attribute__((always_inline)) {
    if (UNL(real_in)) sfor<0, 2>([&](auto TT) { sfor<0, 2>([&](auto A) { uu[1][TT][A] = u32x4{0, 0, 0, 0}; }); });
    unsigned br[4], bq[4]; f32x4 P[2], Q[2], I[2]; float ta[8], tb[8];
    sfor<0, 2>([&](auto TT) { I[TT] = mmh(uu[0][TT][0], tF64[1][0], z4); I[TT] = mmh(uu[0][TT][1], tF64[1][1], I[TT]);
      I[TT] = mmh(uu[1][TT][0], tF64[0][0], I[TT]); I[TT] = mmh(uu[1][TT][1], tF64[0][1], I[TT]); });
    SB();
    sfor<0, 2>([&](auto TT) { P[TT] = mmh(uu[0][TT][0], tF64[0][0], z4); P[TT] = mmh(uu[0][TT][1], tF64[0][1], P[TT]);
      Q[TT] = mmh(uu[1][TT][0], tF64[1][0], z4); Q[TT] = mmh(uu[1][TT][1], tF64[1][1], Q[TT]); });
    real(P, Q);
    sfor<0, 8>([&](auto E) { const unsigned t = tw[0][E >> 2][E & 3]; ta[E] = xmul<0, 0>(P[E >> 2][E & 3], t); tb[E] = xmul<1, 1>(P[E >> 2][E & 3], t); });
    SB();
    sfor<0, 8>([&](auto X) { constexpr int E = (X & 3) * 2 + (X >> 2); const unsigned t = tw[0][E >> 2][E & 3];
      xfmah<1, E & 1>(br[E / 2], I[E >> 2][E & 3], t, ta[E]); xfmah<0, E & 1>(bq[E / 2], I[E >> 2][E & 3], t, tb[E]); if constexpr (X == 3) SB(); });
    Br = u32x4{br[0], br[1], br[2], br[3]}; Bi = u32x4{bq[0], bq[1], bq[2], bq[3]}; pin(Br); pin(Bi); };
  auto segB = [&](auto CB, auto&& mid) __attribute__((always_inline)) {
    float ta[8], tb[8], wp[8], wq[8]; f32x4 P[2], Q[2], I[2];
    pin(wt[0]); pin(wt[1]);
    sfor<0, 2>([&](auto TT) { I[TT] = mmh(tF32[1][TT], Br, z4); I[TT] = mmh(tF32[0][TT], Bi, I[TT]); });
    SB();
    sfor<0, 2>([&](auto TT) { P[TT] = mmh(tF32[0][TT], Br, z4); Q[TT] = mmh(tF32[1][TT], Bi, z4); });
    SB();
    sfor<0, 8>([&](auto E) { const unsigned a = wt[E >> 2][(E >> 1) & 1], c = wt[E >> 2][2 + ((E >> 1) & 1)]; wp[E] = (E & 1) ? hi_f(a) : lo_f(a); wq[E] = (E & 1) ? hi_f(c) : lo_f(c); });
    real(P, Q);
    sfor<0, 8>([&](auto E) { ta[E] = vmul(P[E >> 2][E & 3], wp[E]); tb[E] = vmul(P[E >> 2][E & 3], wq[E]); });
    SB();
    sfor<0, 8>([&](auto E) { ta[E] = vfms(I[E >> 2][E & 3], wq[E], ta[E]); tb[E] = vfma(I[E >> 2][E & 3], wp[E], tb[E]); });
    SB();
    const u32x4 Yr = {pk_bf(ta[0], ta[1]), pk_bf(ta[2], ta[3]), pk_bf(ta[4], ta[5]), pk_bf(ta[6], ta[7])};
    const u32x4 Yi = {pk_bf(tb[0], tb[1]), pk_bf(tb[2], tb[3]), pk_bf(tb[4], tb[5]), pk_bf(tb[6], tb[7])};
    SB();
    sfor<0, 2>([&](auto TT) { I[TT] = mmb(Yr, tG32[1][TT], z4); I[TT] = mmb(Yi, tG32[0][TT], I[TT]); });
    SB();
    sfor<0, 2>([&](auto TT) { P[TT] = mmb(Yr, tG32[0][TT], z4); Q[TT] = mmb(Yi, tG32[1][TT], z4); });
    SB(); mid();
    real(P, Q);
    sfor<0, 8>([&](auto E) { const unsigned t = tw[1][E >> 2][E & 3]; ta[E] = xmul<0, 0>(P[E >> 2][E & 3], t); tb[E] = xmul<1, 0>(P[E >> 2][E & 3], t); });
    SB();
    sfor<0, 8>([&](auto E) { const unsigned t = tw[1][E >> 2][E & 3]; ta[E] = xfma<1, 1>(I[E >> 2][E & 3], t, ta[E]); tb[E] = xfma<0, 0>(I[E >> 2][E & 3], t, tb[E]); });
    SB();
    sfor<0, 2>([&](auto TT) {
      LAS char* p = smem + L_SP + CB * 8192 + (16 * TT + i16) * 128 + (((2 * g + (gq >> 1)) ^ (i16 >> 1)) * 16) + (gq & 1) * 8;
      *(LAS u32x2*)p = u32x2{pk_bf(ta[4 * TT], ta[4 * TT + 1]), pk_bf(ta[4 * TT + 2], ta[4 * TT + 3])};
      *(LAS u32x2*)(p + 4096) = u32x2{pk_bf(tb[4 * TT], tb[4 * TT + 1]), pk_bf(tb[4 * TT + 2], tb[4 * TT + 3])};
    }); };
  auto segC = [&](auto CB, int T) __attribute__((always_inline)) {
    const uint32_t vz = (i16 >> 1) * 147456 + (i16 & 1) * 64 + (gq & 1) * 32 + (gq >> 1) * 16, sz = oq_s(WS_ZT + (T == 288 ? 32 : T * 512) + g * 128);
    const u64 zmask = ~((m_eq(T, 0) | m_eq(T, 288)) & 0xffff0000ffff0000ull);
    sfor<0, 2>([&](auto TT) {
      LAS char* q0 = smem + L_SP + CB * 8192 + (16 * TT + i16) * 128 + ((gq ^ (i16 >> 1)) * 16); LAS char* q1 = smem + L_SP + CB * 8192 + (16 * TT + i16) * 128 + (((gq + 4) ^ (i16 >> 1)) * 16);
      uu[0][TT][0] = *(LAS u32x4*)q0; uu[0][TT][1] = *(LAS u32x4*)q1; uu[1][TT][0] = *(LAS u32x4*)(q0 + 4096); uu[1][TT][1] = *(LAS u32x4*)(q1 + 4096); });
    sfor<0, 2>([&](auto TT) {
      const u32x4 r0 = uu[0][TT][0], r1_ = uu[0][TT][1], i0 = uu[1][TT][0], i1 = uu[1][TT][1];
      f32x4 P = mmb(tG64[0][0], r0, z4); P = mmb(tG64[0][1], r1_, P);
      f32x4 Q = mmb(tG64[1][0], i0, z4); Q = mmb(tG64[1][1], i1, Q);
      f32x4 I = mmb(tG64[1][0], r0, z4); I = mmb(tG64[1][1], r1_, I); I = mmb(tG64[0][0], i0, I); I = mmb(tG64[0][1], i1, I);
      unsigned ra = pk_bf(P.x - Q.x, P.y - Q.y), rb = pk_bf(P.z - Q.z, P.w - Q.w), ia = pk_bf(I.x, I.y), ib = pk_bf(I.z, I.w);
      auto sa = __builtin_amdgcn_permlane16_swap(ra, ia, false, false); auto sb = __builtin_amdgcn_permlane16_swap(rb, ib, false, false);
      const unsigned sa0 = sa[0], sa1 = sa[1], sb0 = sb[0], sb1 = sb[1];
      st16m(u32x4{sa0, sb0, sa1, sb1}, rs, vz, sz + TT * 8 * 147456, zmask);
    }); };
  auto dmas = [&](auto J) { uint32_t t[5]; constexpr uint32_t lb = ((J + 2) & 3) * BUF;
    sfor<0, 5>([&](auto I) { t[I] = lane_of<5 * J + I>(tabw); });
    sfor<0, 4>([&](auto I) { setm0<lb + I * 9216>(sw1); nop0(); dmago(r2, vo2a, t[I]); });
    setm0<lb + 8192>(sh9); dmagoh(r2, vo2b, t[4], hmask); };
  waitv_lb<15>();
  ST_(13);
  if (hf == 0) { tif<0>();
    u32x4 hd[4][2];
    Gelu ge, g2;
    pre(lbuf(F0{}));
    blocks(IC<4>{}, T1{}, T1{}, T1{}, lbuf(T1{}), [&](auto B, auto TT) { return hown[B][TT]; },
      [&](auto S) { if constexpr (S < 16) { ge.run<S>(p0, hown[3][0]); g2.run<S>(p1, hown[3][1]); }
        if constexpr (S == 16) st16c<0>(hown[3][0], rs, v16, sow4); if constexpr (S == 17) st16c<1024>(hown[3][1], rs, v16, sow4);
        if constexpr (S == 17) check1(pl, 1);
        if constexpr (S >= 17 && S < 25 && (S & 1)) sa = lane_of<8 + (S - 17) / 2>(tabh);
        if constexpr (S >= 18 && S < 26 && !(S & 1)) ld2h(hd[(S - 18) / 2], sa);
        if constexpr (S >= 25 && S < 35 && (S & 1)) dma2p(IC<3>{}, IC<(S - 25) / 2>{});
        if constexpr (S >= 26 && !(S & 1)) dma2g(IC<(S - 26) / 2>{});
        if constexpr (S == 35) ld8c(pl, rs, vfh, 2 * WS_FH); });
    ST_(34);
    waitv_b<16>();
    ST_(14);
    hflag(3);
    sfor<0, 4>([&](auto I) { pin(hc[I][0]); pin(hc[I][1]); });
    blocks(IC<4>{}, T1{}, F0{}, T1{}, lbuf(IC<2>{}), [&](auto B, auto TT) { return hc[B][TT]; },
      [&](auto S) { solo(T1{}, S); });
    ST_(35);
    waitv_b<9>();
    ST_(15);
    hflag(4); ld8c(pl, rs, vfh, 3 * WS_FH);
    sfor<0, 4>([&](auto I) { pin(hd[I][0]); pin(hd[I][1]); });
    blocks(IC<4>{}, T1{}, F0{}, T1{}, lbuf(IC<3>{}), [&](auto B, auto TT) { return hd[B][TT]; },
      [&](auto S) { solo(IC<2>{}, S); });
    ST_(36);
    sfor<0, 8>([&](auto I) { kill(hc[I / 2][I & 1]); });
    kill4(tG64); kill(p0[0]); kill(p0[1]); kill(p1[0]); kill(p1[1]);
  }
  tend<0>();
  if (oq_s(hf)) { tif<1>();
    if (PRIO & 2) __builtin_amdgcn_s_setprio(1);
    rdu(F0{}, 0); kill(ur[0]); kill(ur[1]);
    if (UNL(dbl)) { tif<2>(); ldu(T2); } tend<2>();
    { Gelu ge, g2; u32x4 o0, o1;
      const uint32_t g2k = oq_s(g * 2048);
      sfor<0, 16>([&](auto S) { constexpr int C = (S >> 1) & 1, A = S & 1;
        if constexpr (S < 2) ld16c<A * 1024>(wt[A], rs, v16, WS_WT + b * 8192 + g2k);
        else if constexpr (S >= 4) ld16<C * 2048 + A * 1024>((S < 8 ? tF32 : S < 12 ? tG32 : tG64)[C][A], rt, v16, S < 8 ? TAB_F32 : S < 12 ? TAB_G32 : TAB_G64 + 2 * g2k);
        ge.run<S>(p0, o0); g2.run<S>(p1, o1); SB(); });
      st16c<0>(o0, rs, v16, sow4); st16c<1024>(o1, rs, v16, sow4); }
    if (PRIO & 8) __builtin_amdgcn_s_setprio(0);
    waitv<18>(); sfor<0, 8>([&](auto I) { pin(hc[I / 2][I & 1]); });
    segA(b == 0); SB();
    dmas(IC<3>{});
    if (UNL(dbl)) { tif<2>(); waitv<21>(); putu(T1{}, ~0ull); } tend<2>();
    ld8c(pl, rs, vfh, 2 * WS_FH);
    ST_(34);
    if (PRIO & 16) __builtin_amdgcn_s_setprio(0);
    waitv_lb<12>();
    ST_(14);
    pin4(tF32); pin4(tG32);
    segB(F0{}, [&]() { dmas(IC<4>{}); });
    waitv<5>(); check1(pl, 2);
    if (UNL(dbl)) { tif<2>(); ldw(T2); rdu(T1{}, T2); segA(T2 == 288); } tend<2>();
    ldh4(IC<3>{});
    ST_(35);
    waitv_lb<9>();
    ST_(15);
    ld8c(pl, rs, vfh, 3 * WS_FH); pin4(tG64);
    segC(F0{}, b);
    dmas(IC<5>{});
    ldh4(IC<4>{});
    ST_(36);
    if (UNL(dbl)) { tif<2>(); waitv<16>(); segB(T1{}, [&]() {}); asm volatile("s_waitcnt lgkmcnt(0)" ::: "memory"); } tend<2>();
    pre(lbuf(IC<3>{}) + 18432);
    sfor<0, 9>([&](auto DT) { oo[DT] = z4; os[DT] = z4; });
    if (PRIO & 4) __builtin_amdgcn_s_setprio(0);
  }
  tend<1>();
  const u64 mdbl = m_eq(ks, 3) & m_lt(T2, 289);
  u32x4 zr[5], xv, bv;
  u32x2 xv8, bv8; unsigned ov;
  const u32x4 ro = mksrd(out, 2048 * 1152);
  uint32_t vta = 0, vtb = 0;
  auto dmat = [&](auto I) {
    if constexpr (I == 0) { const uint32_t t = TAB_TI + (zb0 * 4 + nt) * 9216 - ks * 36 * 36864, c = oq_v(l);
      vta = vo2a - 864 * (((w * 64 + c) * 3641) >> 16) + t; vtb = vo2b - 864 * (((512 + c) * 3641) >> 16) + t + 4 * sh9; }
    if constexpr (I < 4) dmak<3 * BUF + I * 9216, I * 36864>(rt, vta, sw1);
    else if constexpr (I == 4) dmakh<3 * BUF + 8192, 0>(rt, vtb, sh9, hmask);
    else { dmakh<L_SP, 4 * 36864>(rt, vta, sw1, m_lt(ks, 2)); dmakh<L_SP + 8192, 4 * 36864>(rt, vtb, sh9, m_lt(ks + 2 * w, 2)); }
  };
  const uint32_t azw = (4 * w + 2 * hh + ((l >> 1) & 1)) * 288 + (((l >> 4) & 1) + 2 * ((l >> 2) & 1)) * 64 + ((l >> 3) & 1) * 32 + (l & 1) * 16;
  const uint32_t srow = sx + 288 * nt, vrow = (l >> 2) * 36864;
  auto shared = [&](auto J) {
    if constexpr (J == 8) bar0(); else waitv_b<9>();
    ST_(13 + J);
    if constexpr (J == 3) { flag(WS_FZ + oq_s(b) * 8, lane0 & ~mdbl); check1(pl, 3); if (UNL(dbl && oq_s(hf))) { tif<6>(); segC(T1{}, T2); } tend<6>(); }
    if constexpr (J == 4) { flag(WS_FZ + oq_s(b) * 8, lane0 & mdbl); ld8c(pl, rs, vfh, 4 * WS_FH); }
    if constexpr (J == 5) check1(pl, 4);
    pin4(hr[J % 3]);
    SB();
    const uint32_t nxt = lbuf(IC<J + 1>{}) + hf * 18432;
    blocks(IC<2>{}, T1{}, F0{}, T1{}, nxt, [&](auto B, auto TT) { return hr[J % 3][B][TT]; },
      [&](auto S) {
        if constexpr (LAD & 2) { if constexpr (S == 17) prio<3>(); if constexpr (S == 4) prio<2>(); if constexpr (S == 9) prio<1>(); if constexpr (S == 13) prio<0>(); }
        if constexpr (J < 6) { if constexpr (S % 4 == 0) dma2p(IC<J + 3>{}, IC<S / 4>{}); if constexpr (S % 4 == 1) dma2g(IC<S / 4>{}); }
        if constexpr (J == 6) { if constexpr (S % 4 == 1 && S < 16) dmat(IC<S / 4>{}); if constexpr (S >= 16) dmat(IC<S - 12>{}); }
        if constexpr (J < 7) { if constexpr (S % 8 == 1) sa = lane_of<4 * (J + 2) + S / 8>(tabh); if constexpr (S % 4 == 3) ldh(IC<J + 2>{}, IC<S / 8>{}, IC<(S / 4) & 1>{}, sa); }
        if constexpr (J == 7) {
          if constexpr (S == ZP) { ld16c<0>(pu[0], rs, v16, WS_FZ); ld16c<1024>(pu[1], rs, v16, WS_FZ); }
          if constexpr (S == ZC) { sa = oq_s(WS_ZT + mt * 147456 + zb0 * 8192 + sw1); waitv<0>(); check4(pu, WS_FZ); }
          if constexpr (S >= ZC && S < ZC + 3) sfor<2 * (S - ZC), (S == ZC + 2 ? 5 : 2 * (S - ZC) + 2)>([&](auto K) { ld16c<0>(zr[K], rs, v16, sa + (K == 4 && ks >= 2 ? 0 : (int)K) * 8192); });
        }
        if constexpr (J == 8) {
          if constexpr (S == 0) { ld16<0>(xv, rx, vrow + (l & 3) * 16, srow + 64 * ks); ld8<256>(xv8, rx, vrow + (l & 3) * 8, srow); }
          if constexpr (S == 1) { const u32x4 rb = mksrd(b2, 1152); ld16<0>(bv, rb, (l & 3) * 16, 288 * nt + 64 * ks); ld8<256>(bv8, rb, (l & 3) * 8, 288 * nt); }
          if constexpr (S == 2) ld4(ov, ro, vrow, srow + 64 * ks); }
        if constexpr (J == 8 && S >= 13) { waitv<22 - S>(); pin(zr[S - 13]); *(LAS u32x4*)(smem + azw + (S - 13) * 9216) = zr[S - 13]; }
      });
  };
  sfor<3, 9>(shared);
  { barl();
    ST_(22);
    const uint32_t az = oq_v(a2 + hf * 18432 + g * 64);
    u32x4 z[2][2];
    sfor<0, 2>([&](auto K) { z[K][0] = tr2(smem + az + K * 9216 + hf * 32, 4608); z[K][1] = tr2(smem + az + K * 9216 + (hf ^ 1) * 32, 4608); });
    const u32x4 z5 = tr2(smem + BUF + a2 + g * 64 + hf * 32, 4608);
    const uint32_t aw = oq_v((hf ? 0 : 2 * BUF) + g * 9216 + v16);
    SB();
    blocks(IC<2>{}, T1{}, F0{}, T1{}, L_SP, [&](auto B, auto TT) { return z[B][TT]; },
      [&](auto S) { if constexpr (S == 9) bar0(); if constexpr (S >= 10) *(LAS f32x4*)(smem + aw + (S - 10) * 1024) = os[S - 10]; });
    *(LAS f32x4*)(smem + aw + 8192) = os[8];
    ST_(23);
    if (ks < 2) blocks(T1{}, F0{}, F0{}, F0{}, 0, [&](auto B, auto TT) { return z5; }, [&](auto S) {});
    barl();
    ST_(24);
    const uint32_t ar = oq_v((hf ? 2 * BUF : 0) + g * 9216 + v16);
    sfor<0, 9>([&](auto DT) { oo[(DT + 2) % 9] += *(LAS f32x4*)(smem + ar + ((DT + 2) % 9) * 1024); });
  }
  auto pk1 = [&](float p, float q) { const unsigned v = pk_bf(p, q); return v < ~1u ? v : ~1u; };
  sfor<1, 4>([&](auto K) { const f32x4 p = oo[2 * K], q = oo[2 * K + 1];
    st16c<0>(u32x4{pk1(p.x, p.y), pk1(p.z, p.w), pk1(q.x, q.y), pk1(q.z, q.w)}, rs, v16, WS_PART + ((b & ~6) + ((ks + K) & 3) * 2) * 32768 + ((K - 1) * 8 + w) * 1024); });
  st8m(u32x2{pk1(oo[8].x, oo[8].y), pk1(oo[8].z, oo[8].w)}, rs, l * 8, WS_PART + ((b & ~6) + g * 2) * 32768 + 23552 + ((g - ks) & 3) * 1024 + hf * 512, ~m_eq(g, ks));
  ST_(25);
  { u32x4 pp[3]; u32x2 p8[3];
    const uint32_t s8 = WS_PART + b * 32768 + (g == ks ? 24576 + hf * 512 : w * 1024);
#pragma nounroll
    for (unsigned spins = 0; spins < (1u << 16); ++spins) {
      sfor<0, 3>([&](auto K) { ld16c<0>(pp[K], rs, v16, WS_PART + b * 32768 + (K * 8 + w) * 1024); ld8c(p8[K], rs, l * 8, s8 + (g == ks ? K * 1024 : 0)); });
      waitv<0>();
      unsigned m = 0;
      sfor<0, 3>([&](auto K) { pin(pp[K]); pin(p8[K]); const unsigned a = pp[K].x, c = pp[K].y, d = pp[K].z, e = pp[K].w, f = p8[K].x, h = p8[K].y;
        m = max(max(max(m, a), max(c, d)), max(max(e, f), h)); });
      if (__all(m != ~0u)) break;
      __builtin_amdgcn_s_sleep(2);
    }
    ST_(26);
    LAS char* pt = smem + BUF + w * 3072;
    sfor<0, 3>([&](auto Q) { f32x4 r = oo[Q < 2 ? (int)Q : 8];
      sfor<0, 3>([&](auto K) { const unsigned a = Q == 2 ? p8[K].x : pp[K][2 * (Q & 1)], c = Q == 2 ? p8[K].y : pp[K][2 * (Q & 1) + 1];
        r += f32x4{lo_f(a), hi_f(a), lo_f(c), hi_f(c)}; });
      *(LAS f32x4*)(pt + Q * 1024 + i16 * 64 + gq * 16) = r; });
    pin(ov); pin(xv); pin(bv); pin(xv8); pin(bv8);
    LAS char* pq = pt + ((l >> 1) & 1) * 1024 + (l >> 2) * 64 + (l & 1) * 32;
    const f32x4 ra = *(LAS f32x4*)pq, rb = *(LAS f32x4*)(pq + 16), rc = *(LAS f32x4*)(pt + 2048 + v16);
    auto fin = [&](float a, float c, unsigned xx, unsigned bb) { return pk_bf(a + lo_f(xx) + lo_f(bb), c + hi_f(xx) + hi_f(bb)); };
    st16c<0>(u32x4{fin(ra.x, ra.y, xv.x, bv.x), fin(ra.z, ra.w, xv.y, bv.y), fin(rb.x, rb.y, xv.z, bv.z), fin(rb.z, rb.w, xv.w, bv.w)}, ro, vrow + (l & 3) * 16, srow + 64 * ks);
    st8m(u32x2{fin(rc.x, rc.y, xv8.x, bv8.x), fin(rc.z, rc.w, xv8.y, bv8.y)}, ro, vrow + (l & 3) * 8, srow + 256, m_eq(g, ks));
  }
  flag(WS_EPOCH, lane0 & m_eq(b, 0));
  asm volatile(".Lfk_end:");
}

extern "C" void kernel_launch(void* const* d_in, const int* in_sizes, int n_in, void* d_out, int out_size, void* d_ws, size_t ws_size, hipStream_t stream) {
  (void)hipFuncSetAttribute((const void*)fused_kernel, hipFuncAttributeMaxDynamicSharedMemorySize, LDS_BYTES);
  fused_kernel<<<256, 512, LDS_BYTES, stream>>>((const char*)d_in[0], (const char*)d_in[1], (const char*)d_in[2], (const char*)d_in[4], (const char*)d_in[5],
                                                (const char*)d_in[6], (const char*)d_in[7], (char*)d_out, (char*)d_ws);
}
```
